```python
import torch

def _softmax(x, axis=-1):
    return torch.softmax(x, dim=axis)
def _gelu(x):
    return torch.nn.functional.gelu(x, approximate="tanh")
def _silu(x):
    return torch.nn.functional.silu(x)
def _sigmoid(x):
    return torch.sigmoid(x)
def _layernorm(x, axis=-1, eps=1e-5):
    m = x.mean(dim=axis, keepdim=True)
    v = x.var(dim=axis, keepdim=True, unbiased=False)
    return (x - m) / torch.sqrt(v + eps)
def _scatter_add(tgt, idx, src, axis=0):
    return tgt.clone().index_add_(axis, idx, src)
def _segment_sum(src, seg, n):
    out = torch.zeros((n,) + src.shape[1:], dtype=src.dtype)
    return out.index_add_(0, seg, src)
def _pad(x, pw):
    flat = tuple(v for pair in reversed(pw) for v in pair)
    return torch.nn.functional.pad(x, flat)

kernel_name = 'sequential_preLN__sliding_local_dilated__geglu_ffn__prompt'

def setup_inputs():

    _w = (lambda a: (lambda t: t.to(torch.bfloat16) if t.is_floating_point() else t)(torch.as_tensor(a)))
    def _A_setup():
        B, S, D = 1, 2048, 512
        return {
            "x": _w((lambda *s: torch.randn(*s, dtype=torch.bfloat16) * 0.05)(B, S, D)),
            "wqkv": _w((lambda *s: torch.randn(*s, dtype=torch.bfloat16) * 0.05)(D, 3 * D)),
            "wo": _w((lambda *s: torch.randn(*s, dtype=torch.bfloat16) * 0.05)(D, D)),
        }
    _A_in = _A_setup()

    def _B_setup():
        B, S, D, F = 1, 2048, 512, 1792
        return {
            "x": _w((lambda *s: torch.randn(*s, dtype=torch.bfloat16) * 0.05)(B, S, D)),
            "w_gate": _w((lambda *s: torch.randn(*s, dtype=torch.bfloat16) * 0.05)(D, F)),
            "w_up": _w((lambda *s: torch.randn(*s, dtype=torch.bfloat16) * 0.05)(D, F)),
            "w_down": _w((lambda *s: torch.randn(*s, dtype=torch.bfloat16) * 0.05)(F, D)),
        }
    _B_in = _B_setup()

    return {
        "A_x": _A_in["x"],
        "A_wqkv": _A_in["wqkv"],
        "A_wo": _A_in["wo"],
        "B_x": _B_in["x"],
        "B_w_gate": _B_in["w_gate"],
        "B_w_up": _B_in["w_up"],
        "B_w_down": _B_in["w_down"],
    }

def reference(A_x, A_wqkv, A_wo, B_x, B_w_gate, B_w_up, B_w_down):
    def _A_ref(x, wqkv, wo):
        B, S, D = x.shape
        H = 32; Dh = D // H
        W, dil = 8, 2
        qkv = (lambda x, *d: x.permute(*d))((x @ wqkv).reshape(B, S, 3, H, Dh), 2, 0, 3, 1, 4)
        q, k, v = qkv[0], qkv[1], qkv[2]
        scale = Dh ** -0.5
        pos = torch.arange(S)
        off = torch.arange(W) * dil
        idx = pos[:, None] - off[None, :]
        mask = idx >= 0
        idx_c = torch.where(mask, idx, 0).reshape(-1)
        kg = k[:, :, idx_c, :].reshape(B, H, S, W, Dh)
        vg = v[:, :, idx_c, :].reshape(B, H, S, W, Dh)
        logits = torch.einsum("bhsd,bhswd->bhsw", q, kg) * scale
        logits = torch.where(mask[None, None, :, :], logits, -1e9)
        attn = _softmax(logits, axis=-1)
        o = torch.einsum("bhsw,bhswd->bhsd", attn, vg)
        return (lambda x, *d: x.permute(*d))(o, 0, 2, 1, 3).reshape(B, S, D) @ wo

    def _B_ref(x, w_gate, w_up, w_down):
        return (_gelu(x @ w_gate) * (x @ w_up)) @ w_down

    x = A_x
    h = x + _A_ref(_layernorm(x, axis=-1), A_wqkv, A_wo)
    h2 = h + _B_ref(_layernorm(h, axis=-1), B_w_gate, B_w_up, B_w_down)
    return h2
```

```cpp
#include <hip/hip_runtime.h>
#include <hip/hip_bf16.h>
#include <stdint.h>

typedef unsigned short u16;
using bf16x8 = __attribute__((ext_vector_type(8))) short;
using f32x4 = __attribute__((ext_vector_type(4))) float;

__device__ __forceinline__ float bf2f(u16 v) { return __uint_as_float(((unsigned)v) << 16); }
__device__ __forceinline__ u16 f2bf(float f) {
  unsigned u = __float_as_uint(f);
  u += 0x7FFFu + ((u >> 16) & 1u);
  return (u16)(u >> 16);
}

constexpr int S = 2048, D = 512, F = 1792, H = 32, DH = 16;

__global__ void ln_kernel(const u16* __restrict__ x, u16* __restrict__ y) {
  int row = blockIdx.x;
  int t = threadIdx.x;
  float v[8];
  float s = 0.f;
  for (int j = 0; j < 8; ++j) { v[j] = bf2f(x[row * D + t * 8 + j]); s += v[j]; }
  for (int o = 32; o > 0; o >>= 1) s += __shfl_xor(s, o);
  float m = s / D;
  float q = 0.f;
  for (int j = 0; j < 8; ++j) { float d = v[j] - m; q += d * d; }
  for (int o = 32; o > 0; o >>= 1) q += __shfl_xor(q, o);
  float r = rsqrtf(q / D + 1e-5f);
  for (int j = 0; j < 8; ++j) y[row * D + t * 8 + j] = f2bf((v[j] - m) * r);
}

template <int MODE>
__global__ __launch_bounds__(256) void gemm_kernel(const u16* __restrict__ A, const u16* __restrict__ B,
                                                   const u16* __restrict__ B2, const u16* __restrict__ R,
                                                   u16* __restrict__ C, int M, int N, int K) {
  __shared__ u16 sA[64][40];
  __shared__ u16 sB[64][40];
  __shared__ u16 sB2[64][40];
  int bm = blockIdx.y * 64, bn = blockIdx.x * 64;
  int tid = threadIdx.x, lane = tid & 63, w = tid >> 6, wr = w >> 1, wc = w & 1;
  f32x4 acc[2][2] = {}, acc2[2][2] = {};
  for (int k0 = 0; k0 < K; k0 += 32) {
    for (int e = tid; e < 64 * 32; e += 256) {
      int r = e >> 5, c = e & 31;
      sA[r][c] = A[(size_t)(bm + r) * K + k0 + c];
    }
    for (int e = tid; e < 64 * 32; e += 256) {
      int k = e >> 6, n = e & 63;
      sB[n][k] = B[(size_t)(k0 + k) * N + bn + n];
      if (MODE == 2) sB2[n][k] = B2[(size_t)(k0 + k) * N + bn + n];
    }
    __syncthreads();
    bf16x8 a[2], b[2], b2[2];
    for (int i = 0; i < 2; ++i) {
      a[i] = *(const bf16x8*)&sA[wr * 32 + i * 16 + (lane & 15)][(lane >> 4) * 8];
      b[i] = *(const bf16x8*)&sB[wc * 32 + i * 16 + (lane & 15)][(lane >> 4) * 8];
      if (MODE == 2) b2[i] = *(const bf16x8*)&sB2[wc * 32 + i * 16 + (lane & 15)][(lane >> 4) * 8];
    }
    for (int i = 0; i < 2; ++i)
      for (int j = 0; j < 2; ++j) {
        acc[i][j] = __builtin_amdgcn_mfma_f32_16x16x32_bf16(a[i], b[j], acc[i][j], 0, 0, 0);
        if (MODE == 2) acc2[i][j] = __builtin_amdgcn_mfma_f32_16x16x32_bf16(a[i], b2[j], acc2[i][j], 0, 0, 0);
      }
    __syncthreads();
  }
  for (int i = 0; i < 2; ++i)
    for (int j = 0; j < 2; ++j)
      for (int r = 0; r < 4; ++r) {
        int row = bm + wr * 32 + i * 16 + (lane >> 4) * 4 + r;
        int col = bn + wc * 32 + j * 16 + (lane & 15);
        float v = acc[i][j][r];
        if (MODE == 1) v = bf2f(R[(size_t)row * N + col]) + bf2f(f2bf(v));
        if (MODE == 2) {
          float g = v;
          float u = 0.7978845608028654f * (g + 0.044715f * g * g * g);
          float ge = 0.5f * g * (1.f + tanhf(u));
          v = ge * acc2[i][j][r];
        }
        C[(size_t)row * N + col] = f2bf(v);
      }
}

__global__ void attn_kernel(const u16* __restrict__ qkv, u16* __restrict__ o) {
  int idx = blockIdx.x * blockDim.x + threadIdx.x;
  int t = idx / H, h = idx % H;
  float q[DH];
  for (int d = 0; d < DH; ++d) q[d] = bf2f(qkv[(size_t)t * 3 * D + h * DH + d]);
  float lg[8];
  float mx = -1e30f;
  for (int w = 0; w < 8; ++w) {
    int p = t - 2 * w;
    float s = -1e9f;
    if (p >= 0) {
      s = 0.f;
      for (int d = 0; d < DH; ++d) s += q[d] * bf2f(qkv[(size_t)p * 3 * D + D + h * DH + d]);
      s *= 0.25f;
    }
    lg[w] = s;
    mx = fmaxf(mx, s);
  }
  float sum = 0.f;
  for (int w = 0; w < 8; ++w) { lg[w] = __expf(lg[w] - mx); sum += lg[w]; }
  float inv = 1.f / sum;
  float acc[DH];
  for (int d = 0; d < DH; ++d) acc[d] = 0.f;
  for (int w = 0; w < 8; ++w) {
    int p = t - 2 * w;
    if (p < 0) p = 0;
    float pr = lg[w] * inv;
    for (int d = 0; d < DH; ++d) acc[d] += pr * bf2f(qkv[(size_t)p * 3 * D + 2 * D + h * DH + d]);
  }
  for (int d = 0; d < DH; ++d) o[(size_t)t * D + h * DH + d] = f2bf(acc[d]);
}

extern "C" void kernel_launch(void* const* d_in, const int* in_sizes, int n_in, void* d_out, int out_size,
                              void* d_ws, size_t ws_size, hipStream_t stream) {
  const u16* x = (const u16*)d_in[0];
  const u16* wqkv = (const u16*)d_in[1];
  const u16* wo = (const u16*)d_in[2];
  const u16* wg = (const u16*)d_in[4];
  const u16* wu = (const u16*)d_in[5];
  const u16* wd = (const u16*)d_in[6];
  u16* out = (u16*)d_out;
  u16* ws = (u16*)d_ws;
  u16* xn = ws;
  u16* qkv = xn + S * D;
  u16* o = qkv + S * 3 * D;
  u16* h = o + S * D;
  u16* hn = h + S * D;
  u16* g = hn + S * D;
  ln_kernel<<<S, 64, 0, stream>>>(x, xn);
  gemm_kernel<0><<<dim3(3 * D / 64, S / 64), 256, 0, stream>>>(xn, wqkv, nullptr, nullptr, qkv, S, 3 * D, D);
  attn_kernel<<<S * H / 256, 256, 0, stream>>>(qkv, o);
  gemm_kernel<1><<<dim3(D / 64, S / 64), 256, 0, stream>>>(o, wo, nullptr, x, h, S, D, D);
  ln_kernel<<<S, 64, 0, stream>>>(h, hn);
  gemm_kernel<2><<<dim3(F / 64, S / 64), 256, 0, stream>>>(hn, wg, wu, nullptr, g, S, F, D);
  gemm_kernel<1><<<dim3(D / 64, S / 64), 256, 0, stream>>>(g, wd, nullptr, h, out, S, D, F);
}
```

```cpp
#include <hip/hip_runtime.h>
#include <stdint.h>
#include <utility>

#define MK_STAMP 0

typedef unsigned short u16;
typedef unsigned v4u __attribute__((ext_vector_type(4)));
typedef unsigned v2u __attribute__((ext_vector_type(2)));
typedef short v8s __attribute__((ext_vector_type(8)));
typedef short v4s __attribute__((ext_vector_type(4)));
typedef float f32x2 __attribute__((ext_vector_type(2)));
typedef float f32x4 __attribute__((ext_vector_type(4)));
typedef float f32x16 __attribute__((ext_vector_type(16)));
typedef __bf16 bf16x2 __attribute__((ext_vector_type(2)));
#define GAS __attribute__((address_space(1)))
#define LAS __attribute__((address_space(3)))
#define DEV __device__ __forceinline__

constexpr unsigned WS_FLAGS = 0;
constexpr unsigned WS_O = 131072;
constexpr unsigned WS_H = WS_O + 2097152;
constexpr unsigned WS_HS = WS_H + 2097152;
constexpr unsigned WS_G = WS_HS + 262144;
constexpr unsigned WS_CS = WS_G + 64 * 112 * 1024;
constexpr unsigned WS_STAMP = WS_CS + 16 * 16 * 224 * 4;
constexpr unsigned WS_BYTES = WS_STAMP + 256 * 4 * 48 * 8;
constexpr int FL_EPOCH = 0, FL_F = 1024;

constexpr int LDS_BYTES = 163840;
constexpr int AUX_SC1 = 16;

struct Params {
  const u16* x; const u16* wqkv; const u16* wo; const u16* wg; const u16* wu; const u16* wd;
  u16* out; unsigned char* ws;
};

template <class F, int... I> DEV void static_for_impl(F&& f, std::integer_sequence<int, I...>) { (f(std::integral_constant<int, I>{}), ...); }
template <int N, class F> DEV void static_for(F&& f) { static_for_impl(f, std::make_integer_sequence<int, N>{}); }

DEV float bf_lo(unsigned u) { return __uint_as_float(u << 16); }
DEV float bf_hi(unsigned u) { return __uint_as_float(u & 0xffff0000u); }
DEV unsigned pk_bf16(float a, float b) {
  bf16x2 p; p[0] = (__bf16)a; p[1] = (__bf16)b;
  return __builtin_bit_cast(unsigned, p);
}
DEV f32x2 pk2(float a, float b) { return f32x2{a, b}; }
DEV unsigned cvt2(f32x2 v) { return pk_bf16(v.x, v.y); }
DEV float dot2(unsigned a, unsigned b, float c) {
  return __builtin_amdgcn_fdot2_f32_bf16(__builtin_bit_cast(bf16x2, a), __builtin_bit_cast(bf16x2, b), c, false);
}
DEV f32x4 mfma16(v4u a, v4u b, f32x4 c) {
  return __builtin_amdgcn_mfma_f32_16x16x32_bf16(__builtin_bit_cast(v8s, a), __builtin_bit_cast(v8s, b), c, 0, 0, 0);
}
DEV f32x4 mfma16k16(v2u a, v2u b, f32x4 c) {
  return __builtin_amdgcn_mfma_f32_16x16x16bf16_1k(__builtin_bit_cast(v4s, a), __builtin_bit_cast(v4s, b), c, 0, 0, 0);
}
DEV f32x16 mfma32(v4u a, v4u b, f32x16 c) {
  return __builtin_amdgcn_mfma_f32_32x32x16_bf16(__builtin_bit_cast(v8s, a), __builtin_bit_cast(v8s, b), c, 0, 0, 0);
}
DEV v2u tr_read(LAS unsigned char* p) {
  return __builtin_bit_cast(v2u, __builtin_amdgcn_ds_read_tr16_b64_v4i16((LAS v4s*)p));
}
DEV v4u tr_read2(LAS unsigned char* p0, LAS unsigned char* p1) {
  v2u a = tr_read(p0), b = tr_read(p1);
  return v4u{a.x, a.y, b.x, b.y};
}
DEV void dma16(const void* sbase, unsigned voff, unsigned lds_dst) {
  lds_dst = __builtin_amdgcn_readfirstlane(lds_dst);
  asm volatile("s_mov_b32 m0, %2\n\ts_nop 0\n\tglobal_load_lds_dwordx4 %0, %1"
               :: "v"(voff), "s"(sbase), "s"(lds_dst) : "memory");
}
DEV void set_m0(unsigned v) {
  v = __builtin_amdgcn_readfirstlane(v);
  asm volatile("s_mov_b32 m0, %0\n\ts_nop 0" :: "s"(v) : "memory");
}
template <int IMM> DEV void dma16i(const void* sbase, unsigned voff) {
  asm volatile("global_load_lds_dwordx4 %0, %1 offset:%c2" :: "v"(voff), "s"(sbase), "i"(IMM) : "memory");
}
template <int IMM> DEV void bload_a(v4u& dst, v4u rsrc, unsigned voff, unsigned soff) {
  asm volatile("buffer_load_dwordx4 %0, %1, %2, %3 offen offset:%c4 sc1" : "=a"(dst) : "v"(voff), "s"(rsrc), "s"(soff), "i"(IMM) : "memory");
}
template <int IMM> DEV void bload_v(v4u& dst, v4u rsrc, unsigned voff, unsigned soff) {
  asm volatile("buffer_load_dwordx4 %0, %1, %2, %3 offen offset:%c4 sc1" : "=v"(dst) : "v"(voff), "s"(rsrc), "s"(soff), "i"(IMM) : "memory");
}
DEV void bload1_v(unsigned& dst, v4u rsrc, unsigned voff, unsigned soff) {
  asm volatile("buffer_load_dword %0, %1, %2, %3 offen sc1" : "=v"(dst) : "v"(voff), "s"(rsrc), "s"(soff) : "memory");
}
template <int N> DEV void wait_vm_a(v4u& x) { asm volatile("s_waitcnt vmcnt(%c1)" : "+a"(x) : "i"(N) : "memory"); }
template <int N> DEV void wait_vm_a2(v4u& x, v4u& y) { asm volatile("s_waitcnt vmcnt(%c2)" : "+a"(x), "+a"(y) : "i"(N) : "memory"); }
template <int N> DEV void wait_vm_v2(v4u& x, v4u& y) { asm volatile("s_waitcnt vmcnt(%c2)" : "+v"(x), "+v"(y) : "i"(N) : "memory"); }
template <int N> DEV void wait_vm_v1(unsigned& x) { asm volatile("s_waitcnt vmcnt(%c1)" : "+v"(x) : "i"(N) : "memory"); }
#define PIN_V(x) asm volatile("" : "+v"(x))
#define PIN_A(x) asm volatile("" : "+a"(x))
#define WAIT_VM(n) asm volatile("s_waitcnt vmcnt(" #n ")" ::: "memory")
#define WAIT_VM_BAR(n) asm volatile("s_waitcnt vmcnt(" #n ") lgkmcnt(0)\n\ts_barrier" ::: "memory")
#define BAR_LDS() asm volatile("s_waitcnt lgkmcnt(0)\n\ts_barrier" ::: "memory")
#define WAIT_LGKM() asm volatile("s_waitcnt lgkmcnt(0)" ::: "memory")
#define SCHED() __builtin_amdgcn_sched_barrier(0)

DEV float xsum16_32(float v) {
  auto r = __builtin_amdgcn_permlane16_swap(__float_as_uint(v), __float_as_uint(v), false, false);
  v = __uint_as_float(r[0]) + __uint_as_float(r[1]);
  auto s = __builtin_amdgcn_permlane32_swap(__float_as_uint(v), __float_as_uint(v), false, false);
  return __uint_as_float(s[0]) + __uint_as_float(s[1]);
}
DEV float xmax16_32(float v) {
  auto r = __builtin_amdgcn_permlane16_swap(__float_as_uint(v), __float_as_uint(v), false, false);
  v = fmaxf(__uint_as_float(r[0]), __uint_as_float(r[1]));
  auto s = __builtin_amdgcn_permlane32_swap(__float_as_uint(v), __float_as_uint(v), false, false);
  return fmaxf(__uint_as_float(s[0]), __uint_as_float(s[1]));
}
DEV float xsum32(float v) {
  auto s = __builtin_amdgcn_permlane32_swap(__float_as_uint(v), __float_as_uint(v), false, false);
  return __uint_as_float(s[0]) + __uint_as_float(s[1]);
}

#define STAMP(i) do {} while (0)
#define TSARG
#define TSPASS

DEV GAS unsigned* flag_ptr(GAS unsigned* ctl, int ph, int dom, int j) { return ctl + FL_F + ((ph * 16 + dom) * 16 + j) * 32; }
DEV void wait_flags(GAS unsigned* word, unsigned tag) {
  for (unsigned spins = 0; spins < (1u << 20); ++spins) {
    const unsigned v = __hip_atomic_load(word, __ATOMIC_RELAXED, __HIP_MEMORY_SCOPE_AGENT);
    if (__all(v == tag)) break;
    __builtin_amdgcn_s_sleep(1);
  }
  __builtin_amdgcn_fence(__ATOMIC_ACQUIRE, "wavefront");
  asm volatile("" ::: "memory");
}

constexpr int P1_X = 0;
constexpr int P1_W = 81920;
constexpr int P1_WSLOT = 24576;
constexpr int P1_MISC = 155648;
constexpr int P2_W = 0;
constexpr int P2_X = 151936;
constexpr int CS_BUF = 160128;
constexpr int P3_UP = 14464;
constexpr int P3_CSP = 151936;
DEV constexpr int p3_slot_addr(int ds) { return (ds % 5) == 0 ? 65536 : (ds % 5) == 1 ? 94336 : (ds % 5) == 2 ? 123136 : (ds % 5) == 3 ? 0 : 28800; }
constexpr int P4_EARLY = 16384;
constexpr int P4_WAVE = 40960;

DEV unsigned wrow_voff(int lane) { return (unsigned)(lane >> 3) * 1024u + (unsigned)((lane & 7) ^ (((lane >> 4) & 1) << 2)) * 16u; }

DEV void p1_xvoff(int tok0, int w, int lane, unsigned (&vx)[5]) {
#pragma unroll
  for (int i = 0; i < 5; ++i) {
    const int rg = 5 * (w & 1) + i;
    const int row = 8 * rg + (lane >> 3);
    int tok = tok0 + row; tok = tok < 0 ? 0 : tok;
    const int ch = (lane & 7) ^ ((row >> 1) & 7);
    vx[i] = (unsigned)tok * 1024u + (unsigned)ch * 16u + 4096u - (unsigned)((i - 2) * 1024);
  }
}
DEV void p1_x_setup(int sp0, int w) { set_m0((unsigned)(P1_X + (sp0 + (w >> 1)) * 10240 + (5 * (w & 1) + 2) * 1024)); }
template <int I> DEV void p1_issue_x1(const Params& p, int sp0, int w, int lane, const unsigned (&vx)[5]) {
  const unsigned char* base = (const unsigned char*)p.x + (sp0 + (w >> 1)) * 128 - 4096;
  if constexpr (I == 0) { if ((w & 1) || lane >= 8) dma16i<(I - 2) * 1024>(base, vx[I]); }
  else dma16i<(I - 2) * 1024>(base, vx[I]);
}
DEV void p1_wvoff(int w, int lane, unsigned (&vo)[6]) {
#pragma unroll
  for (int i = 0; i < 6; ++i) {
    const int idx = 6 * w + i;
    const int part = idx >> 3, rg = idx & 7;
    const int row = 8 * rg + (lane >> 3);
    const int f = ((row >> 1) & 1) | (((row >> 3) & 1) << 1);
    const int ch = (lane & 7) ^ (f << 1);
    vo[i] = (unsigned)row * 3072u + (unsigned)part * 1024u + (unsigned)ch * 16u + 4096u - (unsigned)((i - 3) * 1024);
  }
}
DEV void p1_w_setup(int slot, int w) { set_m0((unsigned)(P1_W + slot * P1_WSLOT + (6 * w + 3) * 1024)); }
template <int I> DEV void p1_issue_w1(const Params& p, int hg, int st, const unsigned (&vo)[6]) {
  const unsigned char* base = (const unsigned char*)p.wqkv + st * 64 * 3072 + hg * 128 - 4096;
  dma16i<(I - 3) * 1024>(base, vo[I]);
}
DEV void wrow_voffs(int lane, unsigned (&v)[4]) {
#pragma unroll
  for (int i = 0; i < 4; ++i) v[i] = wrow_voff(lane) + 7168u * i;
}
template <int I> DEV void wrow_issue(const u16* rowbase  , const unsigned (&v)[4]) { dma16i<I * 1024>(rowbase, v[I]); }

struct P1Frag { v4u xf[5]; v4u wq, wk, wv; };

DEV void phase1(const Params& p, LAS unsigned char* L, __amdgpu_buffer_rsrc_t rs, int p64, int hg, int w, int lane,
                const unsigned (&wvo)[6], const unsigned (&vx)[5] TSARG) {
  const int s0 = (p64 & 3) * 2;
  const int g = lane >> 4, tk = lane & 15, q4 = (lane & 15) >> 2, pp = lane & 3;
  const int fsw = ((q4 >> 1) & 1) | ((g & 1) << 1);
  const int wbase = P1_W + (8 * g + q4) * 128 + (((2 * w + (pp >> 1)) ^ (fsw << 1)) * 16) + 8 * (pp & 1);
  const int xsw = (tk >> 1) & 7;
  const int xb0 = P1_X + tk * 128 + ((g ^ xsw) * 16), xb1 = P1_X + tk * 128 + (((4 + g) ^ xsw) * 16);
  const int xbw = (w & 1) ? xb1 : xb0;

  f32x4 aq[5], ak[5], av[5], ssum[5];
  float s2[5];
#pragma unroll
  for (int t = 0; t < 5; ++t) {
    aq[t] = f32x4{0, 0, 0, 0}; ak[t] = f32x4{0, 0, 0, 0}; av[t] = f32x4{0, 0, 0, 0}; ssum[t] = f32x4{0, 0, 0, 0}; s2[t] = 0.f;
  }
  const v4u ones = {0x3f803f80u, 0x3f803f80u, 0x3f803f80u, 0x3f803f80u};

  WAIT_VM_BAR(6);
  STAMP(11);
  P1Frag cur;
  {
    LAS unsigned char* xs = L + s0 * 10240 + xb0;
    LAS unsigned char* wsl = L + wbase;
    cur.wk = tr_read2(wsl + 8192, wsl + 8192 + 512);
    cur.wv = tr_read2(wsl + 16384, wsl + 16384 + 512);
    cur.wq = tr_read2(wsl, wsl + 512);
#pragma unroll
    for (int t = 0; t < 5; ++t) cur.xf[t] = *(LAS v4u*)(xs + t * 2048);
  }
  SCHED();
  static_for<16>([&](auto KS) {
    constexpr int ks = KS;
    if constexpr (ks == 1) { WAIT_VM_BAR(5); }
    else if constexpr (ks == 3 || ks == 7 || ks == 11) { WAIT_VM_BAR(6); }
    else if constexpr (ks == 5 || ks == 9) { WAIT_VM_BAR(11); }
    else if constexpr (ks == 13) { WAIT_VM_BAR(0); }
    constexpr bool isx = (ks == 0 || ks == 4 || ks == 8);
    constexpr int wst = ks == 1 ? 2 : ks == 2 ? 3 : ks == 3 ? 4 : ks == 5 ? 5 : ks == 7 ? 6 : ks == 9 ? 7 : -1;
    if constexpr (isx) p1_x_setup((s0 + 2 + ks / 2) & 7, w);
    if constexpr (wst >= 0) p1_w_setup(wst % 3, w);
    auto dma = [&](auto D) {
      constexpr int d = D;
      if constexpr (isx && d < 5) p1_issue_x1<d>(p, (s0 + 2 + ks / 2) & 7, w, lane, vx);
      if constexpr (wst >= 0) p1_issue_w1<d>(p, hg, (s0 + wst) & 7, wvo);
    };
    P1Frag nxt;
    constexpr int kn = ks + 1, itn = kn >> 1, k2n = kn & 1;
    LAS unsigned char* xs = L + ((s0 + itn) & 7) * 10240 + (k2n ? xb1 : xb0);
    LAS unsigned char* wsl = L + wbase + (itn % 3) * P1_WSLOT + k2n * 4096;
    constexpr int G = ks >> 2, sub = ks & 3;
    LAS unsigned char* sx = L + ((s0 + 2 * G + (w >> 1)) & 7) * 10240 + xbw;
    v4u sva, svb;
    dma(std::integral_constant<int, 0>{});
    static_for<5>([&](auto T) {
      constexpr int t = T;
      if constexpr (ks < 15) {
        if constexpr (t == 0) nxt.wk = tr_read2(wsl + 8192, wsl + 8192 + 512);
        if constexpr (t == 1) nxt.wv = tr_read2(wsl + 16384, wsl + 16384 + 512);
        if constexpr (t == 2) nxt.wq = tr_read2(wsl, wsl + 512);
        nxt.xf[t] = *(LAS v4u*)(xs + t * 2048);
      }
      if constexpr (t == 0) sva = *(LAS v4u*)(sx + sub * 2048);
      if constexpr (t == 1 && sub == 3) svb = *(LAS v4u*)(sx + 4 * 2048);
      ak[t] = mfma16(cur.wk, cur.xf[t], ak[t]);
      av[t] = mfma16(cur.xf[t], cur.wv, av[t]);
      aq[t] = mfma16(cur.wq, cur.xf[t], aq[t]);
      if constexpr (t == 4) {
        ssum[sub] = mfma16(ones, sva, ssum[sub]);
#pragma unroll
        for (int e = 0; e < 4; ++e) s2[sub] = dot2(sva[e], sva[e], s2[sub]);
        if constexpr (sub == 3) {
          ssum[4] = mfma16(ones, svb, ssum[4]);
#pragma unroll
          for (int e = 0; e < 4; ++e) s2[4] = dot2(svb[e], svb[e], s2[4]);
        }
      }
      SCHED();
      dma(std::integral_constant<int, t + 1>{});
      SCHED();
    });
    if constexpr (ks < 15) cur = nxt;
  });
  STAMP(12);
  LAS f32x2* part = (LAS f32x2*)(L + P1_MISC);
  LAS float* fmean = (LAS float*)(L + P1_MISC + 2560 + w * 640);
  LAS float* frstd = fmean + 80;
#pragma unroll
  for (int t = 0; t < 5; ++t) {
    const float b = xsum16_32(s2[t]);
    if (g == 0) part[w * 80 + t * 16 + tk] = f32x2{ssum[t][0], b};
  }
  BAR_LDS();
  unsigned wv4[4];
  wrow_voffs(lane, wv4);
  const u16* wob = p.wo + (128 * w) * 512 + hg * 64;
  auto dmo = [&](auto G) {
    constexpr int gq = G;
    set_m0((unsigned)(P2_W + (128 * w + 32 * gq) * 128));
    static_for<4>([&](auto I) { wrow_issue<I>(wob + gq * 32 * 512, wv4); });
  };
  dmo(std::integral_constant<int, 0>{});
#pragma unroll
  for (int r = 0; r < 2; ++r) {
    const int tok = r * 64 + lane;
    if (tok < 80) {
      const f32x2 a = (part[tok] + part[80 + tok]) + (part[160 + tok] + part[240 + tok]);
      const float mean = a.x * (1.f / 512.f);
      const float var = fmaxf(a.y * (1.f / 512.f) - mean * mean, 0.f);
      fmean[tok] = mean; frstd[tok] = __builtin_amdgcn_rsqf(var + 1e-5f);
    }
  }
  f32x2 csq[2], csk[2];
#pragma unroll
  for (int r = 0; r < 4; ++r) {
    csq[r >> 1][r & 1] = __uint_as_float(__builtin_amdgcn_update_dpp(0, __float_as_uint(aq[0][r]), 0x150, 0xf, 0xf, false));
    csk[r >> 1][r & 1] = __uint_as_float(__builtin_amdgcn_update_dpp(0, __float_as_uint(ak[0][r]), 0x150, 0xf, 0xf, false));
  }
  float csv;
  {
    auto a = __builtin_amdgcn_permlane16_swap(__float_as_uint(av[0][0]), __float_as_uint(av[0][0]), false, false);
    auto c = __builtin_amdgcn_permlane32_swap(a[0], a[0], false, false);
    csv = __uint_as_float(c[0]);
  }
  dmo(std::integral_constant<int, 1>{});
  WAIT_LGKM();
  float m1[5], r1[5];
  f32x4 mv[5], rv[5];
#pragma unroll
  for (int t = 0; t < 5; ++t) {
    m1[t] = fmean[t * 16 + tk]; r1[t] = frstd[t * 16 + tk];
    mv[t] = *(LAS f32x4*)(fmean + t * 16 + 4 * g); rv[t] = *(LAS f32x4*)(frstd + t * 16 + 4 * g);
  }
  SCHED();
  v2u qp[4], kp[5], vp[5];
  const float sc = 0.25f * 1.44269504088896f;
  const f32x2 c2 = {csv, csv};
#pragma unroll
  for (int t = 0; t < 5; ++t) {
    const f32x2 m2 = {m1[t], m1[t]}, r2 = {r1[t], r1[t]};
    kp[t].x = cvt2((pk2(ak[t][0], ak[t][1]) - m2 * csk[0]) * r2);
    kp[t].y = cvt2((pk2(ak[t][2], ak[t][3]) - m2 * csk[1]) * r2);
    if (t > 0) {
      qp[t - 1].x = cvt2(pk2(aq[t][0], aq[t][1]) - m2 * csq[0]);
      qp[t - 1].y = cvt2(pk2(aq[t][2], aq[t][3]) - m2 * csq[1]);
    }
  }
  SCHED();
  const f32x4 z = {0, 0, 0, 0};
  f32x4 sp[4], scu[4];
#pragma unroll
  for (int t = 0; t < 4; ++t) { sp[t] = mfma16k16(kp[t], qp[t], z); scu[t] = mfma16k16(kp[t + 1], qp[t], z); }
  SCHED();
  dmo(std::integral_constant<int, 2>{});
#pragma unroll
  for (int t = 0; t < 5; ++t) {
    vp[t].x = cvt2((pk2(av[t][0], av[t][1]) - pk2(mv[t][0], mv[t][1]) * c2) * pk2(rv[t][0], rv[t][1]));
    vp[t].y = cvt2((pk2(av[t][2], av[t][3]) - pk2(mv[t][2], mv[t][3]) * c2) * pk2(rv[t][2], rv[t][3]));
  }
  SCHED();
  f32x2 biasp[2], biasc[2];
#pragma unroll
  for (int r = 0; r < 4; ++r) {
    const int dc = tk - 4 * g - r, dp = dc + 16;
    biasc[r >> 1][r & 1] = (dc >= 0 && dc <= 14 && !(dc & 1)) ? 0.f : -1e30f;
    biasp[r >> 1][r & 1] = (dp <= 14 && !(dp & 1)) ? 0.f : -1e30f;
  }
  f32x2 a[4][4];
  float mx[4], sm[4];
#pragma unroll
  for (int t = 0; t < 4; ++t) {
    const float sq = sc * r1[t + 1];
    const f32x2 s2v = {sq, sq};
    f32x2 bp0 = biasp[0], bp1 = biasp[1];
    if (t == 0 && p64 == 0) { bp0 = f32x2{-1e30f, -1e30f}; bp1 = bp0; }
    a[t][0] = pk2(sp[t][0], sp[t][1]) * s2v + bp0; a[t][1] = pk2(sp[t][2], sp[t][3]) * s2v + bp1;
    a[t][2] = pk2(scu[t][0], scu[t][1]) * s2v + biasc[0]; a[t][3] = pk2(scu[t][2], scu[t][3]) * s2v + biasc[1];
  }
#pragma unroll
  for (int t = 0; t < 4; ++t)
    mx[t] = fmaxf(fmaxf(__builtin_fmaxf(a[t][0].x, a[t][0].y), fmaxf(a[t][1].x, a[t][1].y)), fmaxf(fmaxf(a[t][2].x, a[t][2].y), fmaxf(a[t][3].x, a[t][3].y)));
  SCHED();
  auto xred2 = [&](float& A, float& B, auto op) {
    auto s1 = __builtin_amdgcn_permlane16_swap(__float_as_uint(A), __float_as_uint(B), false, false);
    const float u = op(__uint_as_float(s1[0]), __uint_as_float(s1[1]));
    auto s2_ = __builtin_amdgcn_permlane32_swap(__float_as_uint(u), __float_as_uint(u), false, false);
    const float v = op(__uint_as_float(s2_[0]), __uint_as_float(s2_[1]));
    auto s3 = __builtin_amdgcn_permlane16_swap(__float_as_uint(v), __float_as_uint(v), false, false);
    A = __uint_as_float(s3[0]); B = __uint_as_float(s3[1]);
  };
  auto fmx = [](float x, float y) { return fmaxf(x, y); };
  auto fad = [](float x, float y) { return x + y; };
  xred2(mx[0], mx[1], fmx); xred2(mx[2], mx[3], fmx);
  SCHED();
  dmo(std::integral_constant<int, 3>{});
#pragma unroll
  for (int t = 0; t < 4; ++t) {
    const f32x2 m2 = {mx[t], mx[t]};
#pragma unroll
    for (int q = 0; q < 4; ++q) a[t][q] -= m2;
  }
#pragma unroll
  for (int t = 0; t < 4; ++t)
#pragma unroll
    for (int q = 0; q < 4; ++q) { a[t][q].x = __builtin_amdgcn_exp2f(a[t][q].x); a[t][q].y = __builtin_amdgcn_exp2f(a[t][q].y); }
  SCHED();
  v4u pf[4];
#pragma unroll
  for (int t = 0; t < 4; ++t) {
    const f32x2 sv = (a[t][0] + a[t][1]) + (a[t][2] + a[t][3]);
    sm[t] = sv.x + sv.y;
    pf[t] = v4u{cvt2(a[t][0]), cvt2(a[t][1]), cvt2(a[t][2]), cvt2(a[t][3])};
  }
  f32x4 o[4];
#pragma unroll
  for (int t = 0; t < 4; ++t) {
    const v4u vf = {vp[t].x, vp[t].y, vp[t + 1].x, vp[t + 1].y};
    o[t] = mfma16(vf, pf[t], z);
  }
  SCHED();
  xred2(sm[0], sm[1], fad); xred2(sm[2], sm[3], fad);
  v2u ob[4];
#pragma unroll
  for (int t = 0; t < 4; ++t) {
    const float inv = __builtin_amdgcn_rcpf(sm[t]);
    const f32x2 i2 = {inv, inv};
    ob[t] = v2u{cvt2(pk2(o[t][0], o[t][1]) * i2), cvt2(pk2(o[t][2], o[t][3]) * i2)};
  }
  const int head = 4 * hg + w;
#pragma unroll
  for (int u = 0; u < 2; ++u) {
    auto sx = __builtin_amdgcn_permlane16_swap(ob[2 * u].x, ob[2 * u + 1].x, false, false);
    auto sy = __builtin_amdgcn_permlane16_swap(ob[2 * u].y, ob[2 * u + 1].y, false, false);
    const v4u o4 = {sx[0], sy[0], sx[1], sy[1]};
    const int voff = ((g >> 1) * 32 + (g & 1) * 16 + tk) * 16;
    __builtin_amdgcn_raw_buffer_store_b128(o4, rs, voff, (int)WS_O + ((2 * p64 + u) * 32 + head) * 1024, AUX_SC1);
  }
}

DEV void cs_load(const Params& p, int c, int r, int tid, v4u (&v)[4]) {
  const int rg = tid >> 5, c0 = tid & 31, cc = c0 < 28 ? c0 : 27;
  const u16* base = (cc < 14 ? p.wg : p.wu) + (size_t)(32 * r + 4 * rg) * 1792 + 112 * c + (cc < 14 ? cc : cc - 14) * 8;
#pragma unroll
  for (int i = 0; i < 4; ++i) v[i] = *(const GAS v4u*)(base + i * 1792);
}
DEV void cs_finish(LAS unsigned char* L, __amdgpu_buffer_rsrc_t rs, int c, int r, int w, int lane, const v4u (&v)[4]) {
  LAS float* buf = (LAS float*)(L + CS_BUF);
  const int c0 = lane & 31;
  f32x4 a, b;
#pragma unroll
  for (int e = 0; e < 2; ++e) {
    a[2 * e] = xsum32((bf_lo(v[0][e]) + bf_lo(v[1][e])) + (bf_lo(v[2][e]) + bf_lo(v[3][e])));
    a[2 * e + 1] = xsum32((bf_hi(v[0][e]) + bf_hi(v[1][e])) + (bf_hi(v[2][e]) + bf_hi(v[3][e])));
    b[2 * e] = xsum32((bf_lo(v[0][2 + e]) + bf_lo(v[1][2 + e])) + (bf_lo(v[2][2 + e]) + bf_lo(v[3][2 + e])));
    b[2 * e + 1] = xsum32((bf_hi(v[0][2 + e]) + bf_hi(v[1][2 + e])) + (bf_hi(v[2][2 + e]) + bf_hi(v[3][2 + e])));
  }
  if (lane < 28) {
    *(LAS f32x4*)(buf + w * 224 + c0 * 8) = a;
    *(LAS f32x4*)(buf + w * 224 + c0 * 8 + 4) = b;
  }
  BAR_LDS();
  const int tid = w * 64 + lane;
  if (tid < 224) {
    const float s = (buf[tid] + buf[224 + tid]) + (buf[448 + tid] + buf[672 + tid]);
    __builtin_amdgcn_raw_buffer_store_b32(__float_as_uint(s), rs, tid * 4, (int)WS_CS + (c * 16 + r) * 896, AUX_SC1);
  }
}

DEV void p3_voffs(int lane, unsigned (&vo)[7]) {
#pragma unroll
  for (int i = 0; i < 7; ++i) {
    const int q = i * 64 + lane, row = q / 14, cc = q - 14 * row;
    vo[i] = (unsigned)row * 3584u + (unsigned)cc * 16u + 4096u - (unsigned)((i - 3) * 1024);
  }
}
DEV void p3_w_setup(int ds, int w) { set_m0((unsigned)(p3_slot_addr(ds) + (w & 1) * P3_UP + (w >> 1) * 7168 + 3 * 1024)); }
template <int I> DEV void p3_issue_w1(const Params& p, int c, int ds, int w, const unsigned (&vo)[7]) {
  const unsigned char* base = (const unsigned char*)((w & 1) ? p.wu : p.wg) + (64 * ds + 32 * (w >> 1)) * 3584 + 224 * c - 4096;
  dma16i<(I - 3) * 1024>(base, vo[I]);
}
DEV void p3_issue_w(const Params& p, int c, int ds, int w, const unsigned (&vo)[7]) {
  p3_w_setup(ds, w);
  static_for<7>([&](auto I) { p3_issue_w1<I>(p, c, ds, w, vo); });
}

DEV void p2_issue_x(const Params& p, int p64, int nt, int w, int lane) {
  const unsigned vx = (unsigned)(lane >> 3) * 1024u + (unsigned)(((lane & 7) ^ (lane >> 3)) * 16);
  const u16* xb = p.x + (64 * p64 + 16 * w) * 512 + nt * 64;
  set_m0((unsigned)(P2_X + 16 * w * 128));
  dma16i<0>(xb, vx);
  dma16i<1024>(xb, vx + 7168u);
}

template <int WSTRIDE>
DEV f32x16 reduce4(f32x16 (&acc)[2][2], LAS unsigned char* red, int w, int lane) {
  WAIT_LGKM();
#pragma unroll
  for (int a = 0; a < 2; ++a)
#pragma unroll
    for (int b = 0; b < 2; ++b) {
      const int dest = a + 2 * b;
      if (dest != w) {
        const int slot = (dest - w - 1) & 3;
#pragma unroll
        for (int qd = 0; qd < 4; ++qd)
          *(LAS f32x4*)(red + w * WSTRIDE + (slot * 4 + qd) * 1024 + lane * 16) =
              f32x4{acc[a][b][4 * qd], acc[a][b][4 * qd + 1], acc[a][b][4 * qd + 2], acc[a][b][4 * qd + 3]};
      }
    }
  BAR_LDS();
  f32x16 r;
  if (w == 0) r = acc[0][0]; else if (w == 1) r = acc[1][0]; else if (w == 2) r = acc[0][1]; else r = acc[1][1];
#pragma unroll
  for (int s = 1; s < 4; ++s) {
    const int src = (w + s) & 3;
#pragma unroll
    for (int qd = 0; qd < 4; ++qd) {
      const f32x4 v = *(LAS f32x4*)(red + src * WSTRIDE + ((3 - s) * 4 + qd) * 1024 + lane * 16);
#pragma unroll
      for (int e = 0; e < 4; ++e) r[4 * qd + e] += v[e];
    }
  }
  return r;
}

DEV void phase2(const Params& p, LAS unsigned char* L, __amdgpu_buffer_rsrc_t rs, int p64, int nt, int w, int lane, v4u (&hkeep)[2],
                int c3, const unsigned (&vo)[7] TSARG) {
  v4u of[2][8];
#pragma unroll
  for (int i = 0; i < 8; ++i)
#pragma unroll
    for (int tt = 0; tt < 2; ++tt)
      of[tt][i] = __builtin_amdgcn_raw_buffer_load_b128(rs, lane * 16, (int)WS_O + ((2 * p64 + tt) * 32 + 8 * w + i) * 1024, AUX_SC1);
  SCHED();
  const int hh = lane >> 5, blk = (lane >> 4) & 1, q4 = (lane & 15) >> 2, pp = lane & 3;
  const int sw = ((q4 >> 1) & 1) << 2;
  LAS unsigned char* wb0 = L + P2_W + (128 * w + 8 * hh + q4) * 128 + (((2 * blk + (pp >> 1)) ^ sw) * 16) + 8 * (pp & 1);
  LAS unsigned char* wb1 = L + P2_W + (128 * w + 8 * hh + q4) * 128 + (((4 + 2 * blk + (pp >> 1)) ^ sw) * 16) + 8 * (pp & 1);
  f32x16 acc[2][2];
#pragma unroll
  for (int a = 0; a < 2; ++a)
#pragma unroll
    for (int b = 0; b < 2; ++b)
#pragma unroll
      for (int e = 0; e < 16; ++e) acc[a][b][e] = 0.f;
  v4u wa[8][2];
#pragma unroll
  for (int i = 0; i < 8; ++i) {
    wa[i][0] = tr_read2(wb0 + i * 2048, wb0 + i * 2048 + 512);
    wa[i][1] = tr_read2(wb1 + i * 2048, wb1 + i * 2048 + 512);
  }
  SCHED();
#pragma unroll
  for (int i = 0; i < 8; ++i) {
    acc[0][0] = mfma32(wa[i][0], of[0][i], acc[0][0]);
    acc[1][0] = mfma32(wa[i][1], of[0][i], acc[1][0]);
    acc[0][1] = mfma32(wa[i][0], of[1][i], acc[0][1]);
    acc[1][1] = mfma32(wa[i][1], of[1][i], acc[1][1]);
  }
  SCHED();
  STAMP(13);
  p3_issue_w(p, c3, 2, w, vo);
  asm volatile("s_waitcnt vmcnt(7)" ::: "memory");
  f32x16 r = reduce4<16384>(acc, L + P2_W, w, lane);
  const int row = 32 * (w >> 1) + (lane & 31);
  float hv[16];
  float s1 = 0.f, s2 = 0.f;
#pragma unroll
  for (int qd = 0; qd < 4; ++qd) {
    const v2u xr = *(LAS v2u*)(L + P2_X + row * 128 + (((4 * (w & 1) + qd) ^ (row & 7)) * 16) + 8 * hh);
    const unsigned y01 = pk_bf16(r[4 * qd + 0], r[4 * qd + 1]), y23 = pk_bf16(r[4 * qd + 2], r[4 * qd + 3]);
    const unsigned h01 = pk_bf16(bf_lo(xr.x) + bf_lo(y01), bf_hi(xr.x) + bf_hi(y01));
    const unsigned h23 = pk_bf16(bf_lo(xr.y) + bf_lo(y23), bf_hi(xr.y) + bf_hi(y23));
    hkeep[qd >> 1][2 * (qd & 1)] = h01; hkeep[qd >> 1][2 * (qd & 1) + 1] = h23;
    hv[4 * qd + 0] = bf_lo(h01); hv[4 * qd + 1] = bf_hi(h01); hv[4 * qd + 2] = bf_lo(h23); hv[4 * qd + 3] = bf_hi(h23);
  }
  const int tile32 = 2 * p64 + (w >> 1);
  const int nt32 = 2 * nt + (w & 1);
#pragma unroll
  for (int ks = 0; ks < 2; ++ks)
    __builtin_amdgcn_raw_buffer_store_b128(hkeep[ks], rs, lane * 16, (int)WS_H + (tile32 * 32 + 2 * nt32 + ks) * 1024, AUX_SC1);
#pragma unroll
  for (int i = 0; i < 16; ++i) { s1 += hv[i]; s2 += hv[i] * hv[i]; }
  s1 = xsum32(s1); s2 = xsum32(s2);
  if (hh == 0) {
    v2u st = {__float_as_uint(s1), __float_as_uint(s2)};
    __builtin_amdgcn_raw_buffer_store_b64(st, rs, (lane & 31) * 16, (int)WS_HS + (tile32 * 8 + (nt32 >> 1)) * 512 + (nt32 & 1) * 8, AUX_SC1);
  }
}

constexpr int P3_KM = 20;
constexpr int p3_nl(int ks) { return ks < 4 ? 2 : 1; }
constexpr int p3_nd(int ks) { return (ks >= 4 && ks < 16) ? ((ks & 3) == 3 ? 1 : 2) : 0; }
constexpr int p3_nx(int ks) { return ks == 7 ? 1 : (ks == 16 || ks == 17) ? 2 : 0; }
constexpr int p3_cum(int ks) { int c = 12; for (int k = 0; k < ks; ++k) c += p3_nl(k) + p3_nd(k) + p3_nx(k); return c; }
constexpr int p3_pos(int i) { return i < 8 ? 4 + i : i < 16 ? p3_cum((i - 8) / 2) + ((i - 8) & 1) : p3_cum(i - 12); }

DEV void phase3(const Params& p, LAS unsigned char* L, __amdgpu_buffer_rsrc_t rs, v4u rsv, GAS unsigned* ctl, unsigned tag, int p128, int c, int w, int lane,
                int nt4, const unsigned (&vo)[7] TSARG) {
  const int tile32 = 4 * p128 + w;
  const int hh = lane >> 5, blk = (lane >> 4) & 1, q4 = (lane & 15) >> 2, pp = lane & 3;
  v4u hb[32];
  v4u hs[4];
  const unsigned hbase = WS_H + tile32 * 32768;
  static_for<4>([&](auto I) { bload_v<I * 512>(hs[I], rsv, (4 * hh) * 512 + (lane & 31) * 16, WS_HS + tile32 * 4096); });
  static_for<8>([&](auto I) { bload_a<(I & 3) * 1024>(hb[I], rsv, lane * 16, hbase + (I >> 2) * 4096); });
  SCHED();
  const int abase = blk * P3_UP + (4 * hh + q4) * 224 + pp * 8;
  int ab0 = abase, ab1 = abase + 65536, ab2 = abase + 123136;
  PIN_V(ab0); PIN_V(ab1); PIN_V(ab2);
  auto slot_ptr = [&](int ds) -> LAS unsigned char* {
    const int a = p3_slot_addr(ds);
    return a == 123136 ? L + ab2 : a >= 65536 ? L + ab1 + (a - 65536) : L + ab0 + a;
  };
  f32x16 acc[7];
#pragma unroll
  for (int t = 0; t < 7; ++t)
#pragma unroll
    for (int e = 0; e < 16; ++e) acc[t][e] = 0.f;
  WAIT_VM_BAR(12);
  STAMP(14);
  v4u an[7];
  {
    LAS unsigned char* sl = slot_ptr(0);
#pragma unroll
    for (int t = 0; t < 7; ++t) an[t] = tr_read2(sl + t * 32, sl + t * 32 + 8 * 224);
  }
  SCHED();
  unsigned csflag = 0;
  v4u csl[4];
  static_for<P3_KM>([&](auto KS) {
    constexpr int ks = KS;
    v4u ac[7];
#pragma unroll
    for (int t = 0; t < 7; ++t) ac[t] = an[t];
    wait_vm_a<p3_cum(ks) - 1 - p3_pos(ks)>(hb[ks]);
    constexpr int ds = (ks + 1) >> 2, ksl = (ks + 1) & 3;
    LAS unsigned char* sl = slot_ptr(ds) + ksl * 16 * 224;
    auto vm = [&](int slot) {
      constexpr int nl = p3_nl(ks), nd = p3_nd(ks), nx = p3_nx(ks);
      if constexpr (true) {
        if (slot < nl) {
          if constexpr (ks < 4) {
            if (slot == 0) bload_a<((8 + 2 * ks) & 3) * 1024>(hb[8 + 2 * ks], rsv, lane * 16, hbase + ((8 + 2 * ks) >> 2) * 4096);
            else bload_a<((9 + 2 * ks) & 3) * 1024>(hb[9 + 2 * ks], rsv, lane * 16, hbase + ((9 + 2 * ks) >> 2) * 4096);
          } else bload_a<((12 + ks) & 3) * 1024>(hb[(12 + ks) & 31], rsv, lane * 16, hbase + ((12 + ks) >> 2) * 4096);
        } else if (slot < nl + nd) {
          if constexpr (nd > 0) {
            if constexpr ((ks & 3) == 0) { if (slot == nl) p3_w_setup(5 + ((ks - 4) >> 2), w); }
            if (slot == nl) p3_issue_w1<2 * (ks & 3)>(p, c, 5 + ((ks - 4) >> 2), w, vo);
            else p3_issue_w1<(2 * (ks & 3) + 1) % 7>(p, c, 5 + ((ks - 4) >> 2), w, vo);
          }
        } else if (slot < nl + nd + nx) {
          if constexpr (ks == 7) bload1_v(csflag, rsv, (unsigned)(FL_F + ((16 + (lane & 15)) * 16 + c) * 32) * 4u, 0u);
          if constexpr (ks == 16 || ks == 17) {
            const int i = slot - nl - nd;
            const unsigned vof = (unsigned)(lane < 56 ? lane : 55) * 16u;
            const unsigned so = WS_CS + (unsigned)(c * 16 + 4 * w) * 896u;
            if constexpr (ks == 16) { if (i == 0) bload_v<0>(csl[0], rsv, vof, so); else bload_v<896>(csl[1], rsv, vof, so); }
            else { if (i == 0) bload_v<1792>(csl[2], rsv, vof, so); else bload_v<2688>(csl[3], rsv, vof, so); }
          }
        }
      }
    };
    if constexpr (ks == 12) {
      wait_vm_v1<p3_cum(12) - 1 - (p3_cum(7) + p3_nl(7) + p3_nd(7))>(csflag);
      if (!__all(csflag == tag)) wait_flags(flag_ptr(ctl, 1, lane & 15, c), tag);
    }
    static_for<7>([&](auto T) {
      constexpr int t = T;
      if constexpr (t == 0 || t == 2) asm volatile("" : "+v"(ac[t]), "+v"(ac[t + 1]));
      if constexpr (t == 4) asm volatile("" : "+v"(ac[4]), "+v"(ac[5]), "+v"(ac[6]));
      acc[t] = mfma32(ac[t], hb[ks], acc[t]);
      SCHED();
      if constexpr (ks + 1 < P3_KM) {
        if constexpr (t == 1 || t == 3 || t == 5) {
          an[t - 1] = tr_read2(sl + (t - 1) * 32, sl + (t - 1) * 32 + 8 * 224);
          an[t] = tr_read2(sl + t * 32, sl + t * 32 + 8 * 224);
        }
        if constexpr (t == 6) an[6] = tr_read2(sl + 6 * 32, sl + 6 * 32 + 8 * 224);
      }
      if constexpr (t == 0 || t == 2 || t == 4) vm(t >> 1);
      SCHED();
    });
    if constexpr ((ks & 3) == 3 && (ks >> 2) < 3) BAR_LDS();
  });
  STAMP(15);
  asm volatile("s_waitcnt vmcnt(0)" : "+v"(hs[0]), "+v"(hs[1]), "+v"(hs[2]), "+v"(hs[3]), "+v"(csl[0]), "+v"(csl[1]), "+v"(csl[2]), "+v"(csl[3]) :: "memory");
#pragma unroll
  for (int k = P3_KM; k < 32; ++k) PIN_A(hb[k]);
  LAS float* cspart = (LAS float*)(L + P3_CSP);
  int cspo = P3_CSP + 896 * 4 + 16 * hh; PIN_V(cspo);
  LAS float* csp = cspart + 896;
  LAS float* csq_ = (LAS float*)(L + cspo);
  {
    f32x4 a;
#pragma unroll
    for (int e = 0; e < 4; ++e)
      a[e] = (__uint_as_float(csl[0][e]) + __uint_as_float(csl[1][e])) + (__uint_as_float(csl[2][e]) + __uint_as_float(csl[3][e]));
    if (lane < 56) *(LAS f32x4*)(cspart + w * 224 + lane * 4) = a;
  }
  float s1 = 0.f, s2 = 0.f;
#pragma unroll
  for (int i = 0; i < 4; ++i) {
    s1 += __uint_as_float(hs[i].x) + __uint_as_float(hs[i].z);
    s2 += __uint_as_float(hs[i].y) + __uint_as_float(hs[i].w);
  }
  s1 = xsum32(s1); s2 = xsum32(s2);
  const float mean = s1 * (1.f / 512.f);
  const float rstd = __builtin_amdgcn_rsqf(fmaxf(s2 * (1.f / 512.f) - mean * mean, 0.f) + 1e-5f);
  BAR_LDS();
  auto rdt = [&](int t, v4u (&f)[12]) {
#pragma unroll
    for (int k = 0; k < 12; ++k) {
      const int ks = P3_KM + k, ds = ks >> 2, ksl = ks & 3;
      LAS unsigned char* sl = slot_ptr(ds) + ksl * 16 * 224 + t * 32;
      f[k] = tr_read2(sl, sl + 8 * 224);
    }
  };
  v4u fn[12];
  rdt(0, fn);
  if (lane < 56) {
    const f32x4 a = (*(LAS f32x4*)(cspart + lane * 4) + *(LAS f32x4*)(cspart + 224 + lane * 4)) +
                    (*(LAS f32x4*)(cspart + 448 + lane * 4) + *(LAS f32x4*)(cspart + 672 + lane * 4));
    *(LAS f32x4*)(csp + lane * 4) = a;
  }
  WAIT_LGKM();
  const f32x2 nm2 = {-mean, -mean};
  const float r2s = rstd * rstd;
  const f32x2 k1 = {-2.302208198f * rstd, -2.302208198f * rstd}, k2 = {-0.1029432397f * rstd * r2s, -0.1029432397f * rstd * r2s};
  const f32x2 rr2 = {r2s, r2s}, one2 = {1.f, 1.f};
  f32x2 yg[4], yu[4], zz[4], rc[4];
  f32x4 cgu[4];
  unsigned gw[4];
  auto epi_piece = [&](auto T, auto K) {
    constexpr int t = T, k = K;
    auto csg = [&](int i) { return pk2(cgu[i >> 1][2 * (i & 1)], cgu[i >> 1][2 * (i & 1) + 1]); };
    auto csu = [&](int i) { return pk2(cgu[2 + (i >> 1)][2 * (i & 1)], cgu[2 + (i >> 1)][2 * (i & 1) + 1]); };
    auto ex2 = [&](int i) { zz[i].x = __builtin_amdgcn_exp2f(zz[i].x); zz[i].y = __builtin_amdgcn_exp2f(zz[i].y); };
    auto rc2 = [&](int i) { rc[i].x = __builtin_amdgcn_rcpf(zz[i].x); rc[i].y = __builtin_amdgcn_rcpf(zz[i].y); };
    if constexpr (k == 0) {
      cgu[0] = *(LAS f32x4*)(csq_ + t * 16);
      cgu[1] = *(LAS f32x4*)(csq_ + t * 16 + 8);
      cgu[2] = *(LAS f32x4*)(csq_ + 112 + t * 16);
      cgu[3] = *(LAS f32x4*)(csq_ + 112 + t * 16 + 8);
    } else if constexpr (k == 1) {
      PIN_A(acc[t]);
#pragma unroll
      for (int i = 0; i < 4; ++i) { yg[i] = pk2(acc[t][2 * i], acc[t][2 * i + 1]); PIN_V(yg[i]); }
    } else if constexpr (k == 2) {
#pragma unroll
      for (int i = 0; i < 4; ++i) { yu[i] = pk2(acc[t][8 + 2 * i], acc[t][8 + 2 * i + 1]); PIN_V(yu[i]); }
    } else if constexpr (k == 3) {
#pragma unroll
      for (int i = 0; i < 4; ++i) yg[i] = nm2 * csg(i) + yg[i];
      yu[0] = nm2 * csu(0) + yu[0]; yu[1] = nm2 * csu(1) + yu[1];
    } else if constexpr (k == 4) {
#pragma unroll
      for (int i = 0; i < 4; ++i) zz[i] = yg[i] * yg[i];
      yu[2] = nm2 * csu(2) + yu[2]; yu[3] = nm2 * csu(3) + yu[3];
    } else if constexpr (k == 5) {
#pragma unroll
      for (int i = 0; i < 4; ++i) zz[i] = zz[i] * k2 + k1;
      yu[0] = yu[0] * yg[0]; yu[1] = yu[1] * yg[1];
    } else if constexpr (k == 6) {
#pragma unroll
      for (int i = 0; i < 4; ++i) zz[i] = zz[i] * yg[i];
      yu[2] = yu[2] * yg[2]; yu[3] = yu[3] * yg[3];
    } else if constexpr (k == 7) {
      ex2(0); ex2(1);
      yu[0] = yu[0] * rr2; yu[1] = yu[1] * rr2;
    } else if constexpr (k == 8) {
      ex2(2); ex2(3);
      zz[0] += one2; zz[1] += one2;
    } else if constexpr (k == 9) {
      rc2(0); rc2(1);
      zz[2] += one2; zz[3] += one2;
    } else if constexpr (k == 10) {
      rc2(2); rc2(3);
      yu[2] = yu[2] * rr2; yu[3] = yu[3] * rr2;
      yu[0] = yu[0] * rc[0]; yu[1] = yu[1] * rc[1];
    } else {
      yu[2] = yu[2] * rc[2]; yu[3] = yu[3] * rc[3];
#pragma unroll
      for (int i = 0; i < 4; ++i) gw[i] = cvt2(yu[i]);
      const v4u f = {gw[0], gw[1], gw[2], gw[3]};
      __builtin_amdgcn_raw_buffer_store_b128(f, rs, lane * 16, (int)WS_G + (tile32 * 112 + 7 * c + t) * 1024, AUX_SC1);
    }
  };
  unsigned wv4[4];
  wrow_voffs(lane, wv4);
  const u16* wdb = p.wd + (448 * w) * 512 + nt4 * 64;
  SCHED();
  STAMP(18);
  static_for<7>([&](auto T) {
    constexpr int t = T;
    v4u fc[12];
#pragma unroll
    for (int k = 0; k < 12; ++k) fc[k] = fn[k];
    static_for<12>([&](auto K) {
      constexpr int k = K;
      acc[t] = mfma32(fc[k], hb[P3_KM + k], acc[t]);
      SCHED();
      if constexpr (t < 6) {
        constexpr int ks = P3_KM + k, ds = ks >> 2, ksl = ks & 3;
        LAS unsigned char* sl = slot_ptr(ds) + ksl * 16 * 224 + (t + 1) * 32;
        fn[k] = tr_read2(sl, sl + 8 * 224);
      }
      if constexpr (t > 0) epi_piece(std::integral_constant<int, t - 1>{}, K);
      if constexpr (t >= 1 && t < 5 && (k & 3) == 0) {
        constexpr int gq = t - 1;
        if constexpr (k == 0) set_m0((unsigned)(w * P4_EARLY + gq * 4096));
        wrow_issue<(k >> 2)>(wdb + gq * 32 * 512, wv4);
        if constexpr (k == 8) wrow_issue<3>(wdb + gq * 32 * 512, wv4);
      }
      SCHED();
    });
  });
  STAMP(19);
  asm volatile("s_nop 15\n\ts_nop 7" ::: "memory");
  SCHED();
  static_for<12>([&](auto K) { epi_piece(std::integral_constant<int, 6>{}, K); });
}

constexpr int P4_UP = 10;
DEV void phase4(const Params& p, LAS unsigned char* L, v4u rsv, int p64, int nt, int w, int lane,
                const v4u (&hkeep)[2], const v4u (&wr)[8][2] TSARG) {
  const int hh = lane >> 5, blk = (lane >> 4) & 1, q4 = (lane & 15) >> 2, pp = lane & 3;
  const int sw = ((q4 >> 1) & 1) << 2;
  LAS unsigned char* wb0 = L + w * P4_WAVE + (4 * hh + q4) * 128 + (((2 * blk + (pp >> 1)) ^ sw) * 16) + 8 * (pp & 1);
  LAS unsigned char* wb1 = L + w * P4_WAVE + (4 * hh + q4) * 128 + (((4 + 2 * blk + (pp >> 1)) ^ sw) * 16) + 8 * (pp & 1);
  f32x16 acc[2][2];
#pragma unroll
  for (int a = 0; a < 2; ++a)
#pragma unroll
    for (int b = 0; b < 2; ++b)
#pragma unroll
      for (int e = 0; e < 16; ++e) acc[a][b][e] = 0.f;
  v4u gf[2][28];
  const unsigned gb0 = WS_G + ((2 * p64) * 112 + 28 * w) * 1024, gb1 = gb0 + 112 * 1024;
  auto ldg = [&](auto I) {
    constexpr int i = I;
    if constexpr (i < 24) {
      bload_a<(i & 3) * 1024>(gf[0][i], rsv, lane * 16, gb0 + (i >> 2) * 4096);
      bload_a<(i & 3) * 1024>(gf[1][i], rsv, lane * 16, gb1 + (i >> 2) * 4096);
    } else {
      bload_v<(i & 3) * 1024>(gf[0][i], rsv, lane * 16, gb0 + (i >> 2) * 4096);
      bload_v<(i & 3) * 1024>(gf[1][i], rsv, lane * 16, gb1 + (i >> 2) * 4096);
    }
  };
  static_for<P4_UP>(ldg);
  SCHED();
  v4u an0 = wr[0][0], an1 = wr[0][1];
  static_for<28>([&](auto I) {
    constexpr int i = I;
    constexpr int cum = (2 * (P4_UP + i) < 56) ? 2 * (P4_UP + i) : 56;
    constexpr int N = cum - 2 * i - 2;
    if constexpr (i < 24) wait_vm_a2<N>(gf[0][i], gf[1][i]); else wait_vm_v2<N>(gf[0][i], gf[1][i]);
    const v4u a0 = an0, a1 = an1;
    if constexpr (i + 1 < 8) { an0 = wr[i + 1][0]; an1 = wr[i + 1][1]; }
    else if constexpr (i + 1 < 28) {
      constexpr int sl = i + 1 - 8;
      an0 = tr_read2(wb0 + sl * 2048, wb0 + sl * 2048 + 8 * 128);
      an1 = tr_read2(wb1 + sl * 2048, wb1 + sl * 2048 + 8 * 128);
    }
    acc[0][0] = mfma32(a0, gf[0][i], acc[0][0]);
    acc[1][0] = mfma32(a1, gf[0][i], acc[1][0]);
    SCHED();
    if constexpr (i + P4_UP < 28) ldg(std::integral_constant<int, i + P4_UP>{});
    SCHED();
    acc[0][1] = mfma32(a0, gf[1][i], acc[0][1]);
    acc[1][1] = mfma32(a1, gf[1][i], acc[1][1]);
    SCHED();
  });
  STAMP(16);
  f32x16 r = reduce4<P4_WAVE>(acc, L, w, lane);
  const int tok = 64 * p64 + 32 * (w >> 1) + (lane & 31);
  const int n0 = 64 * nt + 32 * (w & 1) + 4 * hh;
#pragma unroll
  for (int qd = 0; qd < 4; ++qd) {
    const unsigned h01 = hkeep[qd >> 1][2 * (qd & 1)], h23 = hkeep[qd >> 1][2 * (qd & 1) + 1];
    const unsigned y01 = pk_bf16(r[4 * qd + 0], r[4 * qd + 1]), y23 = pk_bf16(r[4 * qd + 2], r[4 * qd + 3]);
    v2u o = {pk_bf16(bf_lo(h01) + bf_lo(y01), bf_hi(h01) + bf_hi(y01)), pk_bf16(bf_lo(h23) + bf_lo(y23), bf_hi(h23) + bf_hi(y23))};
    *(GAS v2u*)(p.out + tok * 512 + n0 + 8 * qd) = o;
  }
}
DEV void p4_read_wr(LAS unsigned char* L, int w, int lane, v4u (&wr)[8][2]) {
  const int hh = lane >> 5, blk = (lane >> 4) & 1, q4 = (lane & 15) >> 2, pp = lane & 3;
  const int sw = ((q4 >> 1) & 1) << 2;
  LAS unsigned char* wb0 = L + w * P4_EARLY + (4 * hh + q4) * 128 + (((2 * blk + (pp >> 1)) ^ sw) * 16) + 8 * (pp & 1);
  LAS unsigned char* wb1 = L + w * P4_EARLY + (4 * hh + q4) * 128 + (((4 + 2 * blk + (pp >> 1)) ^ sw) * 16) + 8 * (pp & 1);
#pragma unroll
  for (int i = 0; i < 8; ++i) {
    wr[i][0] = tr_read2(wb0 + i * 2048, wb0 + i * 2048 + 8 * 128);
    wr[i][1] = tr_read2(wb1 + i * 2048, wb1 + i * 2048 + 8 * 128);
  }
}

__global__ void __launch_bounds__(256, 1) mk_kernel(Params p) {
  extern __shared__ __attribute__((aligned(16))) unsigned char smem[];
  LAS unsigned char* L = (LAS unsigned char*)smem;
  const int tid = threadIdx.x, lane = tid & 63, w = __builtin_amdgcn_readfirstlane(tid >> 6);
  const int b = blockIdx.x;
  const int dom = (b & 7) * 2 + (b >> 7), j = (b >> 3) & 15;
  const int p64 = 2 * dom + (j >> 3), j8 = j & 7;
  const __amdgpu_buffer_rsrc_t rs = __builtin_amdgcn_make_buffer_rsrc((void*)p.ws, 0, (int)WS_BYTES, 0x00020000);
  const v4u rsv = {(unsigned)(size_t)p.ws, (unsigned)((size_t)p.ws >> 32), WS_BYTES, 0x00020000u};
  GAS unsigned* ctl = (GAS unsigned*)(p.ws + WS_FLAGS);
  STAMP(0);
  unsigned wvo[6], vx[5];
  {
    const int tok0 = 64 * p64 - 16, s0 = (p64 & 3) * 2;
    p1_xvoff(tok0, w, lane, vx);
    p1_wvoff(w, lane, wvo);
    p1_x_setup(s0, w);
    static_for<5>([&](auto I) { p1_issue_x1<I>(p, s0, w, lane, vx); });
    p1_w_setup(0, w);
    static_for<6>([&](auto I) { p1_issue_w1<I>(p, j8, s0, wvo); });
    p1_w_setup(1, w);
    static_for<6>([&](auto I) { p1_issue_w1<I>(p, j8, s0 + 1, wvo); });
    if (w == 0) *(LAS v4u*)(L + P1_X + (lane >> 3) * 10240 + (lane & 7) * 16) = v4u{0x3f803f80u, 0x3f803f80u, 0x3f803f80u, 0x3f803f80u};
  }
  STAMP(1);
  phase1(p, L, rs, p64, j8, w, lane, wvo, vx TSPASS);
  asm volatile("" ::: "memory"); SCHED();
  STAMP(2);
  const unsigned tag = __hip_atomic_load(ctl + FL_EPOCH, __ATOMIC_RELAXED, __HIP_MEMORY_SCOPE_AGENT) + 1u;
  WAIT_VM_BAR(0);
  if (tid == 0) __hip_atomic_store(flag_ptr(ctl, 0, dom, j), tag, __ATOMIC_RELAXED, __HIP_MEMORY_SCOPE_AGENT);
  STAMP(3);
  v4u csv4[4];
  cs_load(p, j, dom, tid, csv4);
  SCHED();
  unsigned vo[7];
  p3_voffs(lane, vo);
  p2_issue_x(p, p64, j8, w, lane);
#pragma unroll
  for (int ds = 0; ds < 2; ++ds) p3_issue_w(p, j, ds, w, vo);
  WAIT_VM(16);
  cs_finish(L, rs, j, dom, w, lane, csv4);
  wait_flags(flag_ptr(ctl, 0, dom, (j & 8) + (lane & 7)), tag);
  STAMP(4);
  v4u hkeep[2];
  phase2(p, L, rs, p64, j8, w, lane, hkeep, j, vo TSPASS);
  STAMP(5);
  WAIT_VM_BAR(0);
  if (tid == 0) __hip_atomic_store(flag_ptr(ctl, 1, dom, j), tag, __ATOMIC_RELAXED, __HIP_MEMORY_SCOPE_AGENT);
  STAMP(6);
#pragma unroll
  for (int ds = 3; ds < 5; ++ds) p3_issue_w(p, j, ds, w, vo);
  wait_flags(flag_ptr(ctl, 1, dom, lane & 15), tag);
  STAMP(7);
  phase3(p, L, rs, rsv, ctl, tag, dom, j, w, lane, j8, vo TSPASS);
  STAMP(8);
  v4u wr[8][2];
  WAIT_VM(0);
  p4_read_wr(L, w, lane, wr);
  BAR_LDS();
  if (tid == 0) __hip_atomic_store(flag_ptr(ctl, 2, dom, j), tag, __ATOMIC_RELAXED, __HIP_MEMORY_SCOPE_AGENT);
  STAMP(9);
  {
    unsigned wv4[4];
    wrow_voffs(lane, wv4);
    const u16* wdb = p.wd + (448 * w + 128) * 512 + j8 * 64;
    static_for<10>([&](auto G) {
      constexpr int gq = G;
      set_m0((unsigned)(w * P4_WAVE + gq * 4096));
      static_for<4>([&](auto I) { wrow_issue<I>(wdb + gq * 32 * 512, wv4); });
    });
  }
  wait_flags(flag_ptr(ctl, 2, dom, lane & 15), tag);
  STAMP(10);
  if (b == 0 && tid == 0) __hip_atomic_store(ctl + FL_EPOCH, tag, __ATOMIC_RELAXED, __HIP_MEMORY_SCOPE_AGENT);
  phase4(p, L, rsv, p64, j8, w, lane, hkeep, wr TSPASS);
  STAMP(17);
}

extern "C" void kernel_launch(void* const* d_in, const int* in_sizes, int n_in, void* d_out, int out_size,
                              void* d_ws, size_t ws_size, hipStream_t stream) {
  Params p;
  p.x = (const u16*)d_in[0];
  p.wqkv = (const u16*)d_in[1];
  p.wo = (const u16*)d_in[2];
  p.wg = (const u16*)d_in[4];
  p.wu = (const u16*)d_in[5];
  p.wd = (const u16*)d_in[6];
  p.out = (u16*)d_out;
  p.ws = (unsigned char*)d_ws;
  (void)hipFuncSetAttribute((const void*)mk_kernel, hipFuncAttributeMaxDynamicSharedMemorySize, LDS_BYTES);
  mk_kernel<<<256, 256, LDS_BYTES, stream>>>(p);
}
```

```cpp
#include <hip/hip_runtime.h>
#include <stdint.h>
#include <utility>
#include <cstdio>

#define MK_STAMP 1

typedef unsigned short u16;
typedef unsigned v4u __attribute__((ext_vector_type(4)));
typedef unsigned v2u __attribute__((ext_vector_type(2)));
typedef short v8s __attribute__((ext_vector_type(8)));
typedef short v4s __attribute__((ext_vector_type(4)));
typedef float f32x2 __attribute__((ext_vector_type(2)));
typedef float f32x4 __attribute__((ext_vector_type(4)));
typedef float f32x16 __attribute__((ext_vector_type(16)));
typedef __bf16 bf16x2 __attribute__((ext_vector_type(2)));
#define GAS __attribute__((address_space(1)))
#define LAS __attribute__((address_space(3)))
#define DEV __device__ __forceinline__

constexpr unsigned WS_FLAGS = 0;
constexpr unsigned WS_O = 131072;
constexpr unsigned WS_H = WS_O + 2097152;
constexpr unsigned WS_HS = WS_H + 2097152;
constexpr unsigned WS_G = WS_HS + 262144;
constexpr unsigned WS_CS = WS_G + 64 * 112 * 1024;
constexpr unsigned WS_STAMP = WS_CS + 16 * 16 * 224 * 4;
constexpr unsigned WS_BYTES = WS_STAMP + 256 * 4 * 48 * 8;
constexpr int FL_EPOCH = 0, FL_F = 1024;

constexpr int LDS_BYTES = 163840;
constexpr int AUX_SC1 = 16;

struct Params {
  const u16* x; const u16* wqkv; const u16* wo; const u16* wg; const u16* wu; const u16* wd;
  u16* out; unsigned char* ws;
};

template <class F, int... I> DEV void static_for_impl(F&& f, std::integer_sequence<int, I...>) { (f(std::integral_constant<int, I>{}), ...); }
template <int N, class F> DEV void static_for(F&& f) { static_for_impl(f, std::make_integer_sequence<int, N>{}); }

DEV float bf_lo(unsigned u) { return __uint_as_float(u << 16); }
DEV float bf_hi(unsigned u) { return __uint_as_float(u & 0xffff0000u); }
DEV unsigned pk_bf16(float a, float b) {
  bf16x2 p; p[0] = (__bf16)a; p[1] = (__bf16)b;
  return __builtin_bit_cast(unsigned, p);
}
DEV f32x2 pk2(float a, float b) { return f32x2{a, b}; }
DEV unsigned cvt2(f32x2 v) { return pk_bf16(v.x, v.y); }
DEV float dot2(unsigned a, unsigned b, float c) {
  return __builtin_amdgcn_fdot2_f32_bf16(__builtin_bit_cast(bf16x2, a), __builtin_bit_cast(bf16x2, b), c, false);
}
DEV f32x4 mfma16(v4u a, v4u b, f32x4 c) {
  return __builtin_amdgcn_mfma_f32_16x16x32_bf16(__builtin_bit_cast(v8s, a), __builtin_bit_cast(v8s, b), c, 0, 0, 0);
}
DEV f32x4 mfma16k16(v2u a, v2u b, f32x4 c) {
  return __builtin_amdgcn_mfma_f32_16x16x16bf16_1k(__builtin_bit_cast(v4s, a), __builtin_bit_cast(v4s, b), c, 0, 0, 0);
}
DEV f32x16 mfma32(v4u a, v4u b, f32x16 c) {
  return __builtin_amdgcn_mfma_f32_32x32x16_bf16(__builtin_bit_cast(v8s, a), __builtin_bit_cast(v8s, b), c, 0, 0, 0);
}
DEV v2u tr_read(LAS unsigned char* p) {
  return __builtin_bit_cast(v2u, __builtin_amdgcn_ds_read_tr16_b64_v4i16((LAS v4s*)p));
}
DEV v4u tr_read2(LAS unsigned char* p0, LAS unsigned char* p1) {
  v2u a = tr_read(p0), b = tr_read(p1);
  return v4u{a.x, a.y, b.x, b.y};
}
DEV void dma16(const void* sbase, unsigned voff, unsigned lds_dst) {
  lds_dst = __builtin_amdgcn_readfirstlane(lds_dst);
  asm volatile("s_mov_b32 m0, %2\n\ts_nop 0\n\tglobal_load_lds_dwordx4 %0, %1"
               :: "v"(voff), "s"(sbase), "s"(lds_dst) : "memory");
}
DEV void set_m0(unsigned v) {
  v = __builtin_amdgcn_readfirstlane(v);
  asm volatile("s_mov_b32 m0, %0\n\ts_nop 0" :: "s"(v) : "memory");
}
template <int IMM> DEV void dma16i(const void* sbase, unsigned voff) {
  asm volatile("global_load_lds_dwordx4 %0, %1 offset:%c2" :: "v"(voff), "s"(sbase), "i"(IMM) : "memory");
}
template <int IMM> DEV void bload_a(v4u& dst, v4u rsrc, unsigned voff, unsigned soff) {
  asm volatile("buffer_load_dwordx4 %0, %1, %2, %3 offen offset:%c4 sc1" : "=a"(dst) : "v"(voff), "s"(rsrc), "s"(soff), "i"(IMM) : "memory");
}
template <int IMM> DEV void bload_v(v4u& dst, v4u rsrc, unsigned voff, unsigned soff) {
  asm volatile("buffer_load_dwordx4 %0, %1, %2, %3 offen offset:%c4 sc1" : "=v"(dst) : "v"(voff), "s"(rsrc), "s"(soff), "i"(IMM) : "memory");
}
DEV void bload1_v(unsigned& dst, v4u rsrc, unsigned voff, unsigned soff) {
  asm volatile("buffer_load_dword %0, %1, %2, %3 offen sc1" : "=v"(dst) : "v"(voff), "s"(rsrc), "s"(soff) : "memory");
}
template <int N> DEV void wait_vm_a(v4u& x) { asm volatile("s_waitcnt vmcnt(%c1)" : "+a"(x) : "i"(N) : "memory"); }
template <int N> DEV void wait_vm_a2(v4u& x, v4u& y) { asm volatile("s_waitcnt vmcnt(%c2)" : "+a"(x), "+a"(y) : "i"(N) : "memory"); }
template <int N> DEV void wait_vm_v2(v4u& x, v4u& y) { asm volatile("s_waitcnt vmcnt(%c2)" : "+v"(x), "+v"(y) : "i"(N) : "memory"); }
template <int N> DEV void wait_vm_v1(unsigned& x) { asm volatile("s_waitcnt vmcnt(%c1)" : "+v"(x) : "i"(N) : "memory"); }
#define PIN_V(x) asm volatile("" : "+v"(x))
#define PIN_A(x) asm volatile("" : "+a"(x))
#define WAIT_VM(n) asm volatile("s_waitcnt vmcnt(" #n ")" ::: "memory")
#define WAIT_VM_BAR(n) asm volatile("s_waitcnt vmcnt(" #n ") lgkmcnt(0)\n\ts_barrier" ::: "memory")
#define BAR_LDS() asm volatile("s_waitcnt lgkmcnt(0)\n\ts_barrier" ::: "memory")
#define WAIT_LGKM() asm volatile("s_waitcnt lgkmcnt(0)" ::: "memory")
#define SCHED() __builtin_amdgcn_sched_barrier(0)

DEV float xsum16_32(float v) {
  auto r = __builtin_amdgcn_permlane16_swap(__float_as_uint(v), __float_as_uint(v), false, false);
  v = __uint_as_float(r[0]) + __uint_as_float(r[1]);
  auto s = __builtin_amdgcn_permlane32_swap(__float_as_uint(v), __float_as_uint(v), false, false);
  return __uint_as_float(s[0]) + __uint_as_float(s[1]);
}
DEV float xmax16_32(float v) {
  auto r = __builtin_amdgcn_permlane16_swap(__float_as_uint(v), __float_as_uint(v), false, false);
  v = fmaxf(__uint_as_float(r[0]), __uint_as_float(r[1]));
  auto s = __builtin_amdgcn_permlane32_swap(__float_as_uint(v), __float_as_uint(v), false, false);
  return fmaxf(__uint_as_float(s[0]), __uint_as_float(s[1]));
}
DEV float xsum32(float v) {
  auto s = __builtin_amdgcn_permlane32_swap(__float_as_uint(v), __float_as_uint(v), false, false);
  return __uint_as_float(s[0]) + __uint_as_float(s[1]);
}

#define STAMP(i) do { SCHED(); ts[i] = __builtin_amdgcn_s_memrealtime(); SCHED(); } while (0)
#define TSARG , unsigned long long (&ts)[48]
#define TSPASS , ts

DEV GAS unsigned* flag_ptr(GAS unsigned* ctl, int ph, int dom, int j) { return ctl + FL_F + ((ph * 16 + dom) * 16 + j) * 32; }
DEV void wait_flags(GAS unsigned* word, unsigned tag) {
  for (unsigned spins = 0; spins < (1u << 20); ++spins) {
    const unsigned v = __hip_atomic_load(word, __ATOMIC_RELAXED, __HIP_MEMORY_SCOPE_AGENT);
    if (__all(v == tag)) break;
    __builtin_amdgcn_s_sleep(1);
  }
  __builtin_amdgcn_fence(__ATOMIC_ACQUIRE, "wavefront");
  asm volatile("" ::: "memory");
}

constexpr int P1_X = 0;
constexpr int P1_W = 81920;
constexpr int P1_WSLOT = 24576;
constexpr int P1_MISC = 155648;
constexpr int P2_W = 0;
constexpr int P2_X = 151936;
constexpr int CS_BUF = 160128;
constexpr int P3_UP = 14464;
constexpr int P3_CSP = 151936;
DEV constexpr int p3_slot_addr(int ds) { return (ds % 5) == 0 ? 65536 : (ds % 5) == 1 ? 94336 : (ds % 5) == 2 ? 123136 : (ds % 5) == 3 ? 0 : 28800; }
constexpr int P4_EARLY = 16384;
constexpr int P4_WAVE = 40960;

DEV unsigned wrow_voff(int lane) { return (unsigned)(lane >> 3) * 1024u + (unsigned)((lane & 7) ^ (((lane >> 4) & 1) << 2)) * 16u; }

DEV void p1_xvoff(int tok0, int w, int lane, unsigned (&vx)[5]) {
#pragma unroll
  for (int i = 0; i < 5; ++i) {
    const int rg = 5 * (w & 1) + i;
    const int row = 8 * rg + (lane >> 3);
    int tok = tok0 + row; tok = tok < 0 ? 0 : tok;
    const int ch = (lane & 7) ^ ((row >> 1) & 7);
    vx[i] = (unsigned)tok * 1024u + (unsigned)ch * 16u + 4096u - (unsigned)((i - 2) * 1024);
  }
}
DEV void p1_x_setup(int sp0, int w) { set_m0((unsigned)(P1_X + (sp0 + (w >> 1)) * 10240 + (5 * (w & 1) + 2) * 1024)); }
template <int I> DEV void p1_issue_x1(const Params& p, int sp0, int w, int lane, const unsigned (&vx)[5]) {
  const unsigned char* base = (const unsigned char*)p.x + (sp0 + (w >> 1)) * 128 - 4096;
  if constexpr (I == 0) { if ((w & 1) || lane >= 8) dma16i<(I - 2) * 1024>(base, vx[I]); }
  else dma16i<(I - 2) * 1024>(base, vx[I]);
}
DEV void p1_wvoff(int w, int lane, unsigned (&vo)[6]) {
#pragma unroll
  for (int i = 0; i < 6; ++i) {
    const int idx = 6 * w + i;
    const int part = idx >> 3, rg = idx & 7;
    const int row = 8 * rg + (lane >> 3);
    const int f = ((row >> 1) & 1) | (((row >> 3) & 1) << 1);
    const int ch = (lane & 7) ^ (f << 1);
    vo[i] = (unsigned)row * 3072u + (unsigned)part * 1024u + (unsigned)ch * 16u + 4096u - (unsigned)((i - 3) * 1024);
  }
}
DEV void p1_w_setup(int slot, int w) { set_m0((unsigned)(P1_W + slot * P1_WSLOT + (6 * w + 3) * 1024)); }
template <int I> DEV void p1_issue_w1(const Params& p, int hg, int st, const unsigned (&vo)[6]) {
  const unsigned char* base = (const unsigned char*)p.wqkv + st * 64 * 3072 + hg * 128 - 4096;
  dma16i<(I - 3) * 1024>(base, vo[I]);
}
DEV void wrow_voffs(int lane, unsigned (&v)[4]) {
#pragma unroll
  for (int i = 0; i < 4; ++i) v[i] = wrow_voff(lane) + 7168u * i;
}
template <int I> DEV void wrow_issue(const u16* rowbase  , const unsigned (&v)[4]) { dma16i<I * 1024>(rowbase, v[I]); }

struct P1Frag { v4u xf[5]; v4u wq, wk, wv; };

DEV void phase1(const Params& p, LAS unsigned char* L, __amdgpu_buffer_rsrc_t rs, int p64, int hg, int w, int lane,
                const unsigned (&wvo)[6], const unsigned (&vx)[5] TSARG) {
  const int s0 = (p64 & 3) * 2;
  const int g = lane >> 4, tk = lane & 15, q4 = (lane & 15) >> 2, pp = lane & 3;
  const int fsw = ((q4 >> 1) & 1) | ((g & 1) << 1);
  const int wbase = P1_W + (8 * g + q4) * 128 + (((2 * w + (pp >> 1)) ^ (fsw << 1)) * 16) + 8 * (pp & 1);
  const int xsw = (tk >> 1) & 7;
  const int xb0 = P1_X + tk * 128 + ((g ^ xsw) * 16), xb1 = P1_X + tk * 128 + (((4 + g) ^ xsw) * 16);
  const int xbw = (w & 1) ? xb1 : xb0;

  f32x4 aq[5], ak[5], av[5], ssum[5];
  float s2[5];
#pragma unroll
  for (int t = 0; t < 5; ++t) {
    aq[t] = f32x4{0, 0, 0, 0}; ak[t] = f32x4{0, 0, 0, 0}; av[t] = f32x4{0, 0, 0, 0}; ssum[t] = f32x4{0, 0, 0, 0}; s2[t] = 0.f;
  }
  const v4u ones = {0x3f803f80u, 0x3f803f80u, 0x3f803f80u, 0x3f803f80u};

  WAIT_VM_BAR(6);
  STAMP(11);
  P1Frag cur;
  {
    LAS unsigned char* xs = L + s0 * 10240 + xb0;
    LAS unsigned char* wsl = L + wbase;
    cur.wk = tr_read2(wsl + 8192, wsl + 8192 + 512);
    cur.wv = tr_read2(wsl + 16384, wsl + 16384 + 512);
    cur.wq = tr_read2(wsl, wsl + 512);
#pragma unroll
    for (int t = 0; t < 5; ++t) cur.xf[t] = *(LAS v4u*)(xs + t * 2048);
  }
  SCHED();
  static_for<16>([&](auto KS) {
    constexpr int ks = KS;
    if constexpr (ks == 1) { WAIT_VM_BAR(5); }
    else if constexpr (ks == 3 || ks == 7 || ks == 11) { WAIT_VM_BAR(6); }
    else if constexpr (ks == 5 || ks == 9) { WAIT_VM_BAR(11); }
    else if constexpr (ks == 13) { WAIT_VM_BAR(0); }
    constexpr bool isx = (ks == 0 || ks == 4 || ks == 8);
    constexpr int wst = ks == 1 ? 2 : ks == 2 ? 3 : ks == 3 ? 4 : ks == 5 ? 5 : ks == 7 ? 6 : ks == 9 ? 7 : -1;
    if constexpr (isx) p1_x_setup((s0 + 2 + ks / 2) & 7, w);
    if constexpr (wst >= 0) p1_w_setup(wst % 3, w);
    auto dma = [&](auto D) {
      constexpr int d = D;
      if constexpr (isx && d < 5) p1_issue_x1<d>(p, (s0 + 2 + ks / 2) & 7, w, lane, vx);
      if constexpr (wst >= 0) p1_issue_w1<d>(p, hg, (s0 + wst) & 7, wvo);
    };
    P1Frag nxt;
    constexpr int kn = ks + 1, itn = kn >> 1, k2n = kn & 1;
    LAS unsigned char* xs = L + ((s0 + itn) & 7) * 10240 + (k2n ? xb1 : xb0);
    LAS unsigned char* wsl = L + wbase + (itn % 3) * P1_WSLOT + k2n * 4096;
    constexpr int G = ks >> 2, sub = ks & 3;
    LAS unsigned char* sx = L + ((s0 + 2 * G + (w >> 1)) & 7) * 10240 + xbw;
    v4u sva, svb;
    dma(std::integral_constant<int, 0>{});
    static_for<5>([&](auto T) {
      constexpr int t = T;
      if constexpr (ks < 15) {
        if constexpr (t == 0) nxt.wk = tr_read2(wsl + 8192, wsl + 8192 + 512);
        if constexpr (t == 1) nxt.wv = tr_read2(wsl + 16384, wsl + 16384 + 512);
        if constexpr (t == 2) nxt.wq = tr_read2(wsl, wsl + 512);
        nxt.xf[t] = *(LAS v4u*)(xs + t * 2048);
      }
      if constexpr (t == 0) sva = *(LAS v4u*)(sx + sub * 2048);
      if constexpr (t == 1 && sub == 3) svb = *(LAS v4u*)(sx + 4 * 2048);
      ak[t] = mfma16(cur.wk, cur.xf[t], ak[t]);
      av[t] = mfma16(cur.xf[t], cur.wv, av[t]);
      aq[t] = mfma16(cur.wq, cur.xf[t], aq[t]);
      if constexpr (t == 4) {
        ssum[sub] = mfma16(ones, sva, ssum[sub]);
#pragma unroll
        for (int e = 0; e < 4; ++e) s2[sub] = dot2(sva[e], sva[e], s2[sub]);
        if constexpr (sub == 3) {
          ssum[4] = mfma16(ones, svb, ssum[4]);
#pragma unroll
          for (int e = 0; e < 4; ++e) s2[4] = dot2(svb[e], svb[e], s2[4]);
        }
      }
      SCHED();
      dma(std::integral_constant<int, t + 1>{});
      SCHED();
    });
    if constexpr (ks < 15) cur = nxt;
  });
  STAMP(12);
  LAS f32x2* part = (LAS f32x2*)(L + P1_MISC);
  LAS float* fmean = (LAS float*)(L + P1_MISC + 2560 + w * 640);
  LAS float* frstd = fmean + 80;
#pragma unroll
  for (int t = 0; t < 5; ++t) {
    const float b = xsum16_32(s2[t]);
    if (g == 0) part[w * 80 + t * 16 + tk] = f32x2{ssum[t][0], b};
  }
  BAR_LDS();
  unsigned wv4[4];
  wrow_voffs(lane, wv4);
  const u16* wob = p.wo + (128 * w) * 512 + hg * 64;
  auto dmo = [&](auto G) {
    constexpr int gq = G;
    set_m0((unsigned)(P2_W + (128 * w + 32 * gq) * 128));
    static_for<4>([&](auto I) { wrow_issue<I>(wob + gq * 32 * 512, wv4); });
  };
  dmo(std::integral_constant<int, 0>{});
#pragma unroll
  for (int r = 0; r < 2; ++r) {
    const int tok = r * 64 + lane;
    if (tok < 80) {
      const f32x2 a = (part[tok] + part[80 + tok]) + (part[160 + tok] + part[240 + tok]);
      const float mean = a.x * (1.f / 512.f);
      const float var = fmaxf(a.y * (1.f / 512.f) - mean * mean, 0.f);
      fmean[tok] = mean; frstd[tok] = __builtin_amdgcn_rsqf(var + 1e-5f);
    }
  }
  f32x2 csq[2], csk[2];
#pragma unroll
  for (int r = 0; r < 4; ++r) {
    csq[r >> 1][r & 1] = __uint_as_float(__builtin_amdgcn_update_dpp(0, __float_as_uint(aq[0][r]), 0x150, 0xf, 0xf, false));
    csk[r >> 1][r & 1] = __uint_as_float(__builtin_amdgcn_update_dpp(0, __float_as_uint(ak[0][r]), 0x150, 0xf, 0xf, false));
  }
  float csv;
  {
    auto a = __builtin_amdgcn_permlane16_swap(__float_as_uint(av[0][0]), __float_as_uint(av[0][0]), false, false);
    auto c = __builtin_amdgcn_permlane32_swap(a[0], a[0], false, false);
    csv = __uint_as_float(c[0]);
  }
  dmo(std::integral_constant<int, 1>{});
  WAIT_LGKM();
  float m1[5], r1[5];
  f32x4 mv[5], rv[5];
#pragma unroll
  for (int t = 0; t < 5; ++t) {
    m1[t] = fmean[t * 16 + tk]; r1[t] = frstd[t * 16 + tk];
    mv[t] = *(LAS f32x4*)(fmean + t * 16 + 4 * g); rv[t] = *(LAS f32x4*)(frstd + t * 16 + 4 * g);
  }
  SCHED();
  v2u qp[4], kp[5], vp[5];
  const float sc = 0.25f * 1.44269504088896f;
  const f32x2 c2 = {csv, csv};
#pragma unroll
  for (int t = 0; t < 5; ++t) {
    const f32x2 m2 = {m1[t], m1[t]}, r2 = {r1[t], r1[t]};
    kp[t].x = cvt2((pk2(ak[t][0], ak[t][1]) - m2 * csk[0]) * r2);
    kp[t].y = cvt2((pk2(ak[t][2], ak[t][3]) - m2 * csk[1]) * r2);
    if (t > 0) {
      qp[t - 1].x = cvt2(pk2(aq[t][0], aq[t][1]) - m2 * csq[0]);
      qp[t - 1].y = cvt2(pk2(aq[t][2], aq[t][3]) - m2 * csq[1]);
    }
  }
  SCHED();
  const f32x4 z = {0, 0, 0, 0};
  f32x4 sp[4], scu[4];
#pragma unroll
  for (int t = 0; t < 4; ++t) { sp[t] = mfma16k16(kp[t], qp[t], z); scu[t] = mfma16k16(kp[t + 1], qp[t], z); }
  SCHED();
  dmo(std::integral_constant<int, 2>{});
#pragma unroll
  for (int t = 0; t < 5; ++t) {
    vp[t].x = cvt2((pk2(av[t][0], av[t][1]) - pk2(mv[t][0], mv[t][1]) * c2) * pk2(rv[t][0], rv[t][1]));
    vp[t].y = cvt2((pk2(av[t][2], av[t][3]) - pk2(mv[t][2], mv[t][3]) * c2) * pk2(rv[t][2], rv[t][3]));
  }
  SCHED();
  f32x2 biasp[2], biasc[2];
#pragma unroll
  for (int r = 0; r < 4; ++r) {
    const int dc = tk - 4 * g - r, dp = dc + 16;
    biasc[r >> 1][r & 1] = (dc >= 0 && dc <= 14 && !(dc & 1)) ? 0.f : -1e30f;
    biasp[r >> 1][r & 1] = (dp <= 14 && !(dp & 1)) ? 0.f : -1e30f;
  }
  f32x2 a[4][4];
  float mx[4], sm[4];
#pragma unroll
  for (int t = 0; t < 4; ++t) {
    const float sq = sc * r1[t + 1];
    const f32x2 s2v = {sq, sq};
    f32x2 bp0 = biasp[0], bp1 = biasp[1];
    if (t == 0 && p64 == 0) { bp0 = f32x2{-1e30f, -1e30f}; bp1 = bp0; }
    a[t][0] = pk2(sp[t][0], sp[t][1]) * s2v + bp0; a[t][1] = pk2(sp[t][2], sp[t][3]) * s2v + bp1;
    a[t][2] = pk2(scu[t][0], scu[t][1]) * s2v + biasc[0]; a[t][3] = pk2(scu[t][2], scu[t][3]) * s2v + biasc[1];
  }
#pragma unroll
  for (int t = 0; t < 4; ++t)
    mx[t] = fmaxf(fmaxf(__builtin_fmaxf(a[t][0].x, a[t][0].y), fmaxf(a[t][1].x, a[t][1].y)), fmaxf(fmaxf(a[t][2].x, a[t][2].y), fmaxf(a[t][3].x, a[t][3].y)));
  SCHED();
  auto xred2 = [&](float& A, float& B, auto op) {
    auto s1 = __builtin_amdgcn_permlane16_swap(__float_as_uint(A), __float_as_uint(B), false, false);
    const float u = op(__uint_as_float(s1[0]), __uint_as_float(s1[1]));
    auto s2_ = __builtin_amdgcn_permlane32_swap(__float_as_uint(u), __float_as_uint(u), false, false);
    const float v = op(__uint_as_float(s2_[0]), __uint_as_float(s2_[1]));
    auto s3 = __builtin_amdgcn_permlane16_swap(__float_as_uint(v), __float_as_uint(v), false, false);
    A = __uint_as_float(s3[0]); B = __uint_as_float(s3[1]);
  };
  auto fmx = [](float x, float y) { return fmaxf(x, y); };
  auto fad = [](float x, float y) { return x + y; };
  xred2(mx[0], mx[1], fmx); xred2(mx[2], mx[3], fmx);
  SCHED();
  dmo(std::integral_constant<int, 3>{});
#pragma unroll
  for (int t = 0; t < 4; ++t) {
    const f32x2 m2 = {mx[t], mx[t]};
#pragma unroll
    for (int q = 0; q < 4; ++q) a[t][q] -= m2;
  }
#pragma unroll
  for (int t = 0; t < 4; ++t)
#pragma unroll
    for (int q = 0; q < 4; ++q) { a[t][q].x = __builtin_amdgcn_exp2f(a[t][q].x); a[t][q].y = __builtin_amdgcn_exp2f(a[t][q].y); }
  SCHED();
  v4u pf[4];
#pragma unroll
  for (int t = 0; t < 4; ++t) {
    const f32x2 sv = (a[t][0] + a[t][1]) + (a[t][2] + a[t][3]);
    sm[t] = sv.x + sv.y;
    pf[t] = v4u{cvt2(a[t][0]), cvt2(a[t][1]), cvt2(a[t][2]), cvt2(a[t][3])};
  }
  f32x4 o[4];
#pragma unroll
  for (int t = 0; t < 4; ++t) {
    const v4u vf = {vp[t].x, vp[t].y, vp[t + 1].x, vp[t + 1].y};
    o[t] = mfma16(vf, pf[t], z);
  }
  SCHED();
  xred2(sm[0], sm[1], fad); xred2(sm[2], sm[3], fad);
  v2u ob[4];
#pragma unroll
  for (int t = 0; t < 4; ++t) {
    const float inv = __builtin_amdgcn_rcpf(sm[t]);
    const f32x2 i2 = {inv, inv};
    ob[t] = v2u{cvt2(pk2(o[t][0], o[t][1]) * i2), cvt2(pk2(o[t][2], o[t][3]) * i2)};
  }
  const int head = 4 * hg + w;
#pragma unroll
  for (int u = 0; u < 2; ++u) {
    auto sx = __builtin_amdgcn_permlane16_swap(ob[2 * u].x, ob[2 * u + 1].x, false, false);
    auto sy = __builtin_amdgcn_permlane16_swap(ob[2 * u].y, ob[2 * u + 1].y, false, false);
    const v4u o4 = {sx[0], sy[0], sx[1], sy[1]};
    const int voff = ((g >> 1) * 32 + (g & 1) * 16 + tk) * 16;
    __builtin_amdgcn_raw_buffer_store_b128(o4, rs, voff, (int)WS_O + ((2 * p64 + u) * 32 + head) * 1024, AUX_SC1);
  }
}

DEV void cs_load(const Params& p, int c, int r, int tid, v4u (&v)[4]) {
  const int rg = tid >> 5, c0 = tid & 31, cc = c0 < 28 ? c0 : 27;
  const u16* base = (cc < 14 ? p.wg : p.wu) + (size_t)(32 * r + 4 * rg) * 1792 + 112 * c + (cc < 14 ? cc : cc - 14) * 8;
#pragma unroll
  for (int i = 0; i < 4; ++i) v[i] = *(const GAS v4u*)(base + i * 1792);
}
DEV void cs_finish(LAS unsigned char* L, __amdgpu_buffer_rsrc_t rs, int c, int r, int w, int lane, const v4u (&v)[4]) {
  LAS float* buf = (LAS float*)(L + CS_BUF);
  const int c0 = lane & 31;
  f32x4 a, b;
#pragma unroll
  for (int e = 0; e < 2; ++e) {
    a[2 * e] = xsum32((bf_lo(v[0][e]) + bf_lo(v[1][e])) + (bf_lo(v[2][e]) + bf_lo(v[3][e])));
    a[2 * e + 1] = xsum32((bf_hi(v[0][e]) + bf_hi(v[1][e])) + (bf_hi(v[2][e]) + bf_hi(v[3][e])));
    b[2 * e] = xsum32((bf_lo(v[0][2 + e]) + bf_lo(v[1][2 + e])) + (bf_lo(v[2][2 + e]) + bf_lo(v[3][2 + e])));
    b[2 * e + 1] = xsum32((bf_hi(v[0][2 + e]) + bf_hi(v[1][2 + e])) + (bf_hi(v[2][2 + e]) + bf_hi(v[3][2 + e])));
  }
  if (lane < 28) {
    *(LAS f32x4*)(buf + w * 224 + c0 * 8) = a;
    *(LAS f32x4*)(buf + w * 224 + c0 * 8 + 4) = b;
  }
  BAR_LDS();
  const int tid = w * 64 + lane;
  if (tid < 224) {
    const float s = (buf[tid] + buf[224 + tid]) + (buf[448 + tid] + buf[672 + tid]);
    __builtin_amdgcn_raw_buffer_store_b32(__float_as_uint(s), rs, tid * 4, (int)WS_CS + (c * 16 + r) * 896, AUX_SC1);
  }
}

DEV void p3_voffs(int lane, unsigned (&vo)[7]) {
#pragma unroll
  for (int i = 0; i < 7; ++i) {
    const int q = i * 64 + lane, row = q / 14, cc = q - 14 * row;
    vo[i] = (unsigned)row * 3584u + (unsigned)cc * 16u + 4096u - (unsigned)((i - 3) * 1024);
  }
}
DEV void p3_w_setup(int ds, int w) { set_m0((unsigned)(p3_slot_addr(ds) + (w & 1) * P3_UP + (w >> 1) * 7168 + 3 * 1024)); }
template <int I> DEV void p3_issue_w1(const Params& p, int c, int ds, int w, const unsigned (&vo)[7]) {
  const unsigned char* base = (const unsigned char*)((w & 1) ? p.wu : p.wg) + (64 * ds + 32 * (w >> 1)) * 3584 + 224 * c - 4096;
  dma16i<(I - 3) * 1024>(base, vo[I]);
}
DEV void p3_issue_w(const Params& p, int c, int ds, int w, const unsigned (&vo)[7]) {
  p3_w_setup(ds, w);
  static_for<7>([&](auto I) { p3_issue_w1<I>(p, c, ds, w, vo); });
}

DEV void p2_issue_x(const Params& p, int p64, int nt, int w, int lane) {
  const unsigned vx = (unsigned)(lane >> 3) * 1024u + (unsigned)(((lane & 7) ^ (lane >> 3)) * 16);
  const u16* xb = p.x + (64 * p64 + 16 * w) * 512 + nt * 64;
  set_m0((unsigned)(P2_X + 16 * w * 128));
  dma16i<0>(xb, vx);
  dma16i<1024>(xb, vx + 7168u);
}

template <int WSTRIDE>
DEV f32x16 reduce4(f32x16 (&acc)[2][2], LAS unsigned char* red, int w, int lane) {
  WAIT_LGKM();
#pragma unroll
  for (int a = 0; a < 2; ++a)
#pragma unroll
    for (int b = 0; b < 2; ++b) {
      const int dest = a + 2 * b;
      if (dest != w) {
        const int slot = (dest - w - 1) & 3;
#pragma unroll
        for (int qd = 0; qd < 4; ++qd)
          *(LAS f32x4*)(red + w * WSTRIDE + (slot * 4 + qd) * 1024 + lane * 16) =
              f32x4{acc[a][b][4 * qd], acc[a][b][4 * qd + 1], acc[a][b][4 * qd + 2], acc[a][b][4 * qd + 3]};
      }
    }
  BAR_LDS();
  f32x16 r;
  if (w == 0) r = acc[0][0]; else if (w == 1) r = acc[1][0]; else if (w == 2) r = acc[0][1]; else r = acc[1][1];
#pragma unroll
  for (int s = 1; s < 4; ++s) {
    const int src = (w + s) & 3;
#pragma unroll
    for (int qd = 0; qd < 4; ++qd) {
      const f32x4 v = *(LAS f32x4*)(red + src * WSTRIDE + ((3 - s) * 4 + qd) * 1024 + lane * 16);
#pragma unroll
      for (int e = 0; e < 4; ++e) r[4 * qd + e] += v[e];
    }
  }
  return r;
}

DEV void phase2(const Params& p, LAS unsigned char* L, __amdgpu_buffer_rsrc_t rs, int p64, int nt, int w, int lane, v4u (&hkeep)[2],
                int c3, const unsigned (&vo)[7] TSARG) {
  v4u of[2][8];
#pragma unroll
  for (int i = 0; i < 8; ++i)
#pragma unroll
    for (int tt = 0; tt < 2; ++tt)
      of[tt][i] = __builtin_amdgcn_raw_buffer_load_b128(rs, lane * 16, (int)WS_O + ((2 * p64 + tt) * 32 + 8 * w + i) * 1024, AUX_SC1);
  SCHED();
  const int hh = lane >> 5, blk = (lane >> 4) & 1, q4 = (lane & 15) >> 2, pp = lane & 3;
  const int sw = ((q4 >> 1) & 1) << 2;
  LAS unsigned char* wb0 = L + P2_W + (128 * w + 8 * hh + q4) * 128 + (((2 * blk + (pp >> 1)) ^ sw) * 16) + 8 * (pp & 1);
  LAS unsigned char* wb1 = L + P2_W + (128 * w + 8 * hh + q4) * 128 + (((4 + 2 * blk + (pp >> 1)) ^ sw) * 16) + 8 * (pp & 1);
  f32x16 acc[2][2];
#pragma unroll
  for (int a = 0; a < 2; ++a)
#pragma unroll
    for (int b = 0; b < 2; ++b)
#pragma unroll
      for (int e = 0; e < 16; ++e) acc[a][b][e] = 0.f;
  v4u wa[8][2];
#pragma unroll
  for (int i = 0; i < 8; ++i) {
    wa[i][0] = tr_read2(wb0 + i * 2048, wb0 + i * 2048 + 512);
    wa[i][1] = tr_read2(wb1 + i * 2048, wb1 + i * 2048 + 512);
  }
  SCHED();
#pragma unroll
  for (int i = 0; i < 8; ++i) {
    acc[0][0] = mfma32(wa[i][0], of[0][i], acc[0][0]);
    acc[1][0] = mfma32(wa[i][1], of[0][i], acc[1][0]);
    acc[0][1] = mfma32(wa[i][0], of[1][i], acc[0][1]);
    acc[1][1] = mfma32(wa[i][1], of[1][i], acc[1][1]);
  }
  SCHED();
  STAMP(13);
  p3_issue_w(p, c3, 2, w, vo);
  asm volatile("s_waitcnt vmcnt(7)" ::: "memory");
  f32x16 r = reduce4<16384>(acc, L + P2_W, w, lane);
  const int row = 32 * (w >> 1) + (lane & 31);
  float hv[16];
  float s1 = 0.f, s2 = 0.f;
#pragma unroll
  for (int qd = 0; qd < 4; ++qd) {
    const v2u xr = *(LAS v2u*)(L + P2_X + row * 128 + (((4 * (w & 1) + qd) ^ (row & 7)) * 16) + 8 * hh);
    const unsigned y01 = pk_bf16(r[4 * qd + 0], r[4 * qd + 1]), y23 = pk_bf16(r[4 * qd + 2], r[4 * qd + 3]);
    const unsigned h01 = pk_bf16(bf_lo(xr.x) + bf_lo(y01), bf_hi(xr.x) + bf_hi(y01));
    const unsigned h23 = pk_bf16(bf_lo(xr.y) + bf_lo(y23), bf_hi(xr.y) + bf_hi(y23));
    hkeep[qd >> 1][2 * (qd & 1)] = h01; hkeep[qd >> 1][2 * (qd & 1) + 1] = h23;
    hv[4 * qd + 0] = bf_lo(h01); hv[4 * qd + 1] = bf_hi(h01); hv[4 * qd + 2] = bf_lo(h23); hv[4 * qd + 3] = bf_hi(h23);
  }
  const int tile32 = 2 * p64 + (w >> 1);
  const int nt32 = 2 * nt + (w & 1);
#pragma unroll
  for (int ks = 0; ks < 2; ++ks)
    __builtin_amdgcn_raw_buffer_store_b128(hkeep[ks], rs, lane * 16, (int)WS_H + (tile32 * 32 + 2 * nt32 + ks) * 1024, AUX_SC1);
#pragma unroll
  for (int i = 0; i < 16; ++i) { s1 += hv[i]; s2 += hv[i] * hv[i]; }
  s1 = xsum32(s1); s2 = xsum32(s2);
  if (hh == 0) {
    v2u st = {__float_as_uint(s1), __float_as_uint(s2)};
    __builtin_amdgcn_raw_buffer_store_b64(st, rs, (lane & 31) * 16, (int)WS_HS + (tile32 * 8 + (nt32 >> 1)) * 512 + (nt32 & 1) * 8, AUX_SC1);
  }
}

constexpr int P3_KM = 20;
constexpr int p3_nl(int ks) { return ks < 4 ? 2 : 1; }
constexpr int p3_nd(int ks) { return (ks >= 4 && ks < 16) ? ((ks & 3) == 3 ? 1 : 2) : 0; }
constexpr int p3_nx(int ks) { return ks == 7 ? 1 : (ks == 16 || ks == 17) ? 2 : 0; }
constexpr int p3_cum(int ks) { int c = 12; for (int k = 0; k < ks; ++k) c += p3_nl(k) + p3_nd(k) + p3_nx(k); return c; }
constexpr int p3_pos(int i) { return i < 8 ? 4 + i : i < 16 ? p3_cum((i - 8) / 2) + ((i - 8) & 1) : p3_cum(i - 12); }

DEV void phase3(const Params& p, LAS unsigned char* L, __amdgpu_buffer_rsrc_t rs, v4u rsv, GAS unsigned* ctl, unsigned tag, int p128, int c, int w, int lane,
                int nt4, const unsigned (&vo)[7] TSARG) {
  const int tile32 = 4 * p128 + w;
  const int hh = lane >> 5, blk = (lane >> 4) & 1, q4 = (lane & 15) >> 2, pp = lane & 3;
  v4u hb[32];
  v4u hs[4];
  const unsigned hbase = WS_H + tile32 * 32768;
  static_for<4>([&](auto I) { bload_v<I * 512>(hs[I], rsv, (4 * hh) * 512 + (lane & 31) * 16, WS_HS + tile32 * 4096); });
  static_for<8>([&](auto I) { bload_a<(I & 3) * 1024>(hb[I], rsv, lane * 16, hbase + (I >> 2) * 4096); });
  SCHED();
  const int abase = blk * P3_UP + (4 * hh + q4) * 224 + pp * 8;
  int ab0 = abase, ab1 = abase + 65536, ab2 = abase + 123136;
  PIN_V(ab0); PIN_V(ab1); PIN_V(ab2);
  auto slot_ptr = [&](int ds) -> LAS unsigned char* {
    const int a = p3_slot_addr(ds);
    return a == 123136 ? L + ab2 : a >= 65536 ? L + ab1 + (a - 65536) : L + ab0 + a;
  };
  f32x16 acc[7];
#pragma unroll
  for (int t = 0; t < 7; ++t)
#pragma unroll
    for (int e = 0; e < 16; ++e) acc[t][e] = 0.f;
  WAIT_VM_BAR(12);
  STAMP(14);
  ts[40] = __builtin_amdgcn_s_memtime(); SCHED();
  v4u an[7];
  {
    LAS unsigned char* sl = slot_ptr(0);
#pragma unroll
    for (int t = 0; t < 7; ++t) an[t] = tr_read2(sl + t * 32, sl + t * 32 + 8 * 224);
  }
  SCHED();
  unsigned csflag = 0;
  v4u csl[4];
  static_for<P3_KM>([&](auto KS) {
    constexpr int ks = KS;
    v4u ac[7];
#pragma unroll
    for (int t = 0; t < 7; ++t) ac[t] = an[t];
    wait_vm_a<p3_cum(ks) - 1 - p3_pos(ks)>(hb[ks]);
    constexpr int ds = (ks + 1) >> 2, ksl = (ks + 1) & 3;
    LAS unsigned char* sl = slot_ptr(ds) + ksl * 16 * 224;
    auto vm = [&](int slot) {
      constexpr int nl = p3_nl(ks), nd = p3_nd(ks), nx = p3_nx(ks);
      if constexpr (true) {
        if (slot < nl) {
          if constexpr (ks < 4) {
            if (slot == 0) bload_a<((8 + 2 * ks) & 3) * 1024>(hb[8 + 2 * ks], rsv, lane * 16, hbase + ((8 + 2 * ks) >> 2) * 4096);
            else bload_a<((9 + 2 * ks) & 3) * 1024>(hb[9 + 2 * ks], rsv, lane * 16, hbase + ((9 + 2 * ks) >> 2) * 4096);
          } else bload_a<((12 + ks) & 3) * 1024>(hb[(12 + ks) & 31], rsv, lane * 16, hbase + ((12 + ks) >> 2) * 4096);
        } else if (slot < nl + nd) {
          if constexpr (nd > 0) {
            if constexpr ((ks & 3) == 0) { if (slot == nl) p3_w_setup(5 + ((ks - 4) >> 2), w); }
            if (slot == nl) p3_issue_w1<2 * (ks & 3)>(p, c, 5 + ((ks - 4) >> 2), w, vo);
            else p3_issue_w1<(2 * (ks & 3) + 1) % 7>(p, c, 5 + ((ks - 4) >> 2), w, vo);
          }
        } else if (slot < nl + nd + nx) {
          if constexpr (ks == 7) bload1_v(csflag, rsv, (unsigned)(FL_F + ((16 + (lane & 15)) * 16 + c) * 32) * 4u, 0u);
          if constexpr (ks == 16 || ks == 17) {
            const int i = slot - nl - nd;
            const unsigned vof = (unsigned)(lane < 56 ? lane : 55) * 16u;
            const unsigned so = WS_CS + (unsigned)(c * 16 + 4 * w) * 896u;
            if constexpr (ks == 16) { if (i == 0) bload_v<0>(csl[0], rsv, vof, so); else bload_v<896>(csl[1], rsv, vof, so); }
            else { if (i == 0) bload_v<1792>(csl[2], rsv, vof, so); else bload_v<2688>(csl[3], rsv, vof, so); }
          }
        }
      }
    };
    if constexpr (ks == 12) {
      wait_vm_v1<p3_cum(12) - 1 - (p3_cum(7) + p3_nl(7) + p3_nd(7))>(csflag);
      if (!__all(csflag == tag)) wait_flags(flag_ptr(ctl, 1, lane & 15, c), tag);
    }
    static_for<7>([&](auto T) {
      constexpr int t = T;
      if constexpr (t == 0 || t == 2) asm volatile("" : "+v"(ac[t]), "+v"(ac[t + 1]));
      if constexpr (t == 4) asm volatile("" : "+v"(ac[4]), "+v"(ac[5]), "+v"(ac[6]));
      acc[t] = mfma32(ac[t], hb[ks], acc[t]);
      SCHED();
      if constexpr (ks + 1 < P3_KM) {
        if constexpr (t == 1 || t == 3 || t == 5) {
          an[t - 1] = tr_read2(sl + (t - 1) * 32, sl + (t - 1) * 32 + 8 * 224);
          an[t] = tr_read2(sl + t * 32, sl + t * 32 + 8 * 224);
        }
        if constexpr (t == 6) an[6] = tr_read2(sl + 6 * 32, sl + 6 * 32 + 8 * 224);
      }
      if constexpr (t == 0 || t == 2 || t == 4) vm(t >> 1);
      SCHED();
    });
    if constexpr ((ks & 3) == 3 && (ks >> 2) < 3) BAR_LDS();
  });
  STAMP(15);
  ts[41] = __builtin_amdgcn_s_memtime(); SCHED();
  asm volatile("s_waitcnt vmcnt(0)" : "+v"(hs[0]), "+v"(hs[1]), "+v"(hs[2]), "+v"(hs[3]), "+v"(csl[0]), "+v"(csl[1]), "+v"(csl[2]), "+v"(csl[3]) :: "memory");
#pragma unroll
  for (int k = P3_KM; k < 32; ++k) PIN_A(hb[k]);
  LAS float* cspart = (LAS float*)(L + P3_CSP);
  int cspo = P3_CSP + 896 * 4 + 16 * hh; PIN_V(cspo);
  LAS float* csp = cspart + 896;
  LAS float* csq_ = (LAS float*)(L + cspo);
  {
    f32x4 a;
#pragma unroll
    for (int e = 0; e < 4; ++e)
      a[e] = (__uint_as_float(csl[0][e]) + __uint_as_float(csl[1][e])) + (__uint_as_float(csl[2][e]) + __uint_as_float(csl[3][e]));
    if (lane < 56) *(LAS f32x4*)(cspart + w * 224 + lane * 4) = a;
  }
  float s1 = 0.f, s2 = 0.f;
#pragma unroll
  for (int i = 0; i < 4; ++i) {
    s1 += __uint_as_float(hs[i].x) + __uint_as_float(hs[i].z);
    s2 += __uint_as_float(hs[i].y) + __uint_as_float(hs[i].w);
  }
  s1 = xsum32(s1); s2 = xsum32(s2);
  const float mean = s1 * (1.f / 512.f);
  const float rstd = __builtin_amdgcn_rsqf(fmaxf(s2 * (1.f / 512.f) - mean * mean, 0.f) + 1e-5f);
  BAR_LDS();
  auto rdt = [&](int t, v4u (&f)[12]) {
#pragma unroll
    for (int k = 0; k < 12; ++k) {
      const int ks = P3_KM + k, ds = ks >> 2, ksl = ks & 3;
      LAS unsigned char* sl = slot_ptr(ds) + ksl * 16 * 224 + t * 32;
      f[k] = tr_read2(sl, sl + 8 * 224);
    }
  };
  v4u fn[12];
  rdt(0, fn);
  if (lane < 56) {
    const f32x4 a = (*(LAS f32x4*)(cspart + lane * 4) + *(LAS f32x4*)(cspart + 224 + lane * 4)) +
                    (*(LAS f32x4*)(cspart + 448 + lane * 4) + *(LAS f32x4*)(cspart + 672 + lane * 4));
    *(LAS f32x4*)(csp + lane * 4) = a;
  }
  WAIT_LGKM();
  const f32x2 nm2 = {-mean, -mean};
  const float r2s = rstd * rstd;
  const f32x2 k1 = {-2.302208198f * rstd, -2.302208198f * rstd}, k2 = {-0.1029432397f * rstd * r2s, -0.1029432397f * rstd * r2s};
  const f32x2 rr2 = {r2s, r2s}, one2 = {1.f, 1.f};
  f32x2 yg[4], yu[4], zz[4], rc[4];
  f32x4 cgu[4];
  unsigned gw[4];
  auto epi_piece = [&](auto T, auto K) {
    constexpr int t = T, k = K;
    auto csg = [&](int i) { return pk2(cgu[i >> 1][2 * (i & 1)], cgu[i >> 1][2 * (i & 1) + 1]); };
    auto csu = [&](int i) { return pk2(cgu[2 + (i >> 1)][2 * (i & 1)], cgu[2 + (i >> 1)][2 * (i & 1) + 1]); };
    auto ex2 = [&](int i) { zz[i].x = __builtin_amdgcn_exp2f(zz[i].x); zz[i].y = __builtin_amdgcn_exp2f(zz[i].y); };
    auto rc2 = [&](int i) { rc[i].x = __builtin_amdgcn_rcpf(zz[i].x); rc[i].y = __builtin_amdgcn_rcpf(zz[i].y); };
    if constexpr (k == 0) {
      cgu[0] = *(LAS f32x4*)(csq_ + t * 16);
      cgu[1] = *(LAS f32x4*)(csq_ + t * 16 + 8);
      cgu[2] = *(LAS f32x4*)(csq_ + 112 + t * 16);
      cgu[3] = *(LAS f32x4*)(csq_ + 112 + t * 16 + 8);
    } else if constexpr (k == 1) {
      PIN_A(acc[t]);
#pragma unroll
      for (int i = 0; i < 4; ++i) { yg[i] = pk2(acc[t][2 * i], acc[t][2 * i + 1]); PIN_V(yg[i]); }
    } else if constexpr (k == 2) {
#pragma unroll
      for (int i = 0; i < 4; ++i) { yu[i] = pk2(acc[t][8 + 2 * i], acc[t][8 + 2 * i + 1]); PIN_V(yu[i]); }
    } else if constexpr (k == 3) {
#pragma unroll
      for (int i = 0; i < 4; ++i) yg[i] = nm2 * csg(i) + yg[i];
      yu[0] = nm2 * csu(0) + yu[0]; yu[1] = nm2 * csu(1) + yu[1];
    } else if constexpr (k == 4) {
#pragma unroll
      for (int i = 0; i < 4; ++i) zz[i] = yg[i] * yg[i];
      yu[2] = nm2 * csu(2) + yu[2]; yu[3] = nm2 * csu(3) + yu[3];
    } else if constexpr (k == 5) {
#pragma unroll
      for (int i = 0; i < 4; ++i) zz[i] = zz[i] * k2 + k1;
      yu[0] = yu[0] * yg[0]; yu[1] = yu[1] * yg[1];
    } else if constexpr (k == 6) {
#pragma unroll
      for (int i = 0; i < 4; ++i) zz[i] = zz[i] * yg[i];
      yu[2] = yu[2] * yg[2]; yu[3] = yu[3] * yg[3];
    } else if constexpr (k == 7) {
      ex2(0); ex2(1);
      yu[0] = yu[0] * rr2; yu[1] = yu[1] * rr2;
    } else if constexpr (k == 8) {
      ex2(2); ex2(3);
      zz[0] += one2; zz[1] += one2;
    } else if constexpr (k == 9) {
      rc2(0); rc2(1);
      zz[2] += one2; zz[3] += one2;
    } else if constexpr (k == 10) {
      rc2(2); rc2(3);
      yu[2] = yu[2] * rr2; yu[3] = yu[3] * rr2;
      yu[0] = yu[0] * rc[0]; yu[1] = yu[1] * rc[1];
    } else {
      yu[2] = yu[2] * rc[2]; yu[3] = yu[3] * rc[3];
#pragma unroll
      for (int i = 0; i < 4; ++i) gw[i] = cvt2(yu[i]);
      const v4u f = {gw[0], gw[1], gw[2], gw[3]};
      __builtin_amdgcn_raw_buffer_store_b128(f, rs, lane * 16, (int)WS_G + (tile32 * 112 + 7 * c + t) * 1024, AUX_SC1);
    }
  };
  unsigned wv4[4];
  wrow_voffs(lane, wv4);
  const u16* wdb = p.wd + (448 * w) * 512 + nt4 * 64;
  SCHED();
  STAMP(18);
  ts[42] = __builtin_amdgcn_s_memtime(); SCHED();
  static_for<7>([&](auto T) {
    constexpr int t = T;
    v4u fc[12];
#pragma unroll
    for (int k = 0; k < 12; ++k) fc[k] = fn[k];
    static_for<12>([&](auto K) {
      constexpr int k = K;
      acc[t] = mfma32(fc[k], hb[P3_KM + k], acc[t]);
      SCHED();
      if constexpr (t < 6) {
        constexpr int ks = P3_KM + k, ds = ks >> 2, ksl = ks & 3;
        LAS unsigned char* sl = slot_ptr(ds) + ksl * 16 * 224 + (t + 1) * 32;
        fn[k] = tr_read2(sl, sl + 8 * 224);
      }
      if constexpr (t > 0) epi_piece(std::integral_constant<int, t - 1>{}, K);
      if constexpr (t >= 1 && t < 5 && (k & 3) == 0) {
        constexpr int gq = t - 1;
        if constexpr (k == 0) set_m0((unsigned)(w * P4_EARLY + gq * 4096));
        wrow_issue<(k >> 2)>(wdb + gq * 32 * 512, wv4);
        if constexpr (k == 8) wrow_issue<3>(wdb + gq * 32 * 512, wv4);
      }
      SCHED();
    });
  });
  STAMP(19);
  ts[43] = __builtin_amdgcn_s_memtime(); SCHED();
  asm volatile("s_nop 15\n\ts_nop 7" ::: "memory");
  SCHED();
  static_for<12>([&](auto K) { epi_piece(std::integral_constant<int, 6>{}, K); });
}

constexpr int P4_UP = 10;
DEV void phase4(const Params& p, LAS unsigned char* L, v4u rsv, int p64, int nt, int w, int lane,
                const v4u (&hkeep)[2], const v4u (&wr)[8][2] TSARG) {
  const int hh = lane >> 5, blk = (lane >> 4) & 1, q4 = (lane & 15) >> 2, pp = lane & 3;
  const int sw = ((q4 >> 1) & 1) << 2;
  LAS unsigned char* wb0 = L + w * P4_WAVE + (4 * hh + q4) * 128 + (((2 * blk + (pp >> 1)) ^ sw) * 16) + 8 * (pp & 1);
  LAS unsigned char* wb1 = L + w * P4_WAVE + (4 * hh + q4) * 128 + (((4 + 2 * blk + (pp >> 1)) ^ sw) * 16) + 8 * (pp & 1);
  f32x16 acc[2][2];
#pragma unroll
  for (int a = 0; a < 2; ++a)
#pragma unroll
    for (int b = 0; b < 2; ++b)
#pragma unroll
      for (int e = 0; e < 16; ++e) acc[a][b][e] = 0.f;
  v4u gf[2][28];
  const unsigned gb0 = WS_G + ((2 * p64) * 112 + 28 * w) * 1024, gb1 = gb0 + 112 * 1024;
  auto ldg = [&](auto I) {
    constexpr int i = I;
    if constexpr (i < 24) {
      bload_a<(i & 3) * 1024>(gf[0][i], rsv, lane * 16, gb0 + (i >> 2) * 4096);
      bload_a<(i & 3) * 1024>(gf[1][i], rsv, lane * 16, gb1 + (i >> 2) * 4096);
    } else {
      bload_v<(i & 3) * 1024>(gf[0][i], rsv, lane * 16, gb0 + (i >> 2) * 4096);
      bload_v<(i & 3) * 1024>(gf[1][i], rsv, lane * 16, gb1 + (i >> 2) * 4096);
    }
  };
  static_for<P4_UP>(ldg);
  SCHED();
  v4u an0 = wr[0][0], an1 = wr[0][1];
  static_for<28>([&](auto I) {
    constexpr int i = I;
    constexpr int cum = (2 * (P4_UP + i) < 56) ? 2 * (P4_UP + i) : 56;
    constexpr int N = cum - 2 * i - 2;
    if constexpr (i < 24) wait_vm_a2<N>(gf[0][i], gf[1][i]); else wait_vm_v2<N>(gf[0][i], gf[1][i]);
    const v4u a0 = an0, a1 = an1;
    if constexpr (i + 1 < 8) { an0 = wr[i + 1][0]; an1 = wr[i + 1][1]; }
    else if constexpr (i + 1 < 28) {
      constexpr int sl = i + 1 - 8;
      an0 = tr_read2(wb0 + sl * 2048, wb0 + sl * 2048 + 8 * 128);
      an1 = tr_read2(wb1 + sl * 2048, wb1 + sl * 2048 + 8 * 128);
    }
    acc[0][0] = mfma32(a0, gf[0][i], acc[0][0]);
    acc[1][0] = mfma32(a1, gf[0][i], acc[1][0]);
    SCHED();
    if constexpr (i + P4_UP < 28) ldg(std::integral_constant<int, i + P4_UP>{});
    SCHED();
    acc[0][1] = mfma32(a0, gf[1][i], acc[0][1]);
    acc[1][1] = mfma32(a1, gf[1][i], acc[1][1]);
    SCHED();
  });
  STAMP(16);
  f32x16 r = reduce4<P4_WAVE>(acc, L, w, lane);
  const int tok = 64 * p64 + 32 * (w >> 1) + (lane & 31);
  const int n0 = 64 * nt + 32 * (w & 1) + 4 * hh;
#pragma unroll
  for (int qd = 0; qd < 4; ++qd) {
    const unsigned h01 = hkeep[qd >> 1][2 * (qd & 1)], h23 = hkeep[qd >> 1][2 * (qd & 1) + 1];
    const unsigned y01 = pk_bf16(r[4 * qd + 0], r[4 * qd + 1]), y23 = pk_bf16(r[4 * qd + 2], r[4 * qd + 3]);
    v2u o = {pk_bf16(bf_lo(h01) + bf_lo(y01), bf_hi(h01) + bf_hi(y01)), pk_bf16(bf_lo(h23) + bf_lo(y23), bf_hi(h23) + bf_hi(y23))};
    *(GAS v2u*)(p.out + tok * 512 + n0 + 8 * qd) = o;
  }
}
DEV void p4_read_wr(LAS unsigned char* L, int w, int lane, v4u (&wr)[8][2]) {
  const int hh = lane >> 5, blk = (lane >> 4) & 1, q4 = (lane & 15) >> 2, pp = lane & 3;
  const int sw = ((q4 >> 1) & 1) << 2;
  LAS unsigned char* wb0 = L + w * P4_EARLY + (4 * hh + q4) * 128 + (((2 * blk + (pp >> 1)) ^ sw) * 16) + 8 * (pp & 1);
  LAS unsigned char* wb1 = L + w * P4_EARLY + (4 * hh + q4) * 128 + (((4 + 2 * blk + (pp >> 1)) ^ sw) * 16) + 8 * (pp & 1);
#pragma unroll
  for (int i = 0; i < 8; ++i) {
    wr[i][0] = tr_read2(wb0 + i * 2048, wb0 + i * 2048 + 8 * 128);
    wr[i][1] = tr_read2(wb1 + i * 2048, wb1 + i * 2048 + 8 * 128);
  }
}

__global__ void __launch_bounds__(256, 1) mk_kernel(Params p) {
  extern __shared__ __attribute__((aligned(16))) unsigned char smem[];
  LAS unsigned char* L = (LAS unsigned char*)smem;
  const int tid = threadIdx.x, lane = tid & 63, w = __builtin_amdgcn_readfirstlane(tid >> 6);
  const int b = blockIdx.x;
  const int dom = (b & 7) * 2 + (b >> 7), j = (b >> 3) & 15;
  const int p64 = 2 * dom + (j >> 3), j8 = j & 7;
  const __amdgpu_buffer_rsrc_t rs = __builtin_amdgcn_make_buffer_rsrc((void*)p.ws, 0, (int)WS_BYTES, 0x00020000);
  const v4u rsv = {(unsigned)(size_t)p.ws, (unsigned)((size_t)p.ws >> 32), WS_BYTES, 0x00020000u};
  GAS unsigned* ctl = (GAS unsigned*)(p.ws + WS_FLAGS);
  unsigned long long ts[48];
  for (int k = 0; k < 48; ++k) ts[k] = 0;
  const unsigned long long cyc0 = __builtin_amdgcn_s_memtime();
  STAMP(0);
  unsigned wvo[6], vx[5];
  {
    const int tok0 = 64 * p64 - 16, s0 = (p64 & 3) * 2;
    p1_xvoff(tok0, w, lane, vx);
    p1_wvoff(w, lane, wvo);
    p1_x_setup(s0, w);
    static_for<5>([&](auto I) { p1_issue_x1<I>(p, s0, w, lane, vx); });
    p1_w_setup(0, w);
    static_for<6>([&](auto I) { p1_issue_w1<I>(p, j8, s0, wvo); });
    p1_w_setup(1, w);
    static_for<6>([&](auto I) { p1_issue_w1<I>(p, j8, s0 + 1, wvo); });
    if (w == 0) *(LAS v4u*)(L + P1_X + (lane >> 3) * 10240 + (lane & 7) * 16) = v4u{0x3f803f80u, 0x3f803f80u, 0x3f803f80u, 0x3f803f80u};
  }
  STAMP(1);
  phase1(p, L, rs, p64, j8, w, lane, wvo, vx TSPASS);
  asm volatile("" ::: "memory"); SCHED();
  STAMP(2);
  const unsigned tag = __hip_atomic_load(ctl + FL_EPOCH, __ATOMIC_RELAXED, __HIP_MEMORY_SCOPE_AGENT) + 1u;
  WAIT_VM_BAR(0);
  if (tid == 0) __hip_atomic_store(flag_ptr(ctl, 0, dom, j), tag, __ATOMIC_RELAXED, __HIP_MEMORY_SCOPE_AGENT);
  STAMP(3);
  v4u csv4[4];
  cs_load(p, j, dom, tid, csv4);
  SCHED();
  unsigned vo[7];
  p3_voffs(lane, vo);
  p2_issue_x(p, p64, j8, w, lane);
#pragma unroll
  for (int ds = 0; ds < 2; ++ds) p3_issue_w(p, j, ds, w, vo);
  WAIT_VM(16);
  cs_finish(L, rs, j, dom, w, lane, csv4);
  wait_flags(flag_ptr(ctl, 0, dom, (j & 8) + (lane & 7)), tag);
  STAMP(4);
  v4u hkeep[2];
  phase2(p, L, rs, p64, j8, w, lane, hkeep, j, vo TSPASS);
  STAMP(5);
  WAIT_VM_BAR(0);
  if (tid == 0) __hip_atomic_store(flag_ptr(ctl, 1, dom, j), tag, __ATOMIC_RELAXED, __HIP_MEMORY_SCOPE_AGENT);
  STAMP(6);
#pragma unroll
  for (int ds = 3; ds < 5; ++ds) p3_issue_w(p, j, ds, w, vo);
  wait_flags(flag_ptr(ctl, 1, dom, lane & 15), tag);
  STAMP(7);
  phase3(p, L, rs, rsv, ctl, tag, dom, j, w, lane, j8, vo TSPASS);
  STAMP(8);
  v4u wr[8][2];
  WAIT_VM(0);
  p4_read_wr(L, w, lane, wr);
  BAR_LDS();
  if (tid == 0) __hip_atomic_store(flag_ptr(ctl, 2, dom, j), tag, __ATOMIC_RELAXED, __HIP_MEMORY_SCOPE_AGENT);
  STAMP(9);
  {
    unsigned wv4[4];
    wrow_voffs(lane, wv4);
    const u16* wdb = p.wd + (448 * w + 128) * 512 + j8 * 64;
    static_for<10>([&](auto G) {
      constexpr int gq = G;
      set_m0((unsigned)(w * P4_WAVE + gq * 4096));
      static_for<4>([&](auto I) { wrow_issue<I>(wdb + gq * 32 * 512, wv4); });
    });
  }
  wait_flags(flag_ptr(ctl, 2, dom, lane & 15), tag);
  STAMP(10);
  if (b == 0 && tid == 0) __hip_atomic_store(ctl + FL_EPOCH, tag, __ATOMIC_RELAXED, __HIP_MEMORY_SCOPE_AGENT);
  phase4(p, L, rsv, p64, j8, w, lane, hkeep, wr TSPASS);
  STAMP(17);
  if (lane == 0) {
    GAS unsigned long long* o = (GAS unsigned long long*)(p.ws + WS_STAMP + (blockIdx.x * 4 + w) * 48 * 8);
    for (int k = 0; k < 47; ++k) o[k] = ts[k];
    o[47] = __builtin_amdgcn_s_memtime() - cyc0;
    if (w == 0 && (b == 0 || b == 100 || b == 255)) {
      printf("WG %d: start %llu dur %.2f us cycles %llu | P1loop %.2f P1epi %.2f e1 %.2f P2 %.2f e2 %.2f P3 %.2f e3 %.2f P4 %.2f | first data %.2f\n", b, ts[0], (ts[17] - ts[0]) * 0.01, o[47],
             (ts[12] - ts[11]) * 0.01, (ts[2] - ts[12]) * 0.01, (ts[4] - ts[2]) * 0.01, (ts[5] - ts[4]) * 0.01, (ts[7] - ts[5]) * 0.01, (ts[8] - ts[7]) * 0.01,
             (ts[10] - ts[8]) * 0.01, (ts[17] - ts[10]) * 0.01, (ts[11] - ts[0]) * 0.01);
    }
  }
}

extern "C" void kernel_launch(void* const* d_in, const int* in_sizes, int n_in, void* d_out, int out_size,
                              void* d_ws, size_t ws_size, hipStream_t stream) {
  Params p;
  p.x = (const u16*)d_in[0];
  p.wqkv = (const u16*)d_in[1];
  p.wo = (const u16*)d_in[2];
  p.wg = (const u16*)d_in[4];
  p.wu = (const u16*)d_in[5];
  p.wd = (const u16*)d_in[6];
  p.out = (u16*)d_out;
  p.ws = (unsigned char*)d_ws;
  (void)hipFuncSetAttribute((const void*)mk_kernel, hipFuncAttributeMaxDynamicSharedMemorySize, LDS_BYTES);
  mk_kernel<<<256, 256, LDS_BYTES, stream>>>(p);
}

__global__ void __launch_bounds__(256, 1) mk_p3only(Params p) {
  extern __shared__ __attribute__((aligned(16))) unsigned char smem[];
  LAS unsigned char* L = (LAS unsigned char*)smem;
  const int tid = threadIdx.x, lane = tid & 63, w = __builtin_amdgcn_readfirstlane(tid >> 6);
  const int b = blockIdx.x;
  const int dom = (b & 7) * 2 + (b >> 7), j = (b >> 3) & 15;
  const __amdgpu_buffer_rsrc_t rs = __builtin_amdgcn_make_buffer_rsrc((void*)p.ws, 0, (int)WS_BYTES, 0x00020000);
  const v4u rsv = {(unsigned)(size_t)p.ws, (unsigned)((size_t)p.ws >> 32), WS_BYTES, 0x00020000u};
  GAS unsigned* ctl = (GAS unsigned*)(p.ws + WS_FLAGS);
  unsigned long long ts[48];
  for (int k = 0; k < 48; ++k) ts[k] = 0;
  const unsigned long long cyc0 = __builtin_amdgcn_s_memtime();
  STAMP(0);
  const unsigned tag = __hip_atomic_load(ctl + FL_EPOCH, __ATOMIC_RELAXED, __HIP_MEMORY_SCOPE_AGENT);
  unsigned vo[7];
  p3_voffs(lane, vo);
#pragma unroll
  for (int ds = 0; ds < 5; ++ds) p3_issue_w(p, j, ds, w, vo);
  WAIT_VM(0);
  STAMP(7);
  phase3(p, L, rs, rsv, ctl, tag, dom, j, w, lane, j & 7, vo, ts);
  STAMP(17);
  if (lane == 0) {
    GAS unsigned long long* o = (GAS unsigned long long*)(p.ws + WS_STAMP + (blockIdx.x * 4 + w) * 48 * 8);
    for (int k = 0; k < 47; ++k) o[k] = ts[k];
    o[47] = __builtin_amdgcn_s_memtime() - cyc0;
  }
}
extern "C" void launch_p3only(void* const* d_in, void* d_out, void* d_ws, hipStream_t stream) {
  Params p;
  p.x = (const u16*)d_in[0]; p.wqkv = (const u16*)d_in[1]; p.wo = (const u16*)d_in[2]; p.wg = (const u16*)d_in[4];
  p.wu = (const u16*)d_in[5]; p.wd = (const u16*)d_in[6]; p.out = (u16*)d_out; p.ws = (unsigned char*)d_ws;
  (void)hipFuncSetAttribute((const void*)mk_p3only, hipFuncAttributeMaxDynamicSharedMemorySize, LDS_BYTES);
  mk_p3only<<<256, 256, LDS_BYTES, stream>>>(p);
}
```

```cpp
#include <hip/hip_runtime.h>
#include <stdint.h>
#include <utility>

#define MK_STAMP 0

typedef unsigned short u16;
typedef unsigned v4u __attribute__((ext_vector_type(4)));
typedef unsigned v2u __attribute__((ext_vector_type(2)));
typedef short v8s __attribute__((ext_vector_type(8)));
typedef short v4s __attribute__((ext_vector_type(4)));
typedef float f32x2 __attribute__((ext_vector_type(2)));
typedef float f32x4 __attribute__((ext_vector_type(4)));
typedef float f32x16 __attribute__((ext_vector_type(16)));
typedef __bf16 bf16x2 __attribute__((ext_vector_type(2)));
#define GAS __attribute__((address_space(1)))
#define LAS __attribute__((address_space(3)))
#define DEV __device__ __forceinline__

constexpr unsigned WS_FLAGS = 0;
constexpr unsigned WS_O = 131072;
constexpr unsigned WS_H = WS_O + 2097152;
constexpr unsigned WS_HS = WS_H + 2097152;
constexpr unsigned WS_G = WS_HS + 262144;
constexpr unsigned WS_CS = WS_G + 64 * 112 * 1024;
constexpr unsigned WS_STAMP = WS_CS + 16 * 16 * 224 * 4;
constexpr unsigned WS_BYTES = WS_STAMP + 256 * 4 * 48 * 8;
constexpr int FL_EPOCH = 0, FL_F = 1024;

constexpr int LDS_BYTES = 163840;
constexpr int AUX_SC1 = 16;

struct Params {
  const u16* x; const u16* wqkv; const u16* wo; const u16* wg; const u16* wu; const u16* wd;
  u16* out; unsigned char* ws;
};

template <class F, int... I> DEV void static_for_impl(F&& f, std::integer_sequence<int, I...>) { (f(std::integral_constant<int, I>{}), ...); }
template <int N, class F> DEV void static_for(F&& f) { static_for_impl(f, std::make_integer_sequence<int, N>{}); }

DEV float bf_lo(unsigned u) { return __uint_as_float(u << 16); }
DEV float bf_hi(unsigned u) { return __uint_as_float(u & 0xffff0000u); }
DEV unsigned pk_bf16(float a, float b) {
  bf16x2 p; p[0] = (__bf16)a; p[1] = (__bf16)b;
  return __builtin_bit_cast(unsigned, p);
}
DEV f32x2 pk2(float a, float b) { return f32x2{a, b}; }
DEV unsigned cvt2(f32x2 v) { return pk_bf16(v.x, v.y); }
DEV float dot2(unsigned a, unsigned b, float c) {
  return __builtin_amdgcn_fdot2_f32_bf16(__builtin_bit_cast(bf16x2, a), __builtin_bit_cast(bf16x2, b), c, false);
}
DEV f32x4 mfma16(v4u a, v4u b, f32x4 c) {
  return __builtin_amdgcn_mfma_f32_16x16x32_bf16(__builtin_bit_cast(v8s, a), __builtin_bit_cast(v8s, b), c, 0, 0, 0);
}
DEV f32x4 mfma16k16(v2u a, v2u b, f32x4 c) {
  return __builtin_amdgcn_mfma_f32_16x16x16bf16_1k(__builtin_bit_cast(v4s, a), __builtin_bit_cast(v4s, b), c, 0, 0, 0);
}
DEV f32x16 mfma32(v4u a, v4u b, f32x16 c) {
  return __builtin_amdgcn_mfma_f32_32x32x16_bf16(__builtin_bit_cast(v8s, a), __builtin_bit_cast(v8s, b), c, 0, 0, 0);
}
DEV v2u tr_read(LAS unsigned char* p) {
  return __builtin_bit_cast(v2u, __builtin_amdgcn_ds_read_tr16_b64_v4i16((LAS v4s*)p));
}
DEV v4u tr_read2(LAS unsigned char* p0, LAS unsigned char* p1) {
  v2u a = tr_read(p0), b = tr_read(p1);
  return v4u{a.x, a.y, b.x, b.y};
}
DEV void dma16(const void* sbase, unsigned voff, unsigned lds_dst) {
  lds_dst = __builtin_amdgcn_readfirstlane(lds_dst);
  asm volatile("s_mov_b32 m0, %2\n\ts_nop 0\n\tglobal_load_lds_dwordx4 %0, %1"
               :: "v"(voff), "s"(sbase), "s"(lds_dst) : "memory");
}
DEV void set_m0(unsigned v) {
  v = __builtin_amdgcn_readfirstlane(v);
  asm volatile("s_mov_b32 m0, %0\n\ts_nop 0" :: "s"(v) : "memory");
}
template <int IMM> DEV void dma16i(const void* sbase, unsigned voff) {
  asm volatile("global_load_lds_dwordx4 %0, %1 offset:%c2" :: "v"(voff), "s"(sbase), "i"(IMM) : "memory");
}
template <int IMM> DEV void bload_a(v4u& dst, v4u rsrc, unsigned voff, unsigned soff) {
  asm volatile("buffer_load_dwordx4 %0, %1, %2, %3 offen offset:%c4 sc1" : "=a"(dst) : "v"(voff), "s"(rsrc), "s"(soff), "i"(IMM) : "memory");
}
template <int IMM> DEV void bload_v(v4u& dst, v4u rsrc, unsigned voff, unsigned soff) {
  asm volatile("buffer_load_dwordx4 %0, %1, %2, %3 offen offset:%c4 sc1" : "=v"(dst) : "v"(voff), "s"(rsrc), "s"(soff), "i"(IMM) : "memory");
}
DEV void bload1_v(unsigned& dst, v4u rsrc, unsigned voff, unsigned soff) {
  asm volatile("buffer_load_dword %0, %1, %2, %3 offen sc1" : "=v"(dst) : "v"(voff), "s"(rsrc), "s"(soff) : "memory");
}
template <int N> DEV void wait_vm_a(v4u& x) { asm volatile("s_waitcnt vmcnt(%c1)" : "+a"(x) : "i"(N) : "memory"); }
template <int N> DEV void wait_vm_a2(v4u& x, v4u& y) { asm volatile("s_waitcnt vmcnt(%c2)" : "+a"(x), "+a"(y) : "i"(N) : "memory"); }
template <int N> DEV void wait_vm_v2(v4u& x, v4u& y) { asm volatile("s_waitcnt vmcnt(%c2)" : "+v"(x), "+v"(y) : "i"(N) : "memory"); }
template <int N> DEV void wait_vm_v1(unsigned& x) { asm volatile("s_waitcnt vmcnt(%c1)" : "+v"(x) : "i"(N) : "memory"); }
#define PIN_V(x) asm volatile("" : "+v"(x))
#define PIN_A(x) asm volatile("" : "+a"(x))
#define WAIT_VM(n) asm volatile("s_waitcnt vmcnt(" #n ")" ::: "memory")
#define WAIT_VM_BAR(n) asm volatile("s_waitcnt vmcnt(" #n ") lgkmcnt(0)\n\ts_barrier" ::: "memory")
#define BAR_LDS() asm volatile("s_waitcnt lgkmcnt(0)\n\ts_barrier" ::: "memory")
#define WAIT_LGKM() asm volatile("s_waitcnt lgkmcnt(0)" ::: "memory")
#define SCHED() __builtin_amdgcn_sched_barrier(0)

DEV float xsum16_32(float v) {
  auto r = __builtin_amdgcn_permlane16_swap(__float_as_uint(v), __float_as_uint(v), false, false);
  v = __uint_as_float(r[0]) + __uint_as_float(r[1]);
  auto s = __builtin_amdgcn_permlane32_swap(__float_as_uint(v), __float_as_uint(v), false, false);
  return __uint_as_float(s[0]) + __uint_as_float(s[1]);
}
DEV float xmax16_32(float v) {
  auto r = __builtin_amdgcn_permlane16_swap(__float_as_uint(v), __float_as_uint(v), false, false);
  v = fmaxf(__uint_as_float(r[0]), __uint_as_float(r[1]));
  auto s = __builtin_amdgcn_permlane32_swap(__float_as_uint(v), __float_as_uint(v), false, false);
  return fmaxf(__uint_as_float(s[0]), __uint_as_float(s[1]));
}
DEV float xsum32(float v) {
  auto s = __builtin_amdgcn_permlane32_swap(__float_as_uint(v), __float_as_uint(v), false, false);
  return __uint_as_float(s[0]) + __uint_as_float(s[1]);
}

#define STAMP(i) do {} while (0)
#define TSARG
#define TSPASS

DEV GAS unsigned* flag_ptr(GAS unsigned* ctl, int ph, int dom, int j) { return ctl + FL_F + ((ph * 16 + dom) * 16 + j) * 32; }
DEV void wait_flags(GAS unsigned* word, unsigned tag) {
  for (unsigned spins = 0; spins < (1u << 20); ++spins) {
    const unsigned v = __hip_atomic_load(word, __ATOMIC_RELAXED, __HIP_MEMORY_SCOPE_AGENT);
    if (__all(v == tag)) break;
    __builtin_amdgcn_s_sleep(1);
  }
  __builtin_amdgcn_fence(__ATOMIC_ACQUIRE, "wavefront");
  asm volatile("" ::: "memory");
}

constexpr int P1_X = 0;
constexpr int P1_W = 81920;
constexpr int P1_WSLOT = 24576;
constexpr int P1_MISC = 155648;
constexpr int P2_W = 0;
constexpr int P2_X = 151936;
constexpr int CS_BUF = 160128;
constexpr int P3_UP = 14464;
constexpr int P3_CSP = 151936;
DEV constexpr int p3_slot_addr(int ds) { return (ds % 5) == 0 ? 65536 : (ds % 5) == 1 ? 94336 : (ds % 5) == 2 ? 123136 : (ds % 5) == 3 ? 0 : 28800; }
constexpr int P4_EARLY = 16384;
constexpr int P4_WAVE = 40960;

DEV unsigned wrow_voff(int lane) { return (unsigned)(lane >> 3) * 1024u + (unsigned)((lane & 7) ^ (((lane >> 4) & 1) << 2)) * 16u; }

DEV void p1_xvoff(int tok0, int w, int lane, unsigned (&vx)[5]) {
#pragma unroll
  for (int i = 0; i < 5; ++i) {
    const int rg = 5 * (w & 1) + i;
    const int row = 8 * rg + (lane >> 3);
    int tok = tok0 + row; tok = tok < 0 ? 0 : tok;
    const int ch = (lane & 7) ^ ((row >> 1) & 7);
    vx[i] = (unsigned)tok * 1024u + (unsigned)ch * 16u + 4096u - (unsigned)((i - 2) * 1024);
  }
}
DEV void p1_x_setup(int sp0, int w) { set_m0((unsigned)(P1_X + (sp0 + (w >> 1)) * 10240 + (5 * (w & 1) + 2) * 1024)); }
template <int I> DEV void p1_issue_x1(const Params& p, int sp0, int w, int lane, const unsigned (&vx)[5]) {
  const unsigned char* base = (const unsigned char*)p.x + (sp0 + (w >> 1)) * 128 - 4096;
  if constexpr (I == 0) { if ((w & 1) || lane >= 8) dma16i<(I - 2) * 1024>(base, vx[I]); }
  else dma16i<(I - 2) * 1024>(base, vx[I]);
}
DEV void p1_wvoff(int w, int lane, unsigned (&vo)[6]) {
#pragma unroll
  for (int i = 0; i < 6; ++i) {
    const int idx = 6 * w + i;
    const int part = idx >> 3, rg = idx & 7;
    const int row = 8 * rg + (lane >> 3);
    const int f = ((row >> 1) & 1) | (((row >> 3) & 1) << 1);
    const int ch = (lane & 7) ^ (f << 1);
    vo[i] = (unsigned)row * 3072u + (unsigned)part * 1024u + (unsigned)ch * 16u + 4096u - (unsigned)((i - 3) * 1024);
  }
}
DEV void p1_w_setup(int slot, int w) { set_m0((unsigned)(P1_W + slot * P1_WSLOT + (6 * w + 3) * 1024)); }
template <int I> DEV void p1_issue_w1(const Params& p, int hg, int st, const unsigned (&vo)[6]) {
  const unsigned char* base = (const unsigned char*)p.wqkv + st * 64 * 3072 + hg * 128 - 4096;
  dma16i<(I - 3) * 1024>(base, vo[I]);
}
DEV void wrow_voffs(int lane, unsigned (&v)[4]) {
#pragma unroll
  for (int i = 0; i < 4; ++i) v[i] = wrow_voff(lane) + 7168u * i;
}
template <int I> DEV void wrow_issue(const u16* rowbase  , const unsigned (&v)[4]) { dma16i<I * 1024>(rowbase, v[I]); }

struct P1Frag { v4u xf[5]; v4u wq, wk, wv; };

DEV void phase1(const Params& p, LAS unsigned char* L, __amdgpu_buffer_rsrc_t rs, int p64, int hg, int w, int lane,
                const unsigned (&wvo)[6], const unsigned (&vx)[5] TSARG) {
  const int s0 = (p64 & 3) * 2;
  const int g = lane >> 4, tk = lane & 15, q4 = (lane & 15) >> 2, pp = lane & 3;
  const int fsw = ((q4 >> 1) & 1) | ((g & 1) << 1);
  const int wbase = P1_W + (8 * g + q4) * 128 + (((2 * w + (pp >> 1)) ^ (fsw << 1)) * 16) + 8 * (pp & 1);
  const int xsw = (tk >> 1) & 7;
  const int xb0 = P1_X + tk * 128 + ((g ^ xsw) * 16), xb1 = P1_X + tk * 128 + (((4 + g) ^ xsw) * 16);
  const int xbw = (w & 1) ? xb1 : xb0;

  f32x4 aq[5], ak[5], av[5], ssum[5];
  float s2[5];
#pragma unroll
  for (int t = 0; t < 5; ++t) {
    aq[t] = f32x4{0, 0, 0, 0}; ak[t] = f32x4{0, 0, 0, 0}; av[t] = f32x4{0, 0, 0, 0}; ssum[t] = f32x4{0, 0, 0, 0}; s2[t] = 0.f;
  }
  const v4u ones = {0x3f803f80u, 0x3f803f80u, 0x3f803f80u, 0x3f803f80u};

  WAIT_VM_BAR(6);
  STAMP(11);
  P1Frag cur;
  {
    LAS unsigned char* xs = L + s0 * 10240 + xb0;
    LAS unsigned char* wsl = L + wbase;
    cur.wk = tr_read2(wsl + 8192, wsl + 8192 + 512);
    cur.wv = tr_read2(wsl + 16384, wsl + 16384 + 512);
    cur.wq = tr_read2(wsl, wsl + 512);
#pragma unroll
    for (int t = 0; t < 5; ++t) cur.xf[t] = *(LAS v4u*)(xs + t * 2048);
  }
  SCHED();
  static_for<16>([&](auto KS) {
    constexpr int ks = KS;
    if constexpr (ks == 1) { WAIT_VM_BAR(5); }
    else if constexpr (ks == 3 || ks == 7 || ks == 11) { WAIT_VM_BAR(6); }
    else if constexpr (ks == 5 || ks == 9) { WAIT_VM_BAR(11); }
    else if constexpr (ks == 13) { WAIT_VM_BAR(0); }
    constexpr bool isx = (ks == 0 || ks == 4 || ks == 8);
    constexpr int wst = ks == 1 ? 2 : ks == 2 ? 3 : ks == 3 ? 4 : ks == 5 ? 5 : ks == 7 ? 6 : ks == 9 ? 7 : -1;
    if constexpr (isx) p1_x_setup((s0 + 2 + ks / 2) & 7, w);
    if constexpr (wst >= 0) p1_w_setup(wst % 3, w);
    auto dma = [&](auto D) {
      constexpr int d = D;
      if constexpr (isx && d < 5) p1_issue_x1<d>(p, (s0 + 2 + ks / 2) & 7, w, lane, vx);
      if constexpr (wst >= 0) p1_issue_w1<d>(p, hg, (s0 + wst) & 7, wvo);
    };
    P1Frag nxt;
    constexpr int kn = ks + 1, itn = kn >> 1, k2n = kn & 1;
    LAS unsigned char* xs = L + ((s0 + itn) & 7) * 10240 + (k2n ? xb1 : xb0);
    LAS unsigned char* wsl = L + wbase + (itn % 3) * P1_WSLOT + k2n * 4096;
    constexpr int G = ks >> 2, sub = ks & 3;
    LAS unsigned char* sx = L + ((s0 + 2 * G + (w >> 1)) & 7) * 10240 + xbw;
    v4u sva, svb;
    dma(std::integral_constant<int, 0>{});
    static_for<5>([&](auto T) {
      constexpr int t = T;
      if constexpr (ks < 15) {
        if constexpr (t == 0) nxt.wk = tr_read2(wsl + 8192, wsl + 8192 + 512);
        if constexpr (t == 1) nxt.wv = tr_read2(wsl + 16384, wsl + 16384 + 512);
        if constexpr (t == 2) nxt.wq = tr_read2(wsl, wsl + 512);
        nxt.xf[t] = *(LAS v4u*)(xs + t * 2048);
      }
      if constexpr (t == 0) sva = *(LAS v4u*)(sx + sub * 2048);
      if constexpr (t == 1 && sub == 3) svb = *(LAS v4u*)(sx + 4 * 2048);
      ak[t] = mfma16(cur.wk, cur.xf[t], ak[t]);
      av[t] = mfma16(cur.xf[t], cur.wv, av[t]);
      aq[t] = mfma16(cur.wq, cur.xf[t], aq[t]);
      if constexpr (t == 4) {
        ssum[sub] = mfma16(ones, sva, ssum[sub]);
#pragma unroll
        for (int e = 0; e < 4; ++e) s2[sub] = dot2(sva[e], sva[e], s2[sub]);
        if constexpr (sub == 3) {
          ssum[4] = mfma16(ones, svb, ssum[4]);
#pragma unroll
          for (int e = 0; e < 4; ++e) s2[4] = dot2(svb[e], svb[e], s2[4]);
        }
      }
      SCHED();
      dma(std::integral_constant<int, t + 1>{});
      SCHED();
    });
    if constexpr (ks < 15) cur = nxt;
  });
  STAMP(12);
  LAS f32x2* part = (LAS f32x2*)(L + P1_MISC);
  LAS float* fmean = (LAS float*)(L + P1_MISC + 2560 + w * 640);
  LAS float* frstd = fmean + 80;
#pragma unroll
  for (int t = 0; t < 5; ++t) {
    const float b = xsum16_32(s2[t]);
    if (g == 0) part[w * 80 + t * 16 + tk] = f32x2{ssum[t][0], b};
  }
  BAR_LDS();
  unsigned wv4[4];
  wrow_voffs(lane, wv4);
  const u16* wob = p.wo + (128 * w) * 512 + hg * 64;
  auto dmo = [&](auto G) {
    constexpr int gq = G;
    set_m0((unsigned)(P2_W + (128 * w + 32 * gq) * 128));
    static_for<4>([&](auto I) { wrow_issue<I>(wob + gq * 32 * 512, wv4); });
  };
  dmo(std::integral_constant<int, 0>{});
#pragma unroll
  for (int r = 0; r < 2; ++r) {
    const int tok = r * 64 + lane;
    if (tok < 80) {
      const f32x2 a = (part[tok] + part[80 + tok]) + (part[160 + tok] + part[240 + tok]);
      const float mean = a.x * (1.f / 512.f);
      const float var = fmaxf(a.y * (1.f / 512.f) - mean * mean, 0.f);
      fmean[tok] = mean; frstd[tok] = __builtin_amdgcn_rsqf(var + 1e-5f);
    }
  }
  f32x2 csq[2], csk[2];
#pragma unroll
  for (int r = 0; r < 4; ++r) {
    csq[r >> 1][r & 1] = __uint_as_float(__builtin_amdgcn_update_dpp(0, __float_as_uint(aq[0][r]), 0x150, 0xf, 0xf, false));
    csk[r >> 1][r & 1] = __uint_as_float(__builtin_amdgcn_update_dpp(0, __float_as_uint(ak[0][r]), 0x150, 0xf, 0xf, false));
  }
  float csv;
  {
    auto a = __builtin_amdgcn_permlane16_swap(__float_as_uint(av[0][0]), __float_as_uint(av[0][0]), false, false);
    auto c = __builtin_amdgcn_permlane32_swap(a[0], a[0], false, false);
    csv = __uint_as_float(c[0]);
  }
  dmo(std::integral_constant<int, 1>{});
  WAIT_LGKM();
  float m1[5], r1[5];
  f32x4 mv[5], rv[5];
#pragma unroll
  for (int t = 0; t < 5; ++t) {
    m1[t] = fmean[t * 16 + tk]; r1[t] = frstd[t * 16 + tk];
    mv[t] = *(LAS f32x4*)(fmean + t * 16 + 4 * g); rv[t] = *(LAS f32x4*)(frstd + t * 16 + 4 * g);
  }
  SCHED();
  v2u qp[4], kp[5], vp[5];
  const float sc = 0.25f * 1.44269504088896f;
  const f32x2 c2 = {csv, csv};
#pragma unroll
  for (int t = 0; t < 5; ++t) {
    const f32x2 m2 = {m1[t], m1[t]}, r2 = {r1[t], r1[t]};
    kp[t].x = cvt2((pk2(ak[t][0], ak[t][1]) - m2 * csk[0]) * r2);
    kp[t].y = cvt2((pk2(ak[t][2], ak[t][3]) - m2 * csk[1]) * r2);
    if (t > 0) {
      qp[t - 1].x = cvt2(pk2(aq[t][0], aq[t][1]) - m2 * csq[0]);
      qp[t - 1].y = cvt2(pk2(aq[t][2], aq[t][3]) - m2 * csq[1]);
    }
  }
  SCHED();
  const f32x4 z = {0, 0, 0, 0};
  f32x4 sp[4], scu[4];
#pragma unroll
  for (int t = 0; t < 4; ++t) { sp[t] = mfma16k16(kp[t], qp[t], z); scu[t] = mfma16k16(kp[t + 1], qp[t], z); }
  SCHED();
  dmo(std::integral_constant<int, 2>{});
#pragma unroll
  for (int t = 0; t < 5; ++t) {
    vp[t].x = cvt2((pk2(av[t][0], av[t][1]) - pk2(mv[t][0], mv[t][1]) * c2) * pk2(rv[t][0], rv[t][1]));
    vp[t].y = cvt2((pk2(av[t][2], av[t][3]) - pk2(mv[t][2], mv[t][3]) * c2) * pk2(rv[t][2], rv[t][3]));
  }
  SCHED();
  f32x2 biasp[2], biasc[2];
#pragma unroll
  for (int r = 0; r < 4; ++r) {
    const int dc = tk - 4 * g - r, dp = dc + 16;
    biasc[r >> 1][r & 1] = (dc >= 0 && dc <= 14 && !(dc & 1)) ? 0.f : -1e30f;
    biasp[r >> 1][r & 1] = (dp <= 14 && !(dp & 1)) ? 0.f : -1e30f;
  }
  f32x2 a[4][4];
  float mx[4], sm[4];
#pragma unroll
  for (int t = 0; t < 4; ++t) {
    const float sq = sc * r1[t + 1];
    const f32x2 s2v = {sq, sq};
    f32x2 bp0 = biasp[0], bp1 = biasp[1];
    if (t == 0 && p64 == 0) { bp0 = f32x2{-1e30f, -1e30f}; bp1 = bp0; }
    a[t][0] = pk2(sp[t][0], sp[t][1]) * s2v + bp0; a[t][1] = pk2(sp[t][2], sp[t][3]) * s2v + bp1;
    a[t][2] = pk2(scu[t][0], scu[t][1]) * s2v + biasc[0]; a[t][3] = pk2(scu[t][2], scu[t][3]) * s2v + biasc[1];
  }
#pragma unroll
  for (int t = 0; t < 4; ++t)
    mx[t] = fmaxf(fmaxf(__builtin_fmaxf(a[t][0].x, a[t][0].y), fmaxf(a[t][1].x, a[t][1].y)), fmaxf(fmaxf(a[t][2].x, a[t][2].y), fmaxf(a[t][3].x, a[t][3].y)));
  SCHED();
  auto xred2 = [&](float& A, float& B, auto op) {
    auto s1 = __builtin_amdgcn_permlane16_swap(__float_as_uint(A), __float_as_uint(B), false, false);
    const float u = op(__uint_as_float(s1[0]), __uint_as_float(s1[1]));
    auto s2_ = __builtin_amdgcn_permlane32_swap(__float_as_uint(u), __float_as_uint(u), false, false);
    const float v = op(__uint_as_float(s2_[0]), __uint_as_float(s2_[1]));
    auto s3 = __builtin_amdgcn_permlane16_swap(__float_as_uint(v), __float_as_uint(v), false, false);
    A = __uint_as_float(s3[0]); B = __uint_as_float(s3[1]);
  };
  auto fmx = [](float x, float y) { return fmaxf(x, y); };
  auto fad = [](float x, float y) { return x + y; };
  xred2(mx[0], mx[1], fmx); xred2(mx[2], mx[3], fmx);
  SCHED();
  dmo(std::integral_constant<int, 3>{});
#pragma unroll
  for (int t = 0; t < 4; ++t) {
    const f32x2 m2 = {mx[t], mx[t]};
#pragma unroll
    for (int q = 0; q < 4; ++q) a[t][q] -= m2;
  }
#pragma unroll
  for (int t = 0; t < 4; ++t)
#pragma unroll
    for (int q = 0; q < 4; ++q) { a[t][q].x = __builtin_amdgcn_exp2f(a[t][q].x); a[t][q].y = __builtin_amdgcn_exp2f(a[t][q].y); }
  SCHED();
  v4u pf[4];
#pragma unroll
  for (int t = 0; t < 4; ++t) {
    const f32x2 sv = (a[t][0] + a[t][1]) + (a[t][2] + a[t][3]);
    sm[t] = sv.x + sv.y;
    pf[t] = v4u{cvt2(a[t][0]), cvt2(a[t][1]), cvt2(a[t][2]), cvt2(a[t][3])};
  }
  f32x4 o[4];
#pragma unroll
  for (int t = 0; t < 4; ++t) {
    const v4u vf = {vp[t].x, vp[t].y, vp[t + 1].x, vp[t + 1].y};
    o[t] = mfma16(vf, pf[t], z);
  }
  SCHED();
  xred2(sm[0], sm[1], fad); xred2(sm[2], sm[3], fad);
  v2u ob[4];
#pragma unroll
  for (int t = 0; t < 4; ++t) {
    const float inv = __builtin_amdgcn_rcpf(sm[t]);
    const f32x2 i2 = {inv, inv};
    ob[t] = v2u{cvt2(pk2(o[t][0], o[t][1]) * i2), cvt2(pk2(o[t][2], o[t][3]) * i2)};
  }
  const int head = 4 * hg + w;
#pragma unroll
  for (int u = 0; u < 2; ++u) {
    auto sx = __builtin_amdgcn_permlane16_swap(ob[2 * u].x, ob[2 * u + 1].x, false, false);
    auto sy = __builtin_amdgcn_permlane16_swap(ob[2 * u].y, ob[2 * u + 1].y, false, false);
    const v4u o4 = {sx[0], sy[0], sx[1], sy[1]};
    const int voff = ((g >> 1) * 32 + (g & 1) * 16 + tk) * 16;
    __builtin_amdgcn_raw_buffer_store_b128(o4, rs, voff, (int)WS_O + ((2 * p64 + u) * 32 + head) * 1024, AUX_SC1);
  }
}

DEV void cs_load(const Params& p, int c, int r, int tid, v4u (&v)[4]) {
  const int rg = tid >> 5, c0 = tid & 31, cc = c0 < 28 ? c0 : 27;
  const u16* base = (cc < 14 ? p.wg : p.wu) + (size_t)(32 * r + 4 * rg) * 1792 + 112 * c + (cc < 14 ? cc : cc - 14) * 8;
#pragma unroll
  for (int i = 0; i < 4; ++i) v[i] = *(const GAS v4u*)(base + i * 1792);
}
DEV void cs_finish(LAS unsigned char* L, __amdgpu_buffer_rsrc_t rs, int c, int r, int w, int lane, const v4u (&v)[4]) {
  LAS float* buf = (LAS float*)(L + CS_BUF);
  const int c0 = lane & 31;
  f32x4 a, b;
#pragma unroll
  for (int e = 0; e < 2; ++e) {
    a[2 * e] = xsum32((bf_lo(v[0][e]) + bf_lo(v[1][e])) + (bf_lo(v[2][e]) + bf_lo(v[3][e])));
    a[2 * e + 1] = xsum32((bf_hi(v[0][e]) + bf_hi(v[1][e])) + (bf_hi(v[2][e]) + bf_hi(v[3][e])));
    b[2 * e] = xsum32((bf_lo(v[0][2 + e]) + bf_lo(v[1][2 + e])) + (bf_lo(v[2][2 + e]) + bf_lo(v[3][2 + e])));
    b[2 * e + 1] = xsum32((bf_hi(v[0][2 + e]) + bf_hi(v[1][2 + e])) + (bf_hi(v[2][2 + e]) + bf_hi(v[3][2 + e])));
  }
  if (lane < 28) {
    *(LAS f32x4*)(buf + w * 224 + c0 * 8) = a;
    *(LAS f32x4*)(buf + w * 224 + c0 * 8 + 4) = b;
  }
  BAR_LDS();
  const int tid = w * 64 + lane;
  if (tid < 224) {
    const float s = (buf[tid] + buf[224 + tid]) + (buf[448 + tid] + buf[672 + tid]);
    __builtin_amdgcn_raw_buffer_store_b32(__float_as_uint(s), rs, tid * 4, (int)WS_CS + (c * 16 + r) * 896, AUX_SC1);
  }
}

DEV void p3_voffs(int lane, unsigned (&vo)[7]) {
#pragma unroll
  for (int i = 0; i < 7; ++i) {
    const int q = i * 64 + lane, row = q / 14, cc = q - 14 * row;
    vo[i] = (unsigned)row * 3584u + (unsigned)cc * 16u + 4096u - (unsigned)((i - 3) * 1024);
  }
}
DEV void p3_w_setup(int ds, int w) { set_m0((unsigned)(p3_slot_addr(ds) + (w & 1) * P3_UP + (w >> 1) * 7168 + 3 * 1024)); }
template <int I> DEV void p3_issue_w1(const Params& p, int c, int ds, int w, const unsigned (&vo)[7]) {
  const unsigned char* base = (const unsigned char*)((w & 1) ? p.wu : p.wg) + (64 * ds + 32 * (w >> 1)) * 3584 + 224 * c - 4096;
  dma16i<(I - 3) * 1024>(base, vo[I]);
}
DEV void p3_issue_w(const Params& p, int c, int ds, int w, const unsigned (&vo)[7]) {
  p3_w_setup(ds, w);
  static_for<7>([&](auto I) { p3_issue_w1<I>(p, c, ds, w, vo); });
}

DEV void p2_issue_x(const Params& p, int p64, int nt, int w, int lane) {
  const unsigned vx = (unsigned)(lane >> 3) * 1024u + (unsigned)(((lane & 7) ^ (lane >> 3)) * 16);
  const u16* xb = p.x + (64 * p64 + 16 * w) * 512 + nt * 64;
  set_m0((unsigned)(P2_X + 16 * w * 128));
  dma16i<0>(xb, vx);
  dma16i<1024>(xb, vx + 7168u);
}

template <int WSTRIDE>
DEV f32x16 reduce4(f32x16 (&acc)[2][2], LAS unsigned char* red, int w, int lane) {
  WAIT_LGKM();
#pragma unroll
  for (int a = 0; a < 2; ++a)
#pragma unroll
    for (int b = 0; b < 2; ++b) {
      const int dest = a + 2 * b;
      if (dest != w) {
        const int slot = (dest - w - 1) & 3;
#pragma unroll
        for (int qd = 0; qd < 4; ++qd)
          *(LAS f32x4*)(red + w * WSTRIDE + (slot * 4 + qd) * 1024 + lane * 16) =
              f32x4{acc[a][b][4 * qd], acc[a][b][4 * qd + 1], acc[a][b][4 * qd + 2], acc[a][b][4 * qd + 3]};
      }
    }
  BAR_LDS();
  f32x16 r;
  if (w == 0) r = acc[0][0]; else if (w == 1) r = acc[1][0]; else if (w == 2) r = acc[0][1]; else r = acc[1][1];
#pragma unroll
  for (int s = 1; s < 4; ++s) {
    const int src = (w + s) & 3;
#pragma unroll
    for (int qd = 0; qd < 4; ++qd) {
      const f32x4 v = *(LAS f32x4*)(red + src * WSTRIDE + ((3 - s) * 4 + qd) * 1024 + lane * 16);
#pragma unroll
      for (int e = 0; e < 4; ++e) r[4 * qd + e] += v[e];
    }
  }
  return r;
}

DEV void phase2(const Params& p, LAS unsigned char* L, __amdgpu_buffer_rsrc_t rs, int p64, int nt, int w, int lane, v4u (&hkeep)[2],
                int c3, const unsigned (&vo)[7] TSARG) {
  v4u of[2][8];
#pragma unroll
  for (int i = 0; i < 8; ++i)
#pragma unroll
    for (int tt = 0; tt < 2; ++tt)
      of[tt][i] = __builtin_amdgcn_raw_buffer_load_b128(rs, lane * 16, (int)WS_O + ((2 * p64 + tt) * 32 + 8 * w + i) * 1024, AUX_SC1);
  SCHED();
  const int hh = lane >> 5, blk = (lane >> 4) & 1, q4 = (lane & 15) >> 2, pp = lane & 3;
  const int sw = ((q4 >> 1) & 1) << 2;
  LAS unsigned char* wb0 = L + P2_W + (128 * w + 8 * hh + q4) * 128 + (((2 * blk + (pp >> 1)) ^ sw) * 16) + 8 * (pp & 1);
  LAS unsigned char* wb1 = L + P2_W + (128 * w + 8 * hh + q4) * 128 + (((4 + 2 * blk + (pp >> 1)) ^ sw) * 16) + 8 * (pp & 1);
  f32x16 acc[2][2];
#pragma unroll
  for (int a = 0; a < 2; ++a)
#pragma unroll
    for (int b = 0; b < 2; ++b)
#pragma unroll
      for (int e = 0; e < 16; ++e) acc[a][b][e] = 0.f;
  v4u wa[8][2];
#pragma unroll
  for (int i = 0; i < 8; ++i) {
    wa[i][0] = tr_read2(wb0 + i * 2048, wb0 + i * 2048 + 512);
    wa[i][1] = tr_read2(wb1 + i * 2048, wb1 + i * 2048 + 512);
  }
  SCHED();
#pragma unroll
  for (int i = 0; i < 8; ++i) {
    acc[0][0] = mfma32(wa[i][0], of[0][i], acc[0][0]);
    acc[1][0] = mfma32(wa[i][1], of[0][i], acc[1][0]);
    acc[0][1] = mfma32(wa[i][0], of[1][i], acc[0][1]);
    acc[1][1] = mfma32(wa[i][1], of[1][i], acc[1][1]);
  }
  SCHED();
  STAMP(13);
  p3_issue_w(p, c3, 2, w, vo);
  asm volatile("s_waitcnt vmcnt(7)" ::: "memory");
  f32x16 r = reduce4<16384>(acc, L + P2_W, w, lane);
  const int row = 32 * (w >> 1) + (lane & 31);
  float hv[16];
  float s1 = 0.f, s2 = 0.f;
#pragma unroll
  for (int qd = 0; qd < 4; ++qd) {
    const v2u xr = *(LAS v2u*)(L + P2_X + row * 128 + (((4 * (w & 1) + qd) ^ (row & 7)) * 16) + 8 * hh);
    const unsigned y01 = pk_bf16(r[4 * qd + 0], r[4 * qd + 1]), y23 = pk_bf16(r[4 * qd + 2], r[4 * qd + 3]);
    const unsigned h01 = pk_bf16(bf_lo(xr.x) + bf_lo(y01), bf_hi(xr.x) + bf_hi(y01));
    const unsigned h23 = pk_bf16(bf_lo(xr.y) + bf_lo(y23), bf_hi(xr.y) + bf_hi(y23));
    hkeep[qd >> 1][2 * (qd & 1)] = h01; hkeep[qd >> 1][2 * (qd & 1) + 1] = h23;
    hv[4 * qd + 0] = bf_lo(h01); hv[4 * qd + 1] = bf_hi(h01); hv[4 * qd + 2] = bf_lo(h23); hv[4 * qd + 3] = bf_hi(h23);
  }
  const int tile32 = 2 * p64 + (w >> 1);
  const int nt32 = 2 * nt + (w & 1);
#pragma unroll
  for (int ks = 0; ks < 2; ++ks)
    __builtin_amdgcn_raw_buffer_store_b128(hkeep[ks], rs, lane * 16, (int)WS_H + (tile32 * 32 + 2 * nt32 + ks) * 1024, AUX_SC1);
#pragma unroll
  for (int i = 0; i < 16; ++i) { s1 += hv[i]; s2 += hv[i] * hv[i]; }
  s1 = xsum32(s1); s2 = xsum32(s2);
  if (hh == 0) {
    v2u st = {__float_as_uint(s1), __float_as_uint(s2)};
    __builtin_amdgcn_raw_buffer_store_b64(st, rs, (lane & 31) * 16, (int)WS_HS + (tile32 * 8 + (nt32 >> 1)) * 512 + (nt32 & 1) * 8, AUX_SC1);
  }
}

constexpr int P3_KM = 20;
constexpr int p3_nl(int ks) { return ks < 4 ? 2 : 1; }
constexpr int p3_nd(int ks) { return (ks >= 4 && ks < 16) ? ((ks & 3) == 3 ? 1 : 2) : 0; }
constexpr int p3_nx(int ks) { return ks == 7 ? 1 : (ks == 16 || ks == 17) ? 2 : 0; }
constexpr int p3_cum(int ks) { int c = 12; for (int k = 0; k < ks; ++k) c += p3_nl(k) + p3_nd(k) + p3_nx(k); return c; }
constexpr int p3_pos(int i) { return i < 8 ? 4 + i : i < 16 ? p3_cum((i - 8) / 2) + ((i - 8) & 1) : p3_cum(i - 12); }

DEV void phase3(const Params& p, LAS unsigned char* L, __amdgpu_buffer_rsrc_t rs, v4u rsv, GAS unsigned* ctl, unsigned tag, int p128, int c, int w, int lane,
                int nt4, const unsigned (&vo)[7] TSARG) {
  const int tile32 = 4 * p128 + w;
  const int hh = lane >> 5, blk = (lane >> 4) & 1, q4 = (lane & 15) >> 2, pp = lane & 3;
  v4u hb[32];
  v4u hs[4];
  const unsigned hbase = WS_H + tile32 * 32768;
  static_for<4>([&](auto I) { bload_v<I * 512>(hs[I], rsv, (4 * hh) * 512 + (lane & 31) * 16, WS_HS + tile32 * 4096); });
  static_for<8>([&](auto I) { bload_a<(I & 3) * 1024>(hb[I], rsv, lane * 16, hbase + (I >> 2) * 4096); });
  SCHED();
  const int abase = blk * P3_UP + (4 * hh + q4) * 224 + pp * 8;
  int ab0 = abase, ab1 = abase + 65536, ab2 = abase + 123136;
  PIN_V(ab0); PIN_V(ab1); PIN_V(ab2);
  auto slot_ptr = [&](int ds) -> LAS unsigned char* {
    const int a = p3_slot_addr(ds);
    return a == 123136 ? L + ab2 : a >= 65536 ? L + ab1 + (a - 65536) : L + ab0 + a;
  };
  f32x16 acc[7];
#pragma unroll
  for (int t = 0; t < 7; ++t)
#pragma unroll
    for (int e = 0; e < 16; ++e) acc[t][e] = 0.f;
  WAIT_VM_BAR(12);
  STAMP(14);
  v4u an[7];
  {
    LAS unsigned char* sl = slot_ptr(0);
#pragma unroll
    for (int t = 0; t < 7; ++t) an[t] = tr_read2(sl + t * 32, sl + t * 32 + 8 * 224);
  }
  SCHED();
  unsigned csflag = 0;
  v4u csl[4];
  static_for<P3_KM>([&](auto KS) {
    constexpr int ks = KS;
    v4u ac[7];
#pragma unroll
    for (int t = 0; t < 7; ++t) ac[t] = an[t];
    wait_vm_a<p3_cum(ks) - 1 - p3_pos(ks)>(hb[ks]);
    constexpr int ds = (ks + 1) >> 2, ksl = (ks + 1) & 3;
    LAS unsigned char* sl = slot_ptr(ds) + ksl * 16 * 224;
    auto vm = [&](int slot) {
      constexpr int nl = p3_nl(ks), nd = p3_nd(ks), nx = p3_nx(ks);
      if constexpr (true) {
        if (slot < nl) {
          if constexpr (ks < 4) {
            if (slot == 0) bload_a<((8 + 2 * ks) & 3) * 1024>(hb[8 + 2 * ks], rsv, lane * 16, hbase + ((8 + 2 * ks) >> 2) * 4096);
            else bload_a<((9 + 2 * ks) & 3) * 1024>(hb[9 + 2 * ks], rsv, lane * 16, hbase + ((9 + 2 * ks) >> 2) * 4096);
          } else bload_a<((12 + ks) & 3) * 1024>(hb[(12 + ks) & 31], rsv, lane * 16, hbase + ((12 + ks) >> 2) * 4096);
        } else if (slot < nl + nd) {
          if constexpr (nd > 0) {
            if constexpr ((ks & 3) == 0) { if (slot == nl) p3_w_setup(5 + ((ks - 4) >> 2), w); }
            if (slot == nl) p3_issue_w1<2 * (ks & 3)>(p, c, 5 + ((ks - 4) >> 2), w, vo);
            else p3_issue_w1<(2 * (ks & 3) + 1) % 7>(p, c, 5 + ((ks - 4) >> 2), w, vo);
          }
        } else if (slot < nl + nd + nx) {
          if constexpr (ks == 7) bload1_v(csflag, rsv, (unsigned)(FL_F + ((16 + (lane & 15)) * 16 + c) * 32) * 4u, 0u);
          if constexpr (ks == 16 || ks == 17) {
            const int i = slot - nl - nd;
            const unsigned vof = (unsigned)(lane < 56 ? lane : 55) * 16u;
            const unsigned so = WS_CS + (unsigned)(c * 16 + 4 * w) * 896u;
            if constexpr (ks == 16) { if (i == 0) bload_v<0>(csl[0], rsv, vof, so); else bload_v<896>(csl[1], rsv, vof, so); }
            else { if (i == 0) bload_v<1792>(csl[2], rsv, vof, so); else bload_v<2688>(csl[3], rsv, vof, so); }
          }
        }
      }
    };
    if constexpr (ks == 12) {
      wait_vm_v1<p3_cum(12) - 1 - (p3_cum(7) + p3_nl(7) + p3_nd(7))>(csflag);
      if (!__all(csflag == tag)) wait_flags(flag_ptr(ctl, 1, lane & 15, c), tag);
    }
    static_for<7>([&](auto T) {
      constexpr int t = T;
      if constexpr (t == 0 || t == 2) asm volatile("" : "+v"(ac[t]), "+v"(ac[t + 1]));
      if constexpr (t == 4) asm volatile("" : "+v"(ac[4]), "+v"(ac[5]), "+v"(ac[6]));
      acc[t] = mfma32(ac[t], hb[ks], acc[t]);
      SCHED();
      if constexpr (ks + 1 < P3_KM) {
        if constexpr (t == 1 || t == 3 || t == 5) {
          an[t - 1] = tr_read2(sl + (t - 1) * 32, sl + (t - 1) * 32 + 8 * 224);
          an[t] = tr_read2(sl + t * 32, sl + t * 32 + 8 * 224);
        }
        if constexpr (t == 6) an[6] = tr_read2(sl + 6 * 32, sl + 6 * 32 + 8 * 224);
      }
      if constexpr (t == 0 || t == 2 || t == 4) vm(t >> 1);
      SCHED();
    });
    if constexpr ((ks & 3) == 3 && (ks >> 2) < 3) BAR_LDS();
  });
  STAMP(15);
  asm volatile("s_waitcnt vmcnt(0)" : "+v"(hs[0]), "+v"(hs[1]), "+v"(hs[2]), "+v"(hs[3]), "+v"(csl[0]), "+v"(csl[1]), "+v"(csl[2]), "+v"(csl[3]) :: "memory");
#pragma unroll
  for (int k = P3_KM; k < 32; ++k) PIN_A(hb[k]);
  LAS float* cspart = (LAS float*)(L + P3_CSP);
  int cspo = P3_CSP + 896 * 4 + 16 * hh; PIN_V(cspo);
  LAS float* csp = cspart + 896;
  LAS float* csq_ = (LAS float*)(L + cspo);
  {
    f32x4 a;
#pragma unroll
    for (int e = 0; e < 4; ++e)
      a[e] = (__uint_as_float(csl[0][e]) + __uint_as_float(csl[1][e])) + (__uint_as_float(csl[2][e]) + __uint_as_float(csl[3][e]));
    if (lane < 56) *(LAS f32x4*)(cspart + w * 224 + lane * 4) = a;
  }
  float s1 = 0.f, s2 = 0.f;
#pragma unroll
  for (int i = 0; i < 4; ++i) {
    s1 += __uint_as_float(hs[i].x) + __uint_as_float(hs[i].z);
    s2 += __uint_as_float(hs[i].y) + __uint_as_float(hs[i].w);
  }
  s1 = xsum32(s1); s2 = xsum32(s2);
  const float mean = s1 * (1.f / 512.f);
  const float rstd = __builtin_amdgcn_rsqf(fmaxf(s2 * (1.f / 512.f) - mean * mean, 0.f) + 1e-5f);
  BAR_LDS();
  auto rdt = [&](int t, v4u (&f)[12]) {
#pragma unroll
    for (int k = 0; k < 12; ++k) {
      const int ks = P3_KM + k, ds = ks >> 2, ksl = ks & 3;
      LAS unsigned char* sl = slot_ptr(ds) + ksl * 16 * 224 + t * 32;
      f[k] = tr_read2(sl, sl + 8 * 224);
    }
  };
  v4u fn[12];
  rdt(0, fn);
  if (lane < 56) {
    const f32x4 a = (*(LAS f32x4*)(cspart + lane * 4) + *(LAS f32x4*)(cspart + 224 + lane * 4)) +
                    (*(LAS f32x4*)(cspart + 448 + lane * 4) + *(LAS f32x4*)(cspart + 672 + lane * 4));
    *(LAS f32x4*)(csp + lane * 4) = a;
  }
  WAIT_LGKM();
  const f32x2 nm2 = {-mean, -mean};
  const float r2s = rstd * rstd;
  const f32x2 k1 = {-2.302208198f * rstd, -2.302208198f * rstd}, k2 = {-0.1029432397f * rstd * r2s, -0.1029432397f * rstd * r2s};
  const f32x2 rr2 = {r2s, r2s}, one2 = {1.f, 1.f};
  f32x2 yg[4], yu[4], zz[4], rc[4];
  f32x4 cgu[4];
  unsigned gw[4];
  auto epi_piece = [&](auto T, auto K) {
    constexpr int t = T, k = K;
    auto csg = [&](int i) { return pk2(cgu[i >> 1][2 * (i & 1)], cgu[i >> 1][2 * (i & 1) + 1]); };
    auto csu = [&](int i) { return pk2(cgu[2 + (i >> 1)][2 * (i & 1)], cgu[2 + (i >> 1)][2 * (i & 1) + 1]); };
    auto ex2 = [&](int i) { zz[i].x = __builtin_amdgcn_exp2f(zz[i].x); zz[i].y = __builtin_amdgcn_exp2f(zz[i].y); };
    auto rc2 = [&](int i) { rc[i].x = __builtin_amdgcn_rcpf(zz[i].x); rc[i].y = __builtin_amdgcn_rcpf(zz[i].y); };
    if constexpr (k == 0) {
      cgu[0] = *(LAS f32x4*)(csq_ + t * 16);
      cgu[1] = *(LAS f32x4*)(csq_ + t * 16 + 8);
      cgu[2] = *(LAS f32x4*)(csq_ + 112 + t * 16);
      cgu[3] = *(LAS f32x4*)(csq_ + 112 + t * 16 + 8);
    } else if constexpr (k == 1) {
      PIN_A(acc[t]);
#pragma unroll
      for (int i = 0; i < 4; ++i) { yg[i] = pk2(acc[t][2 * i], acc[t][2 * i + 1]); PIN_V(yg[i]); }
    } else if constexpr (k == 2) {
#pragma unroll
      for (int i = 0; i < 4; ++i) { yu[i] = pk2(acc[t][8 + 2 * i], acc[t][8 + 2 * i + 1]); PIN_V(yu[i]); }
    } else if constexpr (k == 3) {
#pragma unroll
      for (int i = 0; i < 4; ++i) yg[i] = nm2 * csg(i) + yg[i];
      yu[0] = nm2 * csu(0) + yu[0]; yu[1] = nm2 * csu(1) + yu[1];
    } else if constexpr (k == 4) {
#pragma unroll
      for (int i = 0; i < 4; ++i) zz[i] = yg[i] * yg[i];
      yu[2] = nm2 * csu(2) + yu[2]; yu[3] = nm2 * csu(3) + yu[3];
    } else if constexpr (k == 5) {
#pragma unroll
      for (int i = 0; i < 4; ++i) zz[i] = zz[i] * k2 + k1;
      yu[0] = yu[0] * yg[0]; yu[1] = yu[1] * yg[1];
    } else if constexpr (k == 6) {
#pragma unroll
      for (int i = 0; i < 4; ++i) zz[i] = zz[i] * yg[i];
      yu[2] = yu[2] * yg[2]; yu[3] = yu[3] * yg[3];
    } else if constexpr (k == 7) {
      ex2(0); ex2(1);
      yu[0] = yu[0] * rr2; yu[1] = yu[1] * rr2;
    } else if constexpr (k == 8) {
      ex2(2); ex2(3);
      zz[0] += one2; zz[1] += one2;
    } else if constexpr (k == 9) {
      rc2(0); rc2(1);
      zz[2] += one2; zz[3] += one2;
    } else if constexpr (k == 10) {
      rc2(2); rc2(3);
      yu[2] = yu[2] * rr2; yu[3] = yu[3] * rr2;
      yu[0] = yu[0] * rc[0]; yu[1] = yu[1] * rc[1];
    } else {
      yu[2] = yu[2] * rc[2]; yu[3] = yu[3] * rc[3];
#pragma unroll
      for (int i = 0; i < 4; ++i) gw[i] = cvt2(yu[i]);
      const v4u f = {gw[0], gw[1], gw[2], gw[3]};
      __builtin_amdgcn_raw_buffer_store_b128(f, rs, lane * 16, (int)WS_G + (tile32 * 112 + 7 * c + t) * 1024, AUX_SC1);
    }
  };
  unsigned wv4[4];
  wrow_voffs(lane, wv4);
  const u16* wdb = p.wd + (448 * w) * 512 + nt4 * 64;
  SCHED();
  STAMP(18);
  static_for<7>([&](auto T) {
    constexpr int t = T;
    v4u fc[12];
#pragma unroll
    for (int k = 0; k < 12; ++k) fc[k] = fn[k];
    static_for<12>([&](auto K) {
      constexpr int k = K;
      acc[t] = mfma32(fc[k], hb[P3_KM + k], acc[t]);
      SCHED();
      if constexpr (t < 6) {
        constexpr int ks = P3_KM + k, ds = ks >> 2, ksl = ks & 3;
        LAS unsigned char* sl = slot_ptr(ds) + ksl * 16 * 224 + (t + 1) * 32;
        fn[k] = tr_read2(sl, sl + 8 * 224);
      }
      if constexpr (t > 0) epi_piece(std::integral_constant<int, t - 1>{}, K);
      if constexpr (t >= 1 && t < 5 && (k & 3) == 0) {
        constexpr int gq = t - 1;
        if constexpr (k == 0) set_m0((unsigned)(w * P4_EARLY + gq * 4096));
        wrow_issue<(k >> 2)>(wdb + gq * 32 * 512, wv4);
        if constexpr (k == 8) wrow_issue<3>(wdb + gq * 32 * 512, wv4);
      }
      SCHED();
    });
  });
  STAMP(19);
  asm volatile("s_nop 15\n\ts_nop 7" ::: "memory");
  SCHED();
  static_for<12>([&](auto K) { epi_piece(std::integral_constant<int, 6>{}, K); });
}

constexpr int P4_UP = 10;
DEV void phase4(const Params& p, LAS unsigned char* L, v4u rsv, int p64, int nt, int w, int lane,
                const v4u (&hkeep)[2], const v4u (&wr)[8][2] TSARG) {
  const int hh = lane >> 5, blk = (lane >> 4) & 1, q4 = (lane & 15) >> 2, pp = lane & 3;
  const int sw = ((q4 >> 1) & 1) << 2;
  LAS unsigned char* wb0 = L + w * P4_WAVE + (4 * hh + q4) * 128 + (((2 * blk + (pp >> 1)) ^ sw) * 16) + 8 * (pp & 1);
  LAS unsigned char* wb1 = L + w * P4_WAVE + (4 * hh + q4) * 128 + (((4 + 2 * blk + (pp >> 1)) ^ sw) * 16) + 8 * (pp & 1);
  f32x16 acc[2][2];
#pragma unroll
  for (int a = 0; a < 2; ++a)
#pragma unroll
    for (int b = 0; b < 2; ++b)
#pragma unroll
      for (int e = 0; e < 16; ++e) acc[a][b][e] = 0.f;
  v4u gf[2][28];
  const unsigned gb0 = WS_G + ((2 * p64) * 112 + 28 * w) * 1024, gb1 = gb0 + 112 * 1024;
  auto ldg = [&](auto I) {
    constexpr int i = I;
    if constexpr (i < 24) {
      bload_a<(i & 3) * 1024>(gf[0][i], rsv, lane * 16, gb0 + (i >> 2) * 4096);
      bload_a<(i & 3) * 1024>(gf[1][i], rsv, lane * 16, gb1 + (i >> 2) * 4096);
    } else {
      bload_v<(i & 3) * 1024>(gf[0][i], rsv, lane * 16, gb0 + (i >> 2) * 4096);
      bload_v<(i & 3) * 1024>(gf[1][i], rsv, lane * 16, gb1 + (i >> 2) * 4096);
    }
  };
  static_for<P4_UP>(ldg);
  SCHED();
  v4u an0 = wr[0][0], an1 = wr[0][1];
  static_for<28>([&](auto I) {
    constexpr int i = I;
    constexpr int cum = (2 * (P4_UP + i) < 56) ? 2 * (P4_UP + i) : 56;
    constexpr int N = cum - 2 * i - 2;
    if constexpr (i < 24) wait_vm_a2<N>(gf[0][i], gf[1][i]); else wait_vm_v2<N>(gf[0][i], gf[1][i]);
    const v4u a0 = an0, a1 = an1;
    if constexpr (i + 1 < 8) { an0 = wr[i + 1][0]; an1 = wr[i + 1][1]; }
    else if constexpr (i + 1 < 28) {
      constexpr int sl = i + 1 - 8;
      an0 = tr_read2(wb0 + sl * 2048, wb0 + sl * 2048 + 8 * 128);
      an1 = tr_read2(wb1 + sl * 2048, wb1 + sl * 2048 + 8 * 128);
    }
    acc[0][0] = mfma32(a0, gf[0][i], acc[0][0]);
    acc[1][0] = mfma32(a1, gf[0][i], acc[1][0]);
    SCHED();
    if constexpr (i + P4_UP < 28) ldg(std::integral_constant<int, i + P4_UP>{});
    SCHED();
    acc[0][1] = mfma32(a0, gf[1][i], acc[0][1]);
    acc[1][1] = mfma32(a1, gf[1][i], acc[1][1]);
    SCHED();
  });
  STAMP(16);
  f32x16 r = reduce4<P4_WAVE>(acc, L, w, lane);
  const int tok = 64 * p64 + 32 * (w >> 1) + (lane & 31);
  const int n0 = 64 * nt + 32 * (w & 1) + 4 * hh;
#pragma unroll
  for (int qd = 0; qd < 4; ++qd) {
    const unsigned h01 = hkeep[qd >> 1][2 * (qd & 1)], h23 = hkeep[qd >> 1][2 * (qd & 1) + 1];
    const unsigned y01 = pk_bf16(r[4 * qd + 0], r[4 * qd + 1]), y23 = pk_bf16(r[4 * qd + 2], r[4 * qd + 3]);
    v2u o = {pk_bf16(bf_lo(h01) + bf_lo(y01), bf_hi(h01) + bf_hi(y01)), pk_bf16(bf_lo(h23) + bf_lo(y23), bf_hi(h23) + bf_hi(y23))};
    *(GAS v2u*)(p.out + tok * 512 + n0 + 8 * qd) = o;
  }
}
DEV void p4_read_wr(LAS unsigned char* L, int w, int lane, v4u (&wr)[8][2]) {
  const int hh = lane >> 5, blk = (lane >> 4) & 1, q4 = (lane & 15) >> 2, pp = lane & 3;
  const int sw = ((q4 >> 1) & 1) << 2;
  LAS unsigned char* wb0 = L + w * P4_EARLY + (4 * hh + q4) * 128 + (((2 * blk + (pp >> 1)) ^ sw) * 16) + 8 * (pp & 1);
  LAS unsigned char* wb1 = L + w * P4_EARLY + (4 * hh + q4) * 128 + (((4 + 2 * blk + (pp >> 1)) ^ sw) * 16) + 8 * (pp & 1);
#pragma unroll
  for (int i = 0; i < 8; ++i) {
    wr[i][0] = tr_read2(wb0 + i * 2048, wb0 + i * 2048 + 8 * 128);
    wr[i][1] = tr_read2(wb1 + i * 2048, wb1 + i * 2048 + 8 * 128);
  }
}

__global__ void __launch_bounds__(256, 1) mk_kernel(Params p) {
  extern __shared__ __attribute__((aligned(16))) unsigned char smem[];
  LAS unsigned char* L = (LAS unsigned char*)smem;
  const int tid = threadIdx.x, lane = tid & 63, w = __builtin_amdgcn_readfirstlane(tid >> 6);
  const int b = blockIdx.x;
  const int dom = (b & 7) * 2 + (b >> 7), j = (b >> 3) & 15;
  const int p64 = 2 * dom + (j >> 3), j8 = j & 7;
  const __amdgpu_buffer_rsrc_t rs = __builtin_amdgcn_make_buffer_rsrc((void*)p.ws, 0, (int)WS_BYTES, 0x00020000);
  const v4u rsv = {(unsigned)(size_t)p.ws, (unsigned)((size_t)p.ws >> 32), WS_BYTES, 0x00020000u};
  GAS unsigned* ctl = (GAS unsigned*)(p.ws + WS_FLAGS);
  STAMP(0);
  {
    unsigned long long pc; unsigned csz;
    asm volatile("s_getpc_b64 %0\n.Lmk_pc_here:\n\ts_mov_b32 %1, .Lmk_code_end-.Lmk_pc_here" : "=s"(pc), "=s"(csz));
    const unsigned off = (unsigned)((b >> 3) * 16 + (lane & 15)) * 128u + (unsigned)w * 65536u;
    if (lane < 16 && off < csz) {
      asm volatile("s_mov_b32 m0, %2\n\ts_nop 0\n\tglobal_load_lds_dword %0, %1" :: "v"(off), "s"(pc), "s"(160768u) : "memory");
    }
  }
  unsigned wvo[6], vx[5];
  {
    const int tok0 = 64 * p64 - 16, s0 = (p64 & 3) * 2;
    p1_xvoff(tok0, w, lane, vx);
    p1_wvoff(w, lane, wvo);
    p1_x_setup(s0, w);
    static_for<5>([&](auto I) { p1_issue_x1<I>(p, s0, w, lane, vx); });
    p1_w_setup(0, w);
    static_for<6>([&](auto I) { p1_issue_w1<I>(p, j8, s0, wvo); });
    p1_w_setup(1, w);
    static_for<6>([&](auto I) { p1_issue_w1<I>(p, j8, s0 + 1, wvo); });
    if (w == 0) *(LAS v4u*)(L + P1_X + (lane >> 3) * 10240 + (lane & 7) * 16) = v4u{0x3f803f80u, 0x3f803f80u, 0x3f803f80u, 0x3f803f80u};
  }
  STAMP(1);
  phase1(p, L, rs, p64, j8, w, lane, wvo, vx TSPASS);
  asm volatile("" ::: "memory"); SCHED();
  STAMP(2);
  const unsigned tag = __hip_atomic_load(ctl + FL_EPOCH, __ATOMIC_RELAXED, __HIP_MEMORY_SCOPE_AGENT) + 1u;
  WAIT_VM_BAR(0);
  if (tid == 0) __hip_atomic_store(flag_ptr(ctl, 0, dom, j), tag, __ATOMIC_RELAXED, __HIP_MEMORY_SCOPE_AGENT);
  STAMP(3);
  v4u csv4[4];
  cs_load(p, j, dom, tid, csv4);
  SCHED();
  unsigned vo[7];
  p3_voffs(lane, vo);
  p2_issue_x(p, p64, j8, w, lane);
#pragma unroll
  for (int ds = 0; ds < 2; ++ds) p3_issue_w(p, j, ds, w, vo);
  WAIT_VM(16);
  cs_finish(L, rs, j, dom, w, lane, csv4);
  wait_flags(flag_ptr(ctl, 0, dom, (j & 8) + (lane & 7)), tag);
  STAMP(4);
  v4u hkeep[2];
  phase2(p, L, rs, p64, j8, w, lane, hkeep, j, vo TSPASS);
  STAMP(5);
  WAIT_VM_BAR(0);
  if (tid == 0) __hip_atomic_store(flag_ptr(ctl, 1, dom, j), tag, __ATOMIC_RELAXED, __HIP_MEMORY_SCOPE_AGENT);
  STAMP(6);
#pragma unroll
  for (int ds = 3; ds < 5; ++ds) p3_issue_w(p, j, ds, w, vo);
  wait_flags(flag_ptr(ctl, 1, dom, lane & 15), tag);
  STAMP(7);
  phase3(p, L, rs, rsv, ctl, tag, dom, j, w, lane, j8, vo TSPASS);
  STAMP(8);
  v4u wr[8][2];
  WAIT_VM(0);
  p4_read_wr(L, w, lane, wr);
  BAR_LDS();
  if (tid == 0) __hip_atomic_store(flag_ptr(ctl, 2, dom, j), tag, __ATOMIC_RELAXED, __HIP_MEMORY_SCOPE_AGENT);
  STAMP(9);
  {
    unsigned wv4[4];
    wrow_voffs(lane, wv4);
    const u16* wdb = p.wd + (448 * w + 128) * 512 + j8 * 64;
    static_for<10>([&](auto G) {
      constexpr int gq = G;
      set_m0((unsigned)(w * P4_WAVE + gq * 4096));
      static_for<4>([&](auto I) { wrow_issue<I>(wdb + gq * 32 * 512, wv4); });
    });
  }
  wait_flags(flag_ptr(ctl, 2, dom, lane & 15), tag);
  STAMP(10);
  if (b == 0 && tid == 0) __hip_atomic_store(ctl + FL_EPOCH, tag, __ATOMIC_RELAXED, __HIP_MEMORY_SCOPE_AGENT);
  phase4(p, L, rsv, p64, j8, w, lane, hkeep, wr TSPASS);
  STAMP(17);
  asm volatile(".Lmk_code_end:" ::: "memory");
}

extern "C" void kernel_launch(void* const* d_in, const int* in_sizes, int n_in, void* d_out, int out_size,
                              void* d_ws, size_t ws_size, hipStream_t stream) {
  Params p;
  p.x = (const u16*)d_in[0];
  p.wqkv = (const u16*)d_in[1];
  p.wo = (const u16*)d_in[2];
  p.wg = (const u16*)d_in[4];
  p.wu = (const u16*)d_in[5];
  p.wd = (const u16*)d_in[6];
  p.out = (u16*)d_out;
  p.ws = (unsigned char*)d_ws;
  (void)hipFuncSetAttribute((const void*)mk_kernel, hipFuncAttributeMaxDynamicSharedMemorySize, LDS_BYTES);
  mk_kernel<<<256, 256, LDS_BYTES, stream>>>(p);
}
```

```cpp
#include <hip/hip_runtime.h>
#include <stdint.h>
#include <utility>

#define MK_STAMP 0

typedef unsigned short u16;
typedef unsigned v4u __attribute__((ext_vector_type(4)));
typedef unsigned v2u __attribute__((ext_vector_type(2)));
typedef short v8s __attribute__((ext_vector_type(8)));
typedef short v4s __attribute__((ext_vector_type(4)));
typedef float f32x2 __attribute__((ext_vector_type(2)));
typedef float f32x4 __attribute__((ext_vector_type(4)));
typedef float f32x16 __attribute__((ext_vector_type(16)));
typedef __bf16 bf16x2 __attribute__((ext_vector_type(2)));
#define GAS __attribute__((address_space(1)))
#define LAS __attribute__((address_space(3)))
#define DEV __device__ __forceinline__

constexpr unsigned WS_FLAGS = 0;
constexpr unsigned WS_O = 131072;
constexpr unsigned WS_H = WS_O + 2097152;
constexpr unsigned WS_HS = WS_H + 2097152;
constexpr unsigned WS_G = WS_HS + 262144;
constexpr unsigned WS_CS = WS_G + 64 * 112 * 1024;
constexpr unsigned WS_STAMP = WS_CS + 16 * 16 * 224 * 4;
constexpr unsigned WS_BYTES = WS_STAMP + 256 * 4 * 48 * 8;
constexpr int FL_EPOCH = 0, FL_F = 1024;

constexpr int LDS_BYTES = 163840;
constexpr int AUX_SC1 = 16;

struct Params {
  const u16* x; const u16* wqkv; const u16* wo; const u16* wg; const u16* wu; const u16* wd;
  u16* out; unsigned char* ws;
};

template <class F, int... I> DEV void static_for_impl(F&& f, std::integer_sequence<int, I...>) { (f(std::integral_constant<int, I>{}), ...); }
template <int N, class F> DEV void static_for(F&& f) { static_for_impl(f, std::make_integer_sequence<int, N>{}); }

DEV float bf_lo(unsigned u) { return __uint_as_float(u << 16); }
DEV float bf_hi(unsigned u) { return __uint_as_float(u & 0xffff0000u); }
DEV unsigned pk_bf16(float a, float b) {
  bf16x2 p; p[0] = (__bf16)a; p[1] = (__bf16)b;
  return __builtin_bit_cast(unsigned, p);
}
DEV f32x2 pk2(float a, float b) { return f32x2{a, b}; }
DEV unsigned cvt2(f32x2 v) { return pk_bf16(v.x, v.y); }
DEV float dot2(unsigned a, unsigned b, float c) {
  return __builtin_amdgcn_fdot2_f32_bf16(__builtin_bit_cast(bf16x2, a), __builtin_bit_cast(bf16x2, b), c, false);
}
DEV f32x4 mfma16(v4u a, v4u b, f32x4 c) {
  return __builtin_amdgcn_mfma_f32_16x16x32_bf16(__builtin_bit_cast(v8s, a), __builtin_bit_cast(v8s, b), c, 0, 0, 0);
}
DEV f32x4 mfma16k16(v2u a, v2u b, f32x4 c) {
  return __builtin_amdgcn_mfma_f32_16x16x16bf16_1k(__builtin_bit_cast(v4s, a), __builtin_bit_cast(v4s, b), c, 0, 0, 0);
}
DEV f32x16 mfma32(v4u a, v4u b, f32x16 c) {
  return __builtin_amdgcn_mfma_f32_32x32x16_bf16(__builtin_bit_cast(v8s, a), __builtin_bit_cast(v8s, b), c, 0, 0, 0);
}
DEV v2u tr_read(LAS unsigned char* p) {
  return __builtin_bit_cast(v2u, __builtin_amdgcn_ds_read_tr16_b64_v4i16((LAS v4s*)p));
}
DEV v4u tr_read2(LAS unsigned char* p0, LAS unsigned char* p1) {
  v2u a = tr_read(p0), b = tr_read(p1);
  return v4u{a.x, a.y, b.x, b.y};
}
DEV void dma16(const void* sbase, unsigned voff, unsigned lds_dst) {
  lds_dst = __builtin_amdgcn_readfirstlane(lds_dst);
  asm volatile("s_mov_b32 m0, %2\n\ts_nop 0\n\tglobal_load_lds_dwordx4 %0, %1"
               :: "v"(voff), "s"(sbase), "s"(lds_dst) : "memory");
}
DEV void set_m0(unsigned v) {
  v = __builtin_amdgcn_readfirstlane(v);
  asm volatile("s_mov_b32 m0, %0\n\ts_nop 0" :: "s"(v) : "memory");
}
template <int IMM> DEV void dma16i(const void* sbase, unsigned voff) {
  asm volatile("global_load_lds_dwordx4 %0, %1 offset:%c2" :: "v"(voff), "s"(sbase), "i"(IMM) : "memory");
}
template <int IMM> DEV void bload_a(v4u& dst, v4u rsrc, unsigned voff, unsigned soff) {
  asm volatile("buffer_load_dwordx4 %0, %1, %2, %3 offen offset:%c4 sc1" : "=a"(dst) : "v"(voff), "s"(rsrc), "s"(soff), "i"(IMM) : "memory");
}
template <int IMM> DEV void bload_v(v4u& dst, v4u rsrc, unsigned voff, unsigned soff) {
  asm volatile("buffer_load_dwordx4 %0, %1, %2, %3 offen offset:%c4 sc1" : "=v"(dst) : "v"(voff), "s"(rsrc), "s"(soff), "i"(IMM) : "memory");
}
DEV void bload1_v(unsigned& dst, v4u rsrc, unsigned voff, unsigned soff) {
  asm volatile("buffer_load_dword %0, %1, %2, %3 offen sc1" : "=v"(dst) : "v"(voff), "s"(rsrc), "s"(soff) : "memory");
}
template <int N> DEV void wait_vm_a(v4u& x) { asm volatile("s_waitcnt vmcnt(%c1)" : "+a"(x) : "i"(N) : "memory"); }
template <int N> DEV void wait_vm_a2(v4u& x, v4u& y) { asm volatile("s_waitcnt vmcnt(%c2)" : "+a"(x), "+a"(y) : "i"(N) : "memory"); }
template <int N> DEV void wait_vm_v2(v4u& x, v4u& y) { asm volatile("s_waitcnt vmcnt(%c2)" : "+v"(x), "+v"(y) : "i"(N) : "memory"); }
template <int N> DEV void wait_vm_v1(unsigned& x) { asm volatile("s_waitcnt vmcnt(%c1)" : "+v"(x) : "i"(N) : "memory"); }
#define PIN_V(x) asm volatile("" : "+v"(x))
#define PIN_A(x) asm volatile("" : "+a"(x))
#define WAIT_VM(n) asm volatile("s_waitcnt vmcnt(" #n ")" ::: "memory")
#define WAIT_VM_BAR(n) asm volatile("s_waitcnt vmcnt(" #n ") lgkmcnt(0)\n\ts_barrier" ::: "memory")
#define BAR_LDS() asm volatile("s_waitcnt lgkmcnt(0)\n\ts_barrier" ::: "memory")
#define WAIT_LGKM() asm volatile("s_waitcnt lgkmcnt(0)" ::: "memory")
#define SCHED() __builtin_amdgcn_sched_barrier(0)

DEV float xsum16_32(float v) {
  auto r = __builtin_amdgcn_permlane16_swap(__float_as_uint(v), __float_as_uint(v), false, false);
  v = __uint_as_float(r[0]) + __uint_as_float(r[1]);
  auto s = __builtin_amdgcn_permlane32_swap(__float_as_uint(v), __float_as_uint(v), false, false);
  return __uint_as_float(s[0]) + __uint_as_float(s[1]);
}
DEV float xmax16_32(float v) {
  auto r = __builtin_amdgcn_permlane16_swap(__float_as_uint(v), __float_as_uint(v), false, false);
  v = fmaxf(__uint_as_float(r[0]), __uint_as_float(r[1]));
  auto s = __builtin_amdgcn_permlane32_swap(__float_as_uint(v), __float_as_uint(v), false, false);
  return fmaxf(__uint_as_float(s[0]), __uint_as_float(s[1]));
}
DEV float xsum32(float v) {
  auto s = __builtin_amdgcn_permlane32_swap(__float_as_uint(v), __float_as_uint(v), false, false);
  return __uint_as_float(s[0]) + __uint_as_float(s[1]);
}

#define STAMP(i) do {} while (0)
#define TSARG
#define TSPASS

DEV GAS unsigned* flag_ptr(GAS unsigned* ctl, int ph, int dom, int j) { return ctl + FL_F + ((ph * 16 + dom) * 16 + j) * 32; }
DEV void wait_flags(GAS unsigned* word, unsigned tag) {
  for (unsigned spins = 0; spins < (1u << 20); ++spins) {
    const unsigned v = __hip_atomic_load(word, __ATOMIC_RELAXED, __HIP_MEMORY_SCOPE_AGENT);
    if (__all(v == tag)) break;
    __builtin_amdgcn_s_sleep(1);
  }
  __builtin_amdgcn_fence(__ATOMIC_ACQUIRE, "wavefront");
  asm volatile("" ::: "memory");
}

constexpr int P1_X = 0;
constexpr int P1_W = 81920;
constexpr int P1_WSLOT = 24576;
constexpr int P1_MISC = 155648;
constexpr int P2_W = 0;
constexpr int P2_X = 151936;
constexpr int CS_BUF = 160128;
constexpr int P3_UP = 14464;
constexpr int P3_CSP = 151936;
DEV constexpr int p3_slot_addr(int ds) { return (ds % 5) == 0 ? 65536 : (ds % 5) == 1 ? 94336 : (ds % 5) == 2 ? 123136 : (ds % 5) == 3 ? 0 : 28800; }
constexpr int P4_EARLY = 16384;
constexpr int P4_LATE = 65536, P4_WAVE = 24576;

DEV unsigned wrow_voff(int lane) { return (unsigned)(lane >> 3) * 1024u + (unsigned)((lane & 7) ^ (((lane >> 4) & 1) << 2)) * 16u; }

DEV void p1_xvoff(int tok0, int w, int lane, unsigned (&vx)[5]) {
#pragma unroll
  for (int i = 0; i < 5; ++i) {
    const int rg = 5 * (w & 1) + i;
    const int row = 8 * rg + (lane >> 3);
    int tok = tok0 + row; tok = tok < 0 ? 0 : tok;
    const int ch = (lane & 7) ^ ((row >> 1) & 7);
    vx[i] = (unsigned)tok * 1024u + (unsigned)ch * 16u + 4096u - (unsigned)((i - 2) * 1024);
  }
}
DEV void p1_x_setup(int sp0, int w) { set_m0((unsigned)(P1_X + (sp0 + (w >> 1)) * 10240 + (5 * (w & 1) + 2) * 1024)); }
template <int I> DEV void p1_issue_x1(const Params& p, int sp0, int w, int lane, const unsigned (&vx)[5]) {
  const unsigned char* base = (const unsigned char*)p.x + (sp0 + (w >> 1)) * 128 - 4096;
  if constexpr (I == 0) { if ((w & 1) || lane >= 8) dma16i<(I - 2) * 1024>(base, vx[I]); }
  else dma16i<(I - 2) * 1024>(base, vx[I]);
}
DEV void p1_wvoff(int w, int lane, unsigned (&vo)[6]) {
#pragma unroll
  for (int i = 0; i < 6; ++i) {
    const int idx = 6 * w + i;
    const int part = idx >> 3, rg = idx & 7;
    const int row = 8 * rg + (lane >> 3);
    const int f = ((row >> 1) & 1) | (((row >> 3) & 1) << 1);
    const int ch = (lane & 7) ^ (f << 1);
    vo[i] = (unsigned)row * 3072u + (unsigned)part * 1024u + (unsigned)ch * 16u + 4096u - (unsigned)((i - 3) * 1024);
  }
}
DEV void p1_w_setup(int slot, int w) { set_m0((unsigned)(P1_W + slot * P1_WSLOT + (6 * w + 3) * 1024)); }
template <int I> DEV void p1_issue_w1(const Params& p, int hg, int st, const unsigned (&vo)[6]) {
  const unsigned char* base = (const unsigned char*)p.wqkv + st * 64 * 3072 + hg * 128 - 4096;
  dma16i<(I - 3) * 1024>(base, vo[I]);
}
DEV void wrow_voffs(int lane, unsigned (&v)[4]) {
#pragma unroll
  for (int i = 0; i < 4; ++i) v[i] = wrow_voff(lane) + 7168u * i;
}
template <int I> DEV void wrow_issue(const u16* rowbase  , const unsigned (&v)[4]) { dma16i<I * 1024>(rowbase, v[I]); }

struct P1Frag { v4u xf[5]; v4u wq, wk, wv; };

DEV void phase1(const Params& p, LAS unsigned char* L, __amdgpu_buffer_rsrc_t rs, int p64, int hg, int w, int lane,
                const unsigned (&wvo)[6], const unsigned (&vx)[5] TSARG) {
  const int s0 = (p64 & 3) * 2;
  const int g = lane >> 4, tk = lane & 15, q4 = (lane & 15) >> 2, pp = lane & 3;
  const int fsw = ((q4 >> 1) & 1) | ((g & 1) << 1);
  const int wbase = P1_W + (8 * g + q4) * 128 + (((2 * w + (pp >> 1)) ^ (fsw << 1)) * 16) + 8 * (pp & 1);
  const int xsw = (tk >> 1) & 7;
  const int xb0 = P1_X + tk * 128 + ((g ^ xsw) * 16), xb1 = P1_X + tk * 128 + (((4 + g) ^ xsw) * 16);
  const int xbw = (w & 1) ? xb1 : xb0;

  f32x4 aq[5], ak[5], av[5], ssum[5];
  float s2[5];
#pragma unroll
  for (int t = 0; t < 5; ++t) {
    aq[t] = f32x4{0, 0, 0, 0}; ak[t] = f32x4{0, 0, 0, 0}; av[t] = f32x4{0, 0, 0, 0}; ssum[t] = f32x4{0, 0, 0, 0}; s2[t] = 0.f;
  }
  const v4u ones = {0x3f803f80u, 0x3f803f80u, 0x3f803f80u, 0x3f803f80u};

  WAIT_VM_BAR(6);
  STAMP(11);
  P1Frag cur;
  {
    LAS unsigned char* xs = L + s0 * 10240 + xb0;
    LAS unsigned char* wsl = L + wbase;
    cur.wk = tr_read2(wsl + 8192, wsl + 8192 + 512);
    cur.wv = tr_read2(wsl + 16384, wsl + 16384 + 512);
    cur.wq = tr_read2(wsl, wsl + 512);
#pragma unroll
    for (int t = 0; t < 5; ++t) cur.xf[t] = *(LAS v4u*)(xs + t * 2048);
  }
  SCHED();
  static_for<16>([&](auto KS) {
    constexpr int ks = KS;
    if constexpr (ks == 1) { WAIT_VM_BAR(5); }
    else if constexpr (ks == 3 || ks == 7 || ks == 11) { WAIT_VM_BAR(6); }
    else if constexpr (ks == 5 || ks == 9) { WAIT_VM_BAR(11); }
    else if constexpr (ks == 13) { WAIT_VM_BAR(0); }
    constexpr bool isx = (ks == 0 || ks == 4 || ks == 8);
    constexpr int wst = ks == 1 ? 2 : ks == 2 ? 3 : ks == 3 ? 4 : ks == 5 ? 5 : ks == 7 ? 6 : ks == 9 ? 7 : -1;
    if constexpr (isx) p1_x_setup((s0 + 2 + ks / 2) & 7, w);
    if constexpr (wst >= 0) p1_w_setup(wst % 3, w);
    auto dma = [&](auto D) {
      constexpr int d = D;
      if constexpr (isx && d < 5) p1_issue_x1<d>(p, (s0 + 2 + ks / 2) & 7, w, lane, vx);
      if constexpr (wst >= 0) p1_issue_w1<d>(p, hg, (s0 + wst) & 7, wvo);
    };
    P1Frag nxt;
    constexpr int kn = ks + 1, itn = kn >> 1, k2n = kn & 1;
    LAS unsigned char* xs = L + ((s0 + itn) & 7) * 10240 + (k2n ? xb1 : xb0);
    LAS unsigned char* wsl = L + wbase + (itn % 3) * P1_WSLOT + k2n * 4096;
    constexpr int G = ks >> 2, sub = ks & 3;
    LAS unsigned char* sx = L + ((s0 + 2 * G + (w >> 1)) & 7) * 10240 + xbw;
    v4u sva, svb;
    dma(std::integral_constant<int, 0>{});
    static_for<5>([&](auto T) {
      constexpr int t = T;
      if constexpr (ks < 15) {
        if constexpr (t == 0) nxt.wk = tr_read2(wsl + 8192, wsl + 8192 + 512);
        if constexpr (t == 1) nxt.wv = tr_read2(wsl + 16384, wsl + 16384 + 512);
        if constexpr (t == 2) nxt.wq = tr_read2(wsl, wsl + 512);
        nxt.xf[t] = *(LAS v4u*)(xs + t * 2048);
      }
      if constexpr (t == 0) sva = *(LAS v4u*)(sx + sub * 2048);
      if constexpr (t == 1 && sub == 3) svb = *(LAS v4u*)(sx + 4 * 2048);
      ak[t] = mfma16(cur.wk, cur.xf[t], ak[t]);
      av[t] = mfma16(cur.xf[t], cur.wv, av[t]);
      aq[t] = mfma16(cur.wq, cur.xf[t], aq[t]);
      if constexpr (t == 4) {
        ssum[sub] = mfma16(ones, sva, ssum[sub]);
#pragma unroll
        for (int e = 0; e < 4; ++e) s2[sub] = dot2(sva[e], sva[e], s2[sub]);
        if constexpr (sub == 3) {
          ssum[4] = mfma16(ones, svb, ssum[4]);
#pragma unroll
          for (int e = 0; e < 4; ++e) s2[4] = dot2(svb[e], svb[e], s2[4]);
        }
      }
      SCHED();
      dma(std::integral_constant<int, t + 1>{});
      SCHED();
    });
    if constexpr (ks < 15) cur = nxt;
  });
  STAMP(12);
  LAS f32x2* part = (LAS f32x2*)(L + P1_MISC);
  LAS float* fmean = (LAS float*)(L + P1_MISC + 2560 + w * 640);
  LAS float* frstd = fmean + 80;
#pragma unroll
  for (int t = 0; t < 5; ++t) {
    const float b = xsum16_32(s2[t]);
    if (g == 0) part[w * 80 + t * 16 + tk] = f32x2{ssum[t][0], b};
  }
  BAR_LDS();
  STAMP(28);
  unsigned wv4[4];
  wrow_voffs(lane, wv4);
  const u16* wob = p.wo + (128 * w) * 512 + hg * 64;
  auto dmo = [&](auto G) {
    constexpr int gq = G;
    set_m0((unsigned)(P2_W + (128 * w + 32 * gq) * 128));
    static_for<4>([&](auto I) { wrow_issue<I>(wob + gq * 32 * 512, wv4); });
  };
  dmo(std::integral_constant<int, 0>{});
#pragma unroll
  for (int r = 0; r < 2; ++r) {
    const int tok = r * 64 + lane;
    if (tok < 80) {
      const f32x2 a = (part[tok] + part[80 + tok]) + (part[160 + tok] + part[240 + tok]);
      const float mean = a.x * (1.f / 512.f);
      const float var = fmaxf(a.y * (1.f / 512.f) - mean * mean, 0.f);
      fmean[tok] = mean; frstd[tok] = __builtin_amdgcn_rsqf(var + 1e-5f);
    }
  }
  f32x2 csq[2], csk[2];
#pragma unroll
  for (int r = 0; r < 4; ++r) {
    csq[r >> 1][r & 1] = __uint_as_float(__builtin_amdgcn_update_dpp(0, __float_as_uint(aq[0][r]), 0x150, 0xf, 0xf, false));
    csk[r >> 1][r & 1] = __uint_as_float(__builtin_amdgcn_update_dpp(0, __float_as_uint(ak[0][r]), 0x150, 0xf, 0xf, false));
  }
  float csv;
  {
    auto a = __builtin_amdgcn_permlane16_swap(__float_as_uint(av[0][0]), __float_as_uint(av[0][0]), false, false);
    auto c = __builtin_amdgcn_permlane32_swap(a[0], a[0], false, false);
    csv = __uint_as_float(c[0]);
  }
  dmo(std::integral_constant<int, 1>{});
  WAIT_LGKM();
  float m1[5], r1[5];
  f32x4 mv[5], rv[5];
#pragma unroll
  for (int t = 0; t < 5; ++t) {
    m1[t] = fmean[t * 16 + tk]; r1[t] = frstd[t * 16 + tk];
    mv[t] = *(LAS f32x4*)(fmean + t * 16 + 4 * g); rv[t] = *(LAS f32x4*)(frstd + t * 16 + 4 * g);
  }
  SCHED();
  v2u qp[4], kp[5], vp[5];
  const float sc = 0.25f * 1.44269504088896f;
  const f32x2 c2 = {csv, csv};
#pragma unroll
  for (int t = 0; t < 5; ++t) {
    const f32x2 m2 = {m1[t], m1[t]}, r2 = {r1[t], r1[t]};
    kp[t].x = cvt2((pk2(ak[t][0], ak[t][1]) - m2 * csk[0]) * r2);
    kp[t].y = cvt2((pk2(ak[t][2], ak[t][3]) - m2 * csk[1]) * r2);
    if (t > 0) {
      qp[t - 1].x = cvt2(pk2(aq[t][0], aq[t][1]) - m2 * csq[0]);
      qp[t - 1].y = cvt2(pk2(aq[t][2], aq[t][3]) - m2 * csq[1]);
    }
  }
  SCHED();
  STAMP(29);
  const f32x4 z = {0, 0, 0, 0};
  f32x4 sp[4], scu[4];
#pragma unroll
  for (int t = 0; t < 4; ++t) { sp[t] = mfma16k16(kp[t], qp[t], z); scu[t] = mfma16k16(kp[t + 1], qp[t], z); }
  SCHED();
  dmo(std::integral_constant<int, 2>{});
#pragma unroll
  for (int t = 0; t < 5; ++t) {
    vp[t].x = cvt2((pk2(av[t][0], av[t][1]) - pk2(mv[t][0], mv[t][1]) * c2) * pk2(rv[t][0], rv[t][1]));
    vp[t].y = cvt2((pk2(av[t][2], av[t][3]) - pk2(mv[t][2], mv[t][3]) * c2) * pk2(rv[t][2], rv[t][3]));
  }
  SCHED();
  f32x2 biasp[2], biasc[2];
#pragma unroll
  for (int r = 0; r < 4; ++r) {
    const int dc = tk - 4 * g - r, dp = dc + 16;
    biasc[r >> 1][r & 1] = (dc >= 0 && dc <= 14 && !(dc & 1)) ? 0.f : -1e30f;
    biasp[r >> 1][r & 1] = (dp <= 14 && !(dp & 1)) ? 0.f : -1e30f;
  }
  f32x2 a[4][4];
  float mx[4], sm[4];
#pragma unroll
  for (int t = 0; t < 4; ++t) {
    const float sq = sc * r1[t + 1];
    const f32x2 s2v = {sq, sq};
    f32x2 bp0 = biasp[0], bp1 = biasp[1];
    if (t == 0 && p64 == 0) { bp0 = f32x2{-1e30f, -1e30f}; bp1 = bp0; }
    a[t][0] = pk2(sp[t][0], sp[t][1]) * s2v + bp0; a[t][1] = pk2(sp[t][2], sp[t][3]) * s2v + bp1;
    a[t][2] = pk2(scu[t][0], scu[t][1]) * s2v + biasc[0]; a[t][3] = pk2(scu[t][2], scu[t][3]) * s2v + biasc[1];
  }
#pragma unroll
  for (int t = 0; t < 4; ++t)
    mx[t] = fmaxf(fmaxf(__builtin_fmaxf(a[t][0].x, a[t][0].y), fmaxf(a[t][1].x, a[t][1].y)), fmaxf(fmaxf(a[t][2].x, a[t][2].y), fmaxf(a[t][3].x, a[t][3].y)));
  SCHED();
  auto xred2 = [&](float& A, float& B, auto op) {
    auto s1 = __builtin_amdgcn_permlane16_swap(__float_as_uint(A), __float_as_uint(B), false, false);
    const float u = op(__uint_as_float(s1[0]), __uint_as_float(s1[1]));
    auto s2_ = __builtin_amdgcn_permlane32_swap(__float_as_uint(u), __float_as_uint(u), false, false);
    const float v = op(__uint_as_float(s2_[0]), __uint_as_float(s2_[1]));
    auto s3 = __builtin_amdgcn_permlane16_swap(__float_as_uint(v), __float_as_uint(v), false, false);
    A = __uint_as_float(s3[0]); B = __uint_as_float(s3[1]);
  };
  auto fmx = [](float x, float y) { return fmaxf(x, y); };
  auto fad = [](float x, float y) { return x + y; };
  STAMP(30);
  xred2(mx[0], mx[1], fmx); xred2(mx[2], mx[3], fmx);
  SCHED();
  dmo(std::integral_constant<int, 3>{});
#pragma unroll
  for (int t = 0; t < 4; ++t) {
    const f32x2 m2 = {mx[t], mx[t]};
#pragma unroll
    for (int q = 0; q < 4; ++q) a[t][q] -= m2;
  }
#pragma unroll
  for (int t = 0; t < 4; ++t)
#pragma unroll
    for (int q = 0; q < 4; ++q) { a[t][q].x = __builtin_amdgcn_exp2f(a[t][q].x); a[t][q].y = __builtin_amdgcn_exp2f(a[t][q].y); }
  SCHED();
  v4u pf[4];
#pragma unroll
  for (int t = 0; t < 4; ++t) {
    const f32x2 sv = (a[t][0] + a[t][1]) + (a[t][2] + a[t][3]);
    sm[t] = sv.x + sv.y;
    pf[t] = v4u{cvt2(a[t][0]), cvt2(a[t][1]), cvt2(a[t][2]), cvt2(a[t][3])};
  }
  f32x4 o[4];
#pragma unroll
  for (int t = 0; t < 4; ++t) {
    const v4u vf = {vp[t].x, vp[t].y, vp[t + 1].x, vp[t + 1].y};
    o[t] = mfma16(vf, pf[t], z);
  }
  SCHED();
  STAMP(31);
  xred2(sm[0], sm[1], fad); xred2(sm[2], sm[3], fad);
  v2u ob[4];
#pragma unroll
  for (int t = 0; t < 4; ++t) {
    const float inv = __builtin_amdgcn_rcpf(sm[t]);
    const f32x2 i2 = {inv, inv};
    ob[t] = v2u{cvt2(pk2(o[t][0], o[t][1]) * i2), cvt2(pk2(o[t][2], o[t][3]) * i2)};
  }
  const int head = 4 * hg + w;
#pragma unroll
  for (int u = 0; u < 2; ++u) {
    auto sx = __builtin_amdgcn_permlane16_swap(ob[2 * u].x, ob[2 * u + 1].x, false, false);
    auto sy = __builtin_amdgcn_permlane16_swap(ob[2 * u].y, ob[2 * u + 1].y, false, false);
    const v4u o4 = {sx[0], sy[0], sx[1], sy[1]};
    const int voff = ((g >> 1) * 32 + (g & 1) * 16 + tk) * 16;
    __builtin_amdgcn_raw_buffer_store_b128(o4, rs, voff, (int)WS_O + ((2 * p64 + u) * 32 + head) * 1024, AUX_SC1);
  }
}

DEV void cs_load(const Params& p, int c, int r, int tid, v4u (&v)[4]) {
  const int rg = tid >> 5, c0 = tid & 31, cc = c0 < 28 ? c0 : 27;
  const u16* base = (cc < 14 ? p.wg : p.wu) + (size_t)(32 * r + 4 * rg) * 1792 + 112 * c + (cc < 14 ? cc : cc - 14) * 8;
#pragma unroll
  for (int i = 0; i < 4; ++i) v[i] = *(const GAS v4u*)(base + i * 1792);
}
DEV void cs_finish(LAS unsigned char* L, __amdgpu_buffer_rsrc_t rs, int c, int r, int w, int lane, const v4u (&v)[4]) {
  LAS float* buf = (LAS float*)(L + CS_BUF);
  const int c0 = lane & 31;
  f32x4 a, b;
#pragma unroll
  for (int e = 0; e < 2; ++e) {
    a[2 * e] = xsum32((bf_lo(v[0][e]) + bf_lo(v[1][e])) + (bf_lo(v[2][e]) + bf_lo(v[3][e])));
    a[2 * e + 1] = xsum32((bf_hi(v[0][e]) + bf_hi(v[1][e])) + (bf_hi(v[2][e]) + bf_hi(v[3][e])));
    b[2 * e] = xsum32((bf_lo(v[0][2 + e]) + bf_lo(v[1][2 + e])) + (bf_lo(v[2][2 + e]) + bf_lo(v[3][2 + e])));
    b[2 * e + 1] = xsum32((bf_hi(v[0][2 + e]) + bf_hi(v[1][2 + e])) + (bf_hi(v[2][2 + e]) + bf_hi(v[3][2 + e])));
  }
  if (lane < 28) {
    *(LAS f32x4*)(buf + w * 224 + c0 * 8) = a;
    *(LAS f32x4*)(buf + w * 224 + c0 * 8 + 4) = b;
  }
  BAR_LDS();
  const int tid = w * 64 + lane;
  if (tid < 224) {
    const float s = (buf[tid] + buf[224 + tid]) + (buf[448 + tid] + buf[672 + tid]);
    __builtin_amdgcn_raw_buffer_store_b32(__float_as_uint(s), rs, tid * 4, (int)WS_CS + (c * 16 + r) * 896, AUX_SC1);
  }
}

DEV void p3_voffs(int lane, unsigned (&vo)[7]) {
#pragma unroll
  for (int i = 0; i < 7; ++i) {
    const int q = i * 64 + lane, row = q / 14, cc = q - 14 * row;
    vo[i] = (unsigned)row * 3584u + (unsigned)cc * 16u + 4096u - (unsigned)((i - 3) * 1024);
  }
}
DEV void p3_w_setup(int ds, int w) { set_m0((unsigned)(p3_slot_addr(ds) + (w & 1) * P3_UP + (w >> 1) * 7168 + 3 * 1024)); }
template <int I> DEV void p3_issue_w1(const Params& p, int c, int ds, int w, const unsigned (&vo)[7]) {
  const unsigned char* base = (const unsigned char*)((w & 1) ? p.wu : p.wg) + (64 * ds + 32 * (w >> 1)) * 3584 + 224 * c - 4096;
  dma16i<(I - 3) * 1024>(base, vo[I]);
}
DEV void p3_issue_w(const Params& p, int c, int ds, int w, const unsigned (&vo)[7]) {
  p3_w_setup(ds, w);
  static_for<7>([&](auto I) { p3_issue_w1<I>(p, c, ds, w, vo); });
}

DEV void p2_issue_x(const Params& p, int p64, int nt, int w, int lane) {
  const unsigned vx = (unsigned)(lane >> 3) * 1024u + (unsigned)(((lane & 7) ^ (lane >> 3)) * 16);
  const u16* xb = p.x + (64 * p64 + 16 * w) * 512 + nt * 64;
  set_m0((unsigned)(P2_X + 16 * w * 128));
  dma16i<0>(xb, vx);
  dma16i<1024>(xb, vx + 7168u);
}

template <int WSTRIDE>
DEV f32x16 reduce4(f32x16 (&acc)[2][2], LAS unsigned char* red, int w, int lane) {
  WAIT_LGKM();
#pragma unroll
  for (int a = 0; a < 2; ++a)
#pragma unroll
    for (int b = 0; b < 2; ++b) {
      const int dest = a + 2 * b;
      if (dest != w) {
        const int slot = (dest - w - 1) & 3;
#pragma unroll
        for (int qd = 0; qd < 4; ++qd)
          *(LAS f32x4*)(red + w * WSTRIDE + (slot * 4 + qd) * 1024 + lane * 16) =
              f32x4{acc[a][b][4 * qd], acc[a][b][4 * qd + 1], acc[a][b][4 * qd + 2], acc[a][b][4 * qd + 3]};
      }
    }
  BAR_LDS();
  f32x16 r;
  if (w == 0) r = acc[0][0]; else if (w == 1) r = acc[1][0]; else if (w == 2) r = acc[0][1]; else r = acc[1][1];
#pragma unroll
  for (int s = 1; s < 4; ++s) {
    const int src = (w + s) & 3;
#pragma unroll
    for (int qd = 0; qd < 4; ++qd) {
      const f32x4 v = *(LAS f32x4*)(red + src * WSTRIDE + ((3 - s) * 4 + qd) * 1024 + lane * 16);
#pragma unroll
      for (int e = 0; e < 4; ++e) r[4 * qd + e] += v[e];
    }
  }
  return r;
}

DEV void phase2(const Params& p, LAS unsigned char* L, __amdgpu_buffer_rsrc_t rs, int p64, int nt, int w, int lane, v4u (&hkeep)[2],
                int c3, const unsigned (&vo)[7] TSARG) {
  v4u of[2][8];
#pragma unroll
  for (int i = 0; i < 8; ++i)
#pragma unroll
    for (int tt = 0; tt < 2; ++tt)
      of[tt][i] = __builtin_amdgcn_raw_buffer_load_b128(rs, lane * 16, (int)WS_O + ((2 * p64 + tt) * 32 + 8 * w + i) * 1024, AUX_SC1);
  SCHED();
  const int hh = lane >> 5, blk = (lane >> 4) & 1, q4 = (lane & 15) >> 2, pp = lane & 3;
  const int sw = ((q4 >> 1) & 1) << 2;
  LAS unsigned char* wb0 = L + P2_W + (128 * w + 8 * hh + q4) * 128 + (((2 * blk + (pp >> 1)) ^ sw) * 16) + 8 * (pp & 1);
  LAS unsigned char* wb1 = L + P2_W + (128 * w + 8 * hh + q4) * 128 + (((4 + 2 * blk + (pp >> 1)) ^ sw) * 16) + 8 * (pp & 1);
  f32x16 acc[2][2];
#pragma unroll
  for (int a = 0; a < 2; ++a)
#pragma unroll
    for (int b = 0; b < 2; ++b)
#pragma unroll
      for (int e = 0; e < 16; ++e) acc[a][b][e] = 0.f;
  v4u wa[8][2];
#pragma unroll
  for (int i = 0; i < 8; ++i) {
    wa[i][0] = tr_read2(wb0 + i * 2048, wb0 + i * 2048 + 512);
    wa[i][1] = tr_read2(wb1 + i * 2048, wb1 + i * 2048 + 512);
  }
  SCHED();
#pragma unroll
  for (int i = 0; i < 8; ++i) {
    acc[0][0] = mfma32(wa[i][0], of[0][i], acc[0][0]);
    acc[1][0] = mfma32(wa[i][1], of[0][i], acc[1][0]);
    acc[0][1] = mfma32(wa[i][0], of[1][i], acc[0][1]);
    acc[1][1] = mfma32(wa[i][1], of[1][i], acc[1][1]);
  }
  SCHED();
  STAMP(13);
  p3_issue_w(p, c3, 2, w, vo);
  asm volatile("s_waitcnt vmcnt(7)" ::: "memory");
  f32x16 r = reduce4<16384>(acc, L + P2_W, w, lane);
  const int row = 32 * (w >> 1) + (lane & 31);
  float hv[16];
  float s1 = 0.f, s2 = 0.f;
#pragma unroll
  for (int qd = 0; qd < 4; ++qd) {
    const v2u xr = *(LAS v2u*)(L + P2_X + row * 128 + (((4 * (w & 1) + qd) ^ (row & 7)) * 16) + 8 * hh);
    const unsigned y01 = pk_bf16(r[4 * qd + 0], r[4 * qd + 1]), y23 = pk_bf16(r[4 * qd + 2], r[4 * qd + 3]);
    const unsigned h01 = pk_bf16(bf_lo(xr.x) + bf_lo(y01), bf_hi(xr.x) + bf_hi(y01));
    const unsigned h23 = pk_bf16(bf_lo(xr.y) + bf_lo(y23), bf_hi(xr.y) + bf_hi(y23));
    hkeep[qd >> 1][2 * (qd & 1)] = h01; hkeep[qd >> 1][2 * (qd & 1) + 1] = h23;
    hv[4 * qd + 0] = bf_lo(h01); hv[4 * qd + 1] = bf_hi(h01); hv[4 * qd + 2] = bf_lo(h23); hv[4 * qd + 3] = bf_hi(h23);
  }
  const int tile32 = 2 * p64 + (w >> 1);
  const int nt32 = 2 * nt + (w & 1);
#pragma unroll
  for (int ks = 0; ks < 2; ++ks)
    __builtin_amdgcn_raw_buffer_store_b128(hkeep[ks], rs, lane * 16, (int)WS_H + (tile32 * 32 + 2 * nt32 + ks) * 1024, AUX_SC1);
#pragma unroll
  for (int i = 0; i < 16; ++i) { s1 += hv[i]; s2 += hv[i] * hv[i]; }
  s1 = xsum32(s1); s2 = xsum32(s2);
  if (hh == 0) {
    v2u st = {__float_as_uint(s1), __float_as_uint(s2)};
    __builtin_amdgcn_raw_buffer_store_b64(st, rs, (lane & 31) * 16, (int)WS_HS + (tile32 * 8 + (nt32 >> 1)) * 512 + (nt32 & 1) * 8, AUX_SC1);
  }
}

constexpr int P3_KM = 20;
constexpr int p3_nl(int ks) { return ks < 12 ? 2 : 0; }
constexpr int p3_nd(int ks) { return (ks >= 4 && ks < 16) ? ((ks & 3) == 3 ? 1 : 2) : 0; }
constexpr int p3_nx(int ks) { return (ks == 2 || (ks >= 12 && ks < 16)) ? 1 : 0; }
constexpr int p3_cum(int ks) { int c = 12; for (int k = 0; k < ks; ++k) c += p3_nl(k) + p3_nd(k) + p3_nx(k); return c; }
constexpr int p3_pos(int i) { return i < 8 ? 4 + i : p3_cum((i - 8) / 2) + ((i - 8) & 1); }

DEV void phase3(const Params& p, LAS unsigned char* L, __amdgpu_buffer_rsrc_t rs, v4u rsv, GAS unsigned* ctl, unsigned tag, int p128, int c, int w, int lane,
                int nt4, const unsigned (&vo)[7], v4u (&wr)[8][2] TSARG) {
  const int tile32 = 4 * p128 + w;
  const int hh = lane >> 5, blk = (lane >> 4) & 1, q4 = (lane & 15) >> 2, pp = lane & 3;
  v4u hb[32];
  v4u hs[4];
  const unsigned hbase = WS_H + tile32 * 32768;
  static_for<4>([&](auto I) { bload_v<I * 512>(hs[I], rsv, (4 * hh) * 512 + (lane & 31) * 16, WS_HS + tile32 * 4096); });
  static_for<8>([&](auto I) { bload_a<(I & 3) * 1024>(hb[I], rsv, lane * 16, hbase + (I >> 2) * 4096); });
  SCHED();
  const int abase = blk * P3_UP + (4 * hh + q4) * 224 + pp * 8;
  int ab0 = abase, ab1 = abase + 65536, ab2 = abase + 123136;
  PIN_V(ab0); PIN_V(ab1); PIN_V(ab2);
  auto slot_ptr = [&](int ds) -> LAS unsigned char* {
    const int a = p3_slot_addr(ds);
    return a == 123136 ? L + ab2 : a >= 65536 ? L + ab1 + (a - 65536) : L + ab0 + a;
  };
  f32x16 acc[7];
#pragma unroll
  for (int t = 0; t < 7; ++t)
#pragma unroll
    for (int e = 0; e < 16; ++e) acc[t][e] = 0.f;
  WAIT_VM_BAR(12);
  STAMP(14);
  v4u an[7];
  {
    LAS unsigned char* sl = slot_ptr(0);
#pragma unroll
    for (int t = 0; t < 7; ++t) an[t] = tr_read2(sl + t * 32, sl + t * 32 + 8 * 224);
  }
  SCHED();
  unsigned csflag = 0;
  v4u csl[4];
  v4u fn[12];
  LAS float* cspart = (LAS float*)(L + P3_CSP);
  int cspo = P3_CSP + 896 * 4 + 16 * hh; PIN_V(cspo);
  LAS float* csp = cspart + 896;
  LAS float* csq_ = (LAS float*)(L + cspo);
  float mean = 0.f, rstd = 0.f;
  static_for<P3_KM>([&](auto KS) {
    constexpr int ks = KS;
    v4u ac[7];
#pragma unroll
    for (int t = 0; t < 7; ++t) ac[t] = an[t];
    if constexpr (ks == 19) { WAIT_VM_BAR(0); }
    else wait_vm_a<p3_cum(ks) - 1 - p3_pos(ks)>(hb[ks]);
    if constexpr (ks == 19) {
      PIN_A(hb[19]);
#pragma unroll
      for (int k = P3_KM; k < 32; ++k) PIN_A(hb[k]);
    }
    constexpr int ds = (ks + 1) >> 2, ksl = (ks + 1) & 3;
    LAS unsigned char* sl = slot_ptr(ds) + ksl * 16 * 224;
    auto vm = [&](auto SL) {
      constexpr int slot = SL, nl = p3_nl(ks), nd = p3_nd(ks), nx = p3_nx(ks);
      if constexpr (slot < nl) {
        constexpr int i = 8 + 2 * ks + slot;
        bload_a<(i & 3) * 1024>(hb[i], rsv, lane * 16, hbase + (i >> 2) * 4096);
      } else if constexpr (slot < nl + nd) {
        constexpr int i = 2 * (ks & 3) + (slot - nl);
        if constexpr (i == 0) p3_w_setup(5 + ((ks - 4) >> 2), w);
        p3_issue_w1<i>(p, c, 5 + ((ks - 4) >> 2), w, vo);
      } else if constexpr (slot < nl + nd + nx) {
        if constexpr (ks == 2) bload1_v(csflag, rsv, (unsigned)(FL_F + ((16 + (lane & 15)) * 16 + c) * 32) * 4u, 0u);
        else {
          const unsigned vof = (unsigned)(lane < 56 ? lane : 55) * 16u;
          bload_v<(ks - 12) * 896>(csl[ks - 12], rsv, vof, WS_CS + (unsigned)(c * 16 + 4 * w) * 896u);
        }
      }
    };
    if constexpr (ks == 11) {
      wait_vm_v1<p3_cum(11) - 1 - (p3_cum(2) + p3_nl(2) + p3_nd(2))>(csflag);
      if (!__all(csflag == tag)) wait_flags(flag_ptr(ctl, 1, lane & 15, c), tag);
    }
    if constexpr (ks == 13) {
      float s1 = 0.f, s2 = 0.f;
#pragma unroll
      for (int i = 0; i < 4; ++i) {
        PIN_V(hs[i]);
        s1 += __uint_as_float(hs[i].x) + __uint_as_float(hs[i].z);
        s2 += __uint_as_float(hs[i].y) + __uint_as_float(hs[i].w);
      }
      s1 = xsum32(s1); s2 = xsum32(s2);
      mean = s1 * (1.f / 512.f);
      rstd = __builtin_amdgcn_rsqf(fmaxf(s2 * (1.f / 512.f) - mean * mean, 0.f) + 1e-5f);
    }
    if constexpr (ks == 18) {
      asm volatile("s_waitcnt vmcnt(0)" : "+v"(csl[0]), "+v"(csl[1]), "+v"(csl[2]), "+v"(csl[3]) :: "memory");
      f32x4 a;
#pragma unroll
      for (int e = 0; e < 4; ++e)
        a[e] = (__uint_as_float(csl[0][e]) + __uint_as_float(csl[1][e])) + (__uint_as_float(csl[2][e]) + __uint_as_float(csl[3][e]));
      if (lane < 56) *(LAS f32x4*)(cspart + w * 224 + lane * 4) = a;
    }
    static_for<7>([&](auto T) {
      constexpr int t = T;
      if constexpr (t == 0 || t == 2) asm volatile("" : "+v"(ac[t]), "+v"(ac[t + 1]));
      if constexpr (t == 4) asm volatile("" : "+v"(ac[4]), "+v"(ac[5]), "+v"(ac[6]));
      acc[t] = mfma32(ac[t], hb[ks], acc[t]);
      SCHED();
      if constexpr (ks + 1 < P3_KM) {
        if constexpr (t == 1 || t == 3 || t == 5) {
          an[t - 1] = tr_read2(sl + (t - 1) * 32, sl + (t - 1) * 32 + 8 * 224);
          an[t] = tr_read2(sl + t * 32, sl + t * 32 + 8 * 224);
        }
        if constexpr (t == 6) an[6] = tr_read2(sl + 6 * 32, sl + 6 * 32 + 8 * 224);
      } else {
        static_for<2>([&](auto Q) {
          constexpr int k = 2 * t + Q;
          if constexpr (k < 12) {
            constexpr int k2 = P3_KM + k;
            LAS unsigned char* s2p = slot_ptr(k2 >> 2) + (k2 & 3) * 16 * 224;
            fn[k] = tr_read2(s2p, s2p + 8 * 224);
          }
        });
      }
      if constexpr (t == 0 || t == 2 || t == 4 || t == 6) vm(std::integral_constant<int, (t >> 1)>{});
      SCHED();
    });
    if constexpr ((ks & 3) == 3 && (ks >> 2) < 3) BAR_LDS();
  });
  STAMP(15);
  const f32x2 nm2 = {-mean, -mean};
  const float r2s = rstd * rstd;
  const f32x2 k1 = {-2.302208198f * rstd, -2.302208198f * rstd}, k2 = {-0.1029432397f * rstd * r2s, -0.1029432397f * rstd * r2s};
  const f32x2 rr2 = {r2s, r2s}, one2 = {1.f, 1.f};
  f32x2 yg[4], yu[4], zz[4], rc[4];
  f32x4 cgu[4];
  unsigned gw[4];
  auto epi_piece = [&](auto T, auto K) {
    constexpr int t = T, k = K;
    auto csg = [&](int i) { return pk2(cgu[i >> 1][2 * (i & 1)], cgu[i >> 1][2 * (i & 1) + 1]); };
    auto csu = [&](int i) { return pk2(cgu[2 + (i >> 1)][2 * (i & 1)], cgu[2 + (i >> 1)][2 * (i & 1) + 1]); };
    auto ex2 = [&](int i) { zz[i].x = __builtin_amdgcn_exp2f(zz[i].x); zz[i].y = __builtin_amdgcn_exp2f(zz[i].y); };
    auto rc2 = [&](int i) { rc[i].x = __builtin_amdgcn_rcpf(zz[i].x); rc[i].y = __builtin_amdgcn_rcpf(zz[i].y); };
    if constexpr (k == 0) {
      cgu[0] = *(LAS f32x4*)(csq_ + t * 16);
      cgu[1] = *(LAS f32x4*)(csq_ + t * 16 + 8);
      cgu[2] = *(LAS f32x4*)(csq_ + 112 + t * 16);
      cgu[3] = *(LAS f32x4*)(csq_ + 112 + t * 16 + 8);
    } else if constexpr (k == 1) {
      PIN_A(acc[t]);
#pragma unroll
      for (int i = 0; i < 4; ++i) { yg[i] = pk2(acc[t][2 * i], acc[t][2 * i + 1]); PIN_V(yg[i]); }
    } else if constexpr (k == 2) {
#pragma unroll
      for (int i = 0; i < 4; ++i) { yu[i] = pk2(acc[t][8 + 2 * i], acc[t][8 + 2 * i + 1]); PIN_V(yu[i]); }
    } else if constexpr (k == 3) {
#pragma unroll
      for (int i = 0; i < 4; ++i) yg[i] = nm2 * csg(i) + yg[i];
      yu[0] = nm2 * csu(0) + yu[0]; yu[1] = nm2 * csu(1) + yu[1];
    } else if constexpr (k == 4) {
#pragma unroll
      for (int i = 0; i < 4; ++i) zz[i] = yg[i] * yg[i];
      yu[2] = nm2 * csu(2) + yu[2]; yu[3] = nm2 * csu(3) + yu[3];
    } else if constexpr (k == 5) {
#pragma unroll
      for (int i = 0; i < 4; ++i) zz[i] = zz[i] * k2 + k1;
      yu[0] = yu[0] * yg[0]; yu[1] = yu[1] * yg[1];
    } else if constexpr (k == 6) {
#pragma unroll
      for (int i = 0; i < 4; ++i) zz[i] = zz[i] * yg[i];
      yu[2] = yu[2] * yg[2]; yu[3] = yu[3] * yg[3];
    } else if constexpr (k == 7) {
      ex2(0); ex2(1);
      yu[0] = yu[0] * rr2; yu[1] = yu[1] * rr2;
    } else if constexpr (k == 8) {
      ex2(2); ex2(3);
      zz[0] += one2; zz[1] += one2;
    } else if constexpr (k == 9) {
      rc2(0); rc2(1);
      zz[2] += one2; zz[3] += one2;
    } else if constexpr (k == 10) {
      rc2(2); rc2(3);
      yu[2] = yu[2] * rr2; yu[3] = yu[3] * rr2;
      yu[0] = yu[0] * rc[0]; yu[1] = yu[1] * rc[1];
    } else {
      yu[2] = yu[2] * rc[2]; yu[3] = yu[3] * rc[3];
#pragma unroll
      for (int i = 0; i < 4; ++i) gw[i] = cvt2(yu[i]);
      const v4u f = {gw[0], gw[1], gw[2], gw[3]};
      __builtin_amdgcn_raw_buffer_store_b128(f, rs, lane * 16, (int)WS_G + (tile32 * 112 + 7 * c + t) * 1024, AUX_SC1);
    }
  };
  unsigned wv4[4];
  wrow_voffs(lane, wv4);
  const u16* wdb = p.wd + (448 * w) * 512 + nt4 * 64;
  const int sw4 = ((q4 >> 1) & 1) << 2;
  LAS unsigned char* wb0 = L + w * P4_EARLY + (4 * hh + q4) * 128 + (((2 * blk + (pp >> 1)) ^ sw4) * 16) + 8 * (pp & 1);
  LAS unsigned char* wb1 = L + w * P4_EARLY + (4 * hh + q4) * 128 + (((4 + 2 * blk + (pp >> 1)) ^ sw4) * 16) + 8 * (pp & 1);
  SCHED();
  STAMP(18);
  static_for<7>([&](auto T) {
    constexpr int t = T;
    v4u fc[12];
#pragma unroll
    for (int k = 0; k < 12; ++k) fc[k] = fn[k];
    if constexpr (t == 4) WAIT_VM(3);
    static_for<12>([&](auto K) {
      constexpr int k = K;
      acc[t] = mfma32(fc[k], hb[P3_KM + k], acc[t]);
      SCHED();
      if constexpr (t < 6) {
        constexpr int ks = P3_KM + k, ds = ks >> 2, ksl = ks & 3;
        LAS unsigned char* sl = slot_ptr(ds) + ksl * 16 * 224 + (t + 1) * 32;
        fn[k] = tr_read2(sl, sl + 8 * 224);
      }
      if constexpr (t > 0) epi_piece(std::integral_constant<int, t - 1>{}, K);
      if constexpr (t == 0 && k == 0 ) {
        if (lane < 56) {
          const f32x4 a = (*(LAS f32x4*)(cspart + lane * 4) + *(LAS f32x4*)(cspart + 224 + lane * 4)) +
                          (*(LAS f32x4*)(cspart + 448 + lane * 4) + *(LAS f32x4*)(cspart + 672 + lane * 4));
          *(LAS f32x4*)(csp + lane * 4) = a;
        }
      }
      if constexpr (t < 2) {
        if constexpr (k >= 2 && k < 10) {
          constexpr int n = 8 * t + (k - 2), gq = n >> 2;
          if constexpr ((n & 3) == 0) set_m0((unsigned)(w * P4_EARLY + gq * 4096));
          wrow_issue<(n & 3)>(wdb + gq * 32 * 512, wv4);
        }
      }
      if constexpr (t == 4 && k < 8) {
        wr[k][0] = tr_read2(wb0 + k * 2048, wb0 + k * 2048 + 8 * 128);
        wr[k][1] = tr_read2(wb1 + k * 2048, wb1 + k * 2048 + 8 * 128);
      }
      if constexpr (t == 4 && k == 11) WAIT_LGKM();
      if constexpr (t >= 5) {
        if constexpr (k >= 2 && k < 10) {
          constexpr int n = 8 * (t - 5) + (k - 2), gq = n >> 2;
          if constexpr ((n & 3) == 0) set_m0((unsigned)(w * P4_EARLY + gq * 4096));
          wrow_issue<(n & 3)>(wdb + (4 + gq) * 32 * 512, wv4);
        }
      }
      SCHED();
    });
  });
  STAMP(19);
  asm volatile("s_nop 15\n\ts_nop 7" ::: "memory");
  SCHED();
  static_for<12>([&](auto K) { epi_piece(std::integral_constant<int, 6>{}, K); });
}

constexpr int P4_UP = 10;
DEV void phase4(const Params& p, LAS unsigned char* L, v4u rsv, int p64, int nt, int w, int lane,
                const v4u (&hkeep)[2], const v4u (&wr)[8][2] TSARG) {
  const int hh = lane >> 5, blk = (lane >> 4) & 1, q4 = (lane & 15) >> 2, pp = lane & 3;
  const int sw = ((q4 >> 1) & 1) << 2;
  const int wo0 = (4 * hh + q4) * 128 + (((2 * blk + (pp >> 1)) ^ sw) * 16) + 8 * (pp & 1);
  const int wo1 = (4 * hh + q4) * 128 + (((4 + 2 * blk + (pp >> 1)) ^ sw) * 16) + 8 * (pp & 1);
  int e0 = w * P4_EARLY + wo0, e1 = w * P4_EARLY + wo1, l0 = P4_LATE + w * P4_WAVE + wo0, l1 = P4_LATE + w * P4_WAVE + wo1;
  PIN_V(e0); PIN_V(e1); PIN_V(l0); PIN_V(l1);
  f32x16 acc[2][2];
#pragma unroll
  for (int a = 0; a < 2; ++a)
#pragma unroll
    for (int b = 0; b < 2; ++b)
#pragma unroll
      for (int e = 0; e < 16; ++e) acc[a][b][e] = 0.f;
  v4u gf[2][28];
  const unsigned gb0 = WS_G + ((2 * p64) * 112 + 28 * w) * 1024, gb1 = gb0 + 112 * 1024;
  auto ldg = [&](auto I) {
    constexpr int i = I;
    if constexpr (i < 24) {
      bload_a<(i & 3) * 1024>(gf[0][i], rsv, lane * 16, gb0 + (i >> 2) * 4096);
      bload_a<(i & 3) * 1024>(gf[1][i], rsv, lane * 16, gb1 + (i >> 2) * 4096);
    } else {
      bload_v<(i & 3) * 1024>(gf[0][i], rsv, lane * 16, gb0 + (i >> 2) * 4096);
      bload_v<(i & 3) * 1024>(gf[1][i], rsv, lane * 16, gb1 + (i >> 2) * 4096);
    }
  };
  static_for<P4_UP>(ldg);
  SCHED();
  v4u an0 = wr[0][0], an1 = wr[0][1];
  static_for<28>([&](auto I) {
    constexpr int i = I;
    constexpr int cum = (2 * (P4_UP + i) < 56) ? 2 * (P4_UP + i) : 56;
    constexpr int N = cum - 2 * i - 2;
    if constexpr (i < 24) wait_vm_a2<N>(gf[0][i], gf[1][i]); else wait_vm_v2<N>(gf[0][i], gf[1][i]);
    const v4u a0 = an0, a1 = an1;
    if constexpr (i + 1 < 8) { an0 = wr[i + 1][0]; an1 = wr[i + 1][1]; }
    else if constexpr (i + 1 < 28) {
      constexpr bool early = (i + 1) < 16;
      constexpr int sl = early ? (i + 1 - 8) : (i + 1 - 16);
      LAS unsigned char* q0 = L + (early ? e0 : l0) + sl * 2048;
      LAS unsigned char* q1 = L + (early ? e1 : l1) + sl * 2048;
      an0 = tr_read2(q0, q0 + 8 * 128);
      an1 = tr_read2(q1, q1 + 8 * 128);
    }
    acc[0][0] = mfma32(a0, gf[0][i], acc[0][0]);
    acc[1][0] = mfma32(a1, gf[0][i], acc[1][0]);
    SCHED();
    if constexpr (i + P4_UP < 28) ldg(std::integral_constant<int, i + P4_UP>{});
    SCHED();
    acc[0][1] = mfma32(a0, gf[1][i], acc[0][1]);
    acc[1][1] = mfma32(a1, gf[1][i], acc[1][1]);
    SCHED();
  });
  STAMP(16);
  f32x16 r = reduce4<P4_WAVE>(acc, L + P4_LATE, w, lane);
  const int tok = 64 * p64 + 32 * (w >> 1) + (lane & 31);
  const int n0 = 64 * nt + 32 * (w & 1) + 4 * hh;
#pragma unroll
  for (int qd = 0; qd < 4; ++qd) {
    const unsigned h01 = hkeep[qd >> 1][2 * (qd & 1)], h23 = hkeep[qd >> 1][2 * (qd & 1) + 1];
    const unsigned y01 = pk_bf16(r[4 * qd + 0], r[4 * qd + 1]), y23 = pk_bf16(r[4 * qd + 2], r[4 * qd + 3]);
    v2u o = {pk_bf16(bf_lo(h01) + bf_lo(y01), bf_hi(h01) + bf_hi(y01)), pk_bf16(bf_lo(h23) + bf_lo(y23), bf_hi(h23) + bf_hi(y23))};
    *(GAS v2u*)(p.out + tok * 512 + n0 + 8 * qd) = o;
  }
}
__global__ void __launch_bounds__(256, 1) mk_kernel(Params p) {
  extern __shared__ __attribute__((aligned(16))) unsigned char smem[];
  LAS unsigned char* L = (LAS unsigned char*)smem;
  const int tid = threadIdx.x, lane = tid & 63, w = __builtin_amdgcn_readfirstlane(tid >> 6);
  const int b = blockIdx.x;
  const int dom = (b & 7) * 2 + (b >> 7), j = (b >> 3) & 15;
  const int p64 = 2 * dom + (j >> 3), j8 = j & 7;
  const __amdgpu_buffer_rsrc_t rs = __builtin_amdgcn_make_buffer_rsrc((void*)p.ws, 0, (int)WS_BYTES, 0x00020000);
  const v4u rsv = {(unsigned)(size_t)p.ws, (unsigned)((size_t)p.ws >> 32), WS_BYTES, 0x00020000u};
  GAS unsigned* ctl = (GAS unsigned*)(p.ws + WS_FLAGS);
  STAMP(0);
  {
    unsigned long long pc; unsigned csz;
    asm volatile("s_getpc_b64 %0\n.Lmk_pc_here:\n\ts_mov_b32 %1, .Lmk_code_end-.Lmk_pc_here" : "=s"(pc), "=s"(csz));
    const unsigned off = (unsigned)((b >> 3) * 16 + (lane & 15)) * 128u + (unsigned)w * 65536u;
    if (lane < 16 && off < csz) {
      asm volatile("s_mov_b32 m0, %2\n\ts_nop 0\n\tglobal_load_lds_dword %0, %1" :: "v"(off), "s"(pc), "s"(160768u) : "memory");
    }
  }
  unsigned wvo[6], vx[5];
  {
    const int tok0 = 64 * p64 - 16, s0 = (p64 & 3) * 2;
    p1_xvoff(tok0, w, lane, vx);
    p1_wvoff(w, lane, wvo);
    p1_x_setup(s0, w);
    static_for<5>([&](auto I) { p1_issue_x1<I>(p, s0, w, lane, vx); });
    p1_w_setup(0, w);
    static_for<6>([&](auto I) { p1_issue_w1<I>(p, j8, s0, wvo); });
    p1_w_setup(1, w);
    static_for<6>([&](auto I) { p1_issue_w1<I>(p, j8, s0 + 1, wvo); });
    if (w == 0) *(LAS v4u*)(L + P1_X + (lane >> 3) * 10240 + (lane & 7) * 16) = v4u{0x3f803f80u, 0x3f803f80u, 0x3f803f80u, 0x3f803f80u};
  }
  STAMP(1);
  phase1(p, L, rs, p64, j8, w, lane, wvo, vx TSPASS);
  asm volatile("" ::: "memory"); SCHED();
  STAMP(2);
  const unsigned tag = __hip_atomic_load(ctl + FL_EPOCH, __ATOMIC_RELAXED, __HIP_MEMORY_SCOPE_AGENT) + 1u;
  WAIT_VM_BAR(0);
  if (tid == 0) __hip_atomic_store(flag_ptr(ctl, 0, dom, j), tag, __ATOMIC_RELAXED, __HIP_MEMORY_SCOPE_AGENT);
  STAMP(3);
  v4u csv4[4];
  cs_load(p, j, dom, tid, csv4);
  SCHED();
  unsigned vo[7];
  p3_voffs(lane, vo);
  p2_issue_x(p, p64, j8, w, lane);
#pragma unroll
  for (int ds = 0; ds < 2; ++ds) p3_issue_w(p, j, ds, w, vo);
  WAIT_VM(16);
  cs_finish(L, rs, j, dom, w, lane, csv4);
  wait_flags(flag_ptr(ctl, 0, dom, (j & 8) + (lane & 7)), tag);
  STAMP(4);
  v4u hkeep[2];
  phase2(p, L, rs, p64, j8, w, lane, hkeep, j, vo TSPASS);
  STAMP(5);
  WAIT_VM_BAR(0);
  if (tid == 0) __hip_atomic_store(flag_ptr(ctl, 1, dom, j), tag, __ATOMIC_RELAXED, __HIP_MEMORY_SCOPE_AGENT);
  STAMP(6);
#pragma unroll
  for (int ds = 3; ds < 5; ++ds) p3_issue_w(p, j, ds, w, vo);
  wait_flags(flag_ptr(ctl, 1, dom, lane & 15), tag);
  STAMP(7);
  v4u wr[8][2];
  phase3(p, L, rs, rsv, ctl, tag, dom, j, w, lane, j8, vo, wr TSPASS);
  STAMP(8);
  WAIT_VM_BAR(0);
  if (tid == 0) __hip_atomic_store(flag_ptr(ctl, 2, dom, j), tag, __ATOMIC_RELAXED, __HIP_MEMORY_SCOPE_AGENT);
  STAMP(9);
  {
    unsigned wv4[4];
    wrow_voffs(lane, wv4);
    const u16* wdb = p.wd + (448 * w + 256) * 512 + j8 * 64;
    static_for<6>([&](auto G) {
      constexpr int gq = G;
      set_m0((unsigned)(P4_LATE + w * P4_WAVE + gq * 4096));
      static_for<4>([&](auto I) { wrow_issue<I>(wdb + gq * 32 * 512, wv4); });
    });
  }
  wait_flags(flag_ptr(ctl, 2, dom, lane & 15), tag);
  STAMP(10);
  if (b == 0 && tid == 0) __hip_atomic_store(ctl + FL_EPOCH, tag, __ATOMIC_RELAXED, __HIP_MEMORY_SCOPE_AGENT);
  phase4(p, L, rsv, p64, j8, w, lane, hkeep, wr TSPASS);
  STAMP(17);
  asm volatile(".Lmk_code_end:" ::: "memory");
}

extern "C" void kernel_launch(void* const* d_in, const int* in_sizes, int n_in, void* d_out, int out_size,
                              void* d_ws, size_t ws_size, hipStream_t stream) {
  Params p;
  p.x = (const u16*)d_in[0];
  p.wqkv = (const u16*)d_in[1];
  p.wo = (const u16*)d_in[2];
  p.wg = (const u16*)d_in[4];
  p.wu = (const u16*)d_in[5];
  p.wd = (const u16*)d_in[6];
  p.out = (u16*)d_out;
  p.ws = (unsigned char*)d_ws;
  (void)hipFuncSetAttribute((const void*)mk_kernel, hipFuncAttributeMaxDynamicSharedMemorySize, LDS_BYTES);
  mk_kernel<<<256, 256, LDS_BYTES, stream>>>(p);
}
```

```cpp
#include <hip/hip_runtime.h>
#include <stdint.h>
#include <utility>

#define OUT_AUX 0
#define MK_STAMP 0

typedef unsigned short u16;
typedef unsigned v4u __attribute__((ext_vector_type(4)));
typedef unsigned v2u __attribute__((ext_vector_type(2)));
typedef short v8s __attribute__((ext_vector_type(8)));
typedef short v4s __attribute__((ext_vector_type(4)));
typedef float f32x2 __attribute__((ext_vector_type(2)));
typedef float f32x4 __attribute__((ext_vector_type(4)));
typedef float f32x16 __attribute__((ext_vector_type(16)));
typedef __bf16 bf16x2 __attribute__((ext_vector_type(2)));
#define GAS __attribute__((address_space(1)))
#define LAS __attribute__((address_space(3)))
#define DEV __device__ __forceinline__

constexpr unsigned WS_FLAGS = 0;
constexpr unsigned WS_O = 262144;
constexpr unsigned WS_H = WS_O + 2097152;
constexpr unsigned WS_HS = WS_H + 2097152;
constexpr unsigned WS_G = WS_HS + 262144;
constexpr unsigned WS_CS = WS_G + 64 * 112 * 1024;
constexpr unsigned WS_STAMP = WS_CS + 16 * 16 * 224 * 4;
constexpr unsigned WS_BYTES = WS_STAMP + 256 * 4 * 48 * 8;
constexpr int FL_EPOCH = 0, FL_F = 1024;

constexpr int LDS_BYTES = 163840;
constexpr int AUX_SC1 = 16;

struct Params {
  const u16* x; const u16* wqkv; const u16* wo; const u16* wg; const u16* wu; const u16* wd;
  u16* out; unsigned char* ws;
};

template <class F, int... I> DEV void static_for_impl(F&& f, std::integer_sequence<int, I...>) { (f(std::integral_constant<int, I>{}), ...); }
template <int N, class F> DEV void static_for(F&& f) { static_for_impl(f, std::make_integer_sequence<int, N>{}); }

DEV float bf_lo(unsigned u) { return __uint_as_float(u << 16); }
DEV float bf_hi(unsigned u) { return __uint_as_float(u & 0xffff0000u); }
DEV unsigned pk_bf16(float a, float b) {
  bf16x2 p; p[0] = (__bf16)a; p[1] = (__bf16)b;
  return __builtin_bit_cast(unsigned, p);
}
DEV f32x2 pk2(float a, float b) { return f32x2{a, b}; }
DEV unsigned cvt2(f32x2 v) { return pk_bf16(v.x, v.y); }
DEV float dot2(unsigned a, unsigned b, float c) {
  return __builtin_amdgcn_fdot2_f32_bf16(__builtin_bit_cast(bf16x2, a), __builtin_bit_cast(bf16x2, b), c, false);
}
DEV f32x4 mfma16(v4u a, v4u b, f32x4 c) {
  return __builtin_amdgcn_mfma_f32_16x16x32_bf16(__builtin_bit_cast(v8s, a), __builtin_bit_cast(v8s, b), c, 0, 0, 0);
}
DEV f32x4 mfma16k16(v2u a, v2u b, f32x4 c) {
  return __builtin_amdgcn_mfma_f32_16x16x16bf16_1k(__builtin_bit_cast(v4s, a), __builtin_bit_cast(v4s, b), c, 0, 0, 0);
}
DEV f32x16 mfma32(v4u a, v4u b, f32x16 c) {
  return __builtin_amdgcn_mfma_f32_32x32x16_bf16(__builtin_bit_cast(v8s, a), __builtin_bit_cast(v8s, b), c, 0, 0, 0);
}
DEV v2u tr_read(LAS unsigned char* p) {
  return __builtin_bit_cast(v2u, __builtin_amdgcn_ds_read_tr16_b64_v4i16((LAS v4s*)p));
}
DEV v4u tr_read2(LAS unsigned char* p0, LAS unsigned char* p1) {
  v2u a = tr_read(p0), b = tr_read(p1);
  return v4u{a.x, a.y, b.x, b.y};
}
DEV void dma16(const void* sbase, unsigned voff, unsigned lds_dst) {
  lds_dst = __builtin_amdgcn_readfirstlane(lds_dst);
  asm volatile("s_mov_b32 m0, %2\n\ts_nop 0\n\tglobal_load_lds_dwordx4 %0, %1"
               :: "v"(voff), "s"(sbase), "s"(lds_dst) : "memory");
}
DEV void set_m0(unsigned v) {
  v = __builtin_amdgcn_readfirstlane(v);
  asm volatile("s_mov_b32 m0, %0\n\ts_nop 0" :: "s"(v) : "memory");
}
template <int IMM> DEV void dma16i(const void* sbase, unsigned voff) {
  asm volatile("global_load_lds_dwordx4 %0, %1 offset:%c2" :: "v"(voff), "s"(sbase), "i"(IMM) : "memory");
}
template <int IMM> DEV void bload_a(v4u& dst, v4u rsrc, unsigned voff, unsigned soff) {
  asm volatile("buffer_load_dwordx4 %0, %1, %2, %3 offen offset:%c4 sc1" : "=a"(dst) : "v"(voff), "s"(rsrc), "s"(soff), "i"(IMM) : "memory");
}
template <int IMM> DEV void bload_v(v4u& dst, v4u rsrc, unsigned voff, unsigned soff) {
  asm volatile("buffer_load_dwordx4 %0, %1, %2, %3 offen offset:%c4 sc1" : "=v"(dst) : "v"(voff), "s"(rsrc), "s"(soff), "i"(IMM) : "memory");
}
DEV void bload1_v(unsigned& dst, v4u rsrc, unsigned voff, unsigned soff) {
  asm volatile("buffer_load_dword %0, %1, %2, %3 offen sc1" : "=v"(dst) : "v"(voff), "s"(rsrc), "s"(soff) : "memory");
}
template <int N> DEV void wait_vm_a(v4u& x) { asm volatile("s_waitcnt vmcnt(%c1)" : "+a"(x) : "i"(N) : "memory"); }
template <int N> DEV void wait_vm_a2(v4u& x, v4u& y) { asm volatile("s_waitcnt vmcnt(%c2)" : "+a"(x), "+a"(y) : "i"(N) : "memory"); }
template <int N> DEV void wait_vm_v2(v4u& x, v4u& y) { asm volatile("s_waitcnt vmcnt(%c2)" : "+v"(x), "+v"(y) : "i"(N) : "memory"); }
template <int N> DEV void wait_vm_v1(unsigned& x) { asm volatile("s_waitcnt vmcnt(%c1)" : "+v"(x) : "i"(N) : "memory"); }
#define PIN_V(x) asm volatile("" : "+v"(x))
#define PIN_A(x) asm volatile("" : "+a"(x))
#define WAIT_VM(n) asm volatile("s_waitcnt vmcnt(" #n ")" ::: "memory")
#define WAIT_VM_BAR(n) asm volatile("s_waitcnt vmcnt(" #n ") lgkmcnt(0)\n\ts_barrier" ::: "memory")
#define BAR_LDS() asm volatile("s_waitcnt lgkmcnt(0)\n\ts_barrier" ::: "memory")
#define WAIT_LGKM() asm volatile("s_waitcnt lgkmcnt(0)" ::: "memory")
#define SCHED() __builtin_amdgcn_sched_barrier(0)

DEV float xsum16_32(float v) {
  auto r = __builtin_amdgcn_permlane16_swap(__float_as_uint(v), __float_as_uint(v), false, false);
  v = __uint_as_float(r[0]) + __uint_as_float(r[1]);
  auto s = __builtin_amdgcn_permlane32_swap(__float_as_uint(v), __float_as_uint(v), false, false);
  return __uint_as_float(s[0]) + __uint_as_float(s[1]);
}
DEV float xmax16_32(float v) {
  auto r = __builtin_amdgcn_permlane16_swap(__float_as_uint(v), __float_as_uint(v), false, false);
  v = fmaxf(__uint_as_float(r[0]), __uint_as_float(r[1]));
  auto s = __builtin_amdgcn_permlane32_swap(__float_as_uint(v), __float_as_uint(v), false, false);
  return fmaxf(__uint_as_float(s[0]), __uint_as_float(s[1]));
}
DEV float xsum32(float v) {
  auto s = __builtin_amdgcn_permlane32_swap(__float_as_uint(v), __float_as_uint(v), false, false);
  return __uint_as_float(s[0]) + __uint_as_float(s[1]);
}

#define STAMP(i) do {} while (0)
#define TSARG
#define TSPASS

DEV GAS unsigned* flag_ptr(GAS unsigned* ctl, int ph, int dom, int j) { return ctl + FL_F + ((ph * 16 + dom) * 16 + j) * 32; }
DEV void wait_flags(GAS unsigned* word, unsigned tag) {
  for (unsigned spins = 0; spins < (1u << 20); ++spins) {
    const unsigned v = __hip_atomic_load(word, __ATOMIC_RELAXED, __HIP_MEMORY_SCOPE_AGENT);
    if (__all(v == tag)) break;
    __builtin_amdgcn_s_sleep(1);
  }
  __builtin_amdgcn_fence(__ATOMIC_ACQUIRE, "wavefront");
  asm volatile("" ::: "memory");
}

constexpr int P1_X = 0;
constexpr int P1_W = 81920;
constexpr int P1_WSLOT = 24576;
constexpr int P1_MISC = 155648;
constexpr int P2_W = 0;
constexpr int P2_X = 151936;
constexpr int CS_BUF = 160128;
constexpr int P3_UP = 14464;
constexpr int P3_CSP = 151936;
DEV constexpr int p3_slot_addr(int ds) { return (ds % 5) == 0 ? 65536 : (ds % 5) == 1 ? 94336 : (ds % 5) == 2 ? 123136 : (ds % 5) == 3 ? 0 : 28800; }
constexpr int P4_EARLY = 16384;
constexpr int P4_LATE = 65536, P4_WAVE = 24576;

DEV unsigned wrow_voff(int lane) { return (unsigned)(lane >> 3) * 1024u + (unsigned)((lane & 7) ^ (((lane >> 4) & 1) << 2)) * 16u; }

DEV void p1_xvoff(int tok0, int w, int lane, unsigned (&vx)[5]) {
#pragma unroll
  for (int i = 0; i < 5; ++i) {
    const int rg = 5 * (w & 1) + i;
    const int row = 8 * rg + (lane >> 3);
    int tok = tok0 + row; tok = tok < 0 ? 0 : tok;
    const int ch = (lane & 7) ^ ((row >> 1) & 7);
    vx[i] = (unsigned)tok * 1024u + (unsigned)ch * 16u + 4096u - (unsigned)((i - 2) * 1024);
  }
}
DEV void p1_x_setup(int sp0, int w) { set_m0((unsigned)(P1_X + (sp0 + (w >> 1)) * 10240 + (5 * (w & 1) + 2) * 1024)); }
template <int I> DEV void p1_issue_x1(const Params& p, int sp0, int w, int lane, const unsigned (&vx)[5]) {
  const unsigned char* base = (const unsigned char*)p.x + (sp0 + (w >> 1)) * 128 - 4096;
  if constexpr (I == 0) { if ((w & 1) || lane >= 8) dma16i<(I - 2) * 1024>(base, vx[I]); }
  else dma16i<(I - 2) * 1024>(base, vx[I]);
}
DEV void p1_wvoff(int w, int lane, unsigned (&vo)[6]) {
#pragma unroll
  for (int i = 0; i < 6; ++i) {
    const int idx = 6 * w + i;
    const int part = idx >> 3, rg = idx & 7;
    const int row = 8 * rg + (lane >> 3);
    const int f = ((row >> 1) & 1) | (((row >> 3) & 1) << 1);
    const int ch = (lane & 7) ^ (f << 1);
    vo[i] = (unsigned)row * 3072u + (unsigned)part * 1024u + (unsigned)ch * 16u + 4096u - (unsigned)((i - 3) * 1024);
  }
}
DEV void p1_w_setup(int slot, int w) { set_m0((unsigned)(P1_W + slot * P1_WSLOT + (6 * w + 3) * 1024)); }
template <int I> DEV void p1_issue_w1(const Params& p, int hg, int st, const unsigned (&vo)[6]) {
  const unsigned char* base = (const unsigned char*)p.wqkv + st * 64 * 3072 + hg * 128 - 4096;
  dma16i<(I - 3) * 1024>(base, vo[I]);
}
DEV void wrow_voffs(int lane, unsigned (&v)[4]) {
#pragma unroll
  for (int i = 0; i < 4; ++i) v[i] = wrow_voff(lane) + 7168u * i;
}
template <int I> DEV void wrow_issue(const u16* rowbase  , const unsigned (&v)[4]) { dma16i<I * 1024>(rowbase, v[I]); }

struct P1Frag { v4u xf[5]; v4u wq, wk, wv; };

DEV void phase1(const Params& p, LAS unsigned char* L, __amdgpu_buffer_rsrc_t rs, int p64, int hg, int w, int lane,
                const unsigned (&wvo)[6], const unsigned (&vx)[5], const unsigned (&wv4)[4] TSARG) {
  const int s0 = (p64 & 3) * 2;
  const int g = lane >> 4, tk = lane & 15, q4 = (lane & 15) >> 2, pp = lane & 3;
  const int fsw = ((q4 >> 1) & 1) | ((g & 1) << 1);
  const int wbase = P1_W + (8 * g + q4) * 128 + (((2 * w + (pp >> 1)) ^ (fsw << 1)) * 16) + 8 * (pp & 1);
  const int xsw = (tk >> 1) & 7;
  const int xb0 = P1_X + tk * 128 + ((g ^ xsw) * 16), xb1 = P1_X + tk * 128 + (((4 + g) ^ xsw) * 16);
  const int xbw = (w & 1) ? xb1 : xb0;

  f32x4 aq[5], ak[5], av[5], ssum[5], sgr[5];
#pragma unroll
  for (int t = 0; t < 5; ++t) {
    aq[t] = f32x4{0, 0, 0, 0}; ak[t] = f32x4{0, 0, 0, 0}; av[t] = f32x4{0, 0, 0, 0}; ssum[t] = f32x4{0, 0, 0, 0}; sgr[t] = f32x4{0, 0, 0, 0};
  }
  const v4u ones = {0x3f803f80u, 0x3f803f80u, 0x3f803f80u, 0x3f803f80u};
  LAS f32x2* part = (LAS f32x2*)(L + P1_MISC);
  LAS float* fmean = (LAS float*)(L + P1_MISC + 2560 + w * 640);
  LAS float* frstd = fmean + 80;
  const u16* wob = p.wo + (128 * w) * 512 + hg * 64;
  auto dmo = [&](auto G) {
    constexpr int gq = G;
    set_m0((unsigned)(P2_W + (128 * w + 32 * gq) * 128));
    static_for<4>([&](auto I) { wrow_issue<I>(wob + gq * 32 * 512, wv4); });
  };
  float m1[5], r1[5];
  f32x4 mv[5], rv[5];

  WAIT_VM_BAR(6);
  STAMP(11);
  P1Frag cur;
  {
    LAS unsigned char* xs = L + s0 * 10240 + xb0;
    LAS unsigned char* wsl = L + wbase;
    cur.wk = tr_read2(wsl + 8192, wsl + 8192 + 512);
    cur.wv = tr_read2(wsl + 16384, wsl + 16384 + 512);
    cur.wq = tr_read2(wsl, wsl + 512);
#pragma unroll
    for (int t = 0; t < 5; ++t) cur.xf[t] = *(LAS v4u*)(xs + t * 2048);
  }
  SCHED();
  static_for<16>([&](auto KS) {
    constexpr int ks = KS;
    if constexpr (ks == 1) { WAIT_VM_BAR(5); }
    else if constexpr (ks == 3 || ks == 7 || ks == 11) { WAIT_VM_BAR(6); }
    else if constexpr (ks == 5 || ks == 9) { WAIT_VM_BAR(11); }
    else if constexpr (ks == 13) { WAIT_VM_BAR(0); }
    else if constexpr (ks == 15) { BAR_LDS(); }
    constexpr bool isx = (ks == 0 || ks == 4 || ks == 8);
    constexpr int wst = ks == 1 ? 2 : ks == 2 ? 3 : ks == 3 ? 4 : ks == 5 ? 5 : ks == 7 ? 6 : ks == 9 ? 7 : -1;
    if constexpr (isx) p1_x_setup((s0 + 2 + ks / 2) & 7, w);
    if constexpr (wst >= 0) p1_w_setup(wst % 3, w);
    auto dma = [&](auto D) {
      constexpr int d = D;
      if constexpr (isx && d < 5) p1_issue_x1<d>(p, (s0 + 2 + ks / 2) & 7, w, lane, vx);
      if constexpr (wst >= 0) p1_issue_w1<d>(p, hg, (s0 + wst) & 7, wvo);
      if constexpr (ks == 15 && d == 2) dmo(std::integral_constant<int, 0>{});
    };
    P1Frag nxt;
    constexpr int kn = ks + 1, itn = kn >> 1, k2n = kn & 1;
    LAS unsigned char* xs = L + ((s0 + itn) & 7) * 10240 + (k2n ? xb1 : xb0);
    LAS unsigned char* wsl = L + wbase + (itn % 3) * P1_WSLOT + k2n * 4096;
    constexpr int G = ks < 8 ? (ks >> 2) : ks < 11 ? 2 : 3;
    constexpr int st0 = ks < 8 ? (ks & 3) : ks == 8 ? 0 : ks == 9 ? 2 : ks == 10 ? 4 : ks == 11 ? 0 : 2;
    constexpr int nst = ks < 8 ? ((ks & 3) == 3 ? 2 : 1) : ks == 8 ? 2 : ks == 9 ? 2 : ks == 10 ? 1 : ks == 11 ? 2 : ks == 12 ? 3 : 0;
    LAS unsigned char* sx = L + ((s0 + 2 * G + (w >> 1)) & 7) * 10240 + xbw;
    v4u sv[3];
    dma(std::integral_constant<int, 0>{});
    static_for<5>([&](auto T) {
      constexpr int t = T;
      if constexpr (ks < 15) {
        if constexpr (t == 0) nxt.wk = tr_read2(wsl + 8192, wsl + 8192 + 512);
        if constexpr (t == 1) nxt.wv = tr_read2(wsl + 16384, wsl + 16384 + 512);
        if constexpr (t == 2) nxt.wq = tr_read2(wsl, wsl + 512);
        nxt.xf[t] = *(LAS v4u*)(xs + t * 2048);
      }
      if constexpr (t < nst) sv[t] = *(LAS v4u*)(sx + (st0 + t) * 2048);
      if constexpr (ks == 15) {
        m1[t] = fmean[t * 16 + tk]; r1[t] = frstd[t * 16 + tk];
        mv[t] = *(LAS f32x4*)(fmean + t * 16 + 4 * g); rv[t] = *(LAS f32x4*)(frstd + t * 16 + 4 * g);
      }
      ak[t] = mfma16(cur.wk, cur.xf[t], ak[t]);
      av[t] = mfma16(cur.xf[t], cur.wv, av[t]);
      aq[t] = mfma16(cur.wq, cur.xf[t], aq[t]);
      if constexpr (t == 4) {
        static_for<3>([&](auto I) {
          constexpr int i = I;
          if constexpr (i < nst) { ssum[st0 + i] = mfma16(ones, sv[i], ssum[st0 + i]); sgr[st0 + i] = mfma16(sv[i], sv[i], sgr[st0 + i]); }
        });
      }
      if constexpr (ks == 13 && t < 2) {
        const int tok = t * 64 + lane;
        if (tok < 80) {
          const f32x2 a = (part[tok] + part[80 + tok]) + (part[160 + tok] + part[240 + tok]);
          const float mean = a.x * (1.f / 512.f);
          const float var = fmaxf(a.y * (1.f / 512.f) - mean * mean, 0.f);
          fmean[tok] = mean; frstd[tok] = __builtin_amdgcn_rsqf(var + 1e-5f);
        }
      }
      SCHED();
      dma(std::integral_constant<int, t + 1>{});
      SCHED();
    });
    if constexpr (ks == 12) {
      const int r = tk & 3;
#pragma unroll
      for (int t = 0; t < 5; ++t) {
        const float d = r == 0 ? sgr[t][0] : r == 1 ? sgr[t][1] : r == 2 ? sgr[t][2] : sgr[t][3];
        if ((tk >> 2) == g) part[w * 80 + t * 16 + tk] = f32x2{ssum[t][0], d};
      }
      SCHED();
    }
    if constexpr (ks < 15) cur = nxt;
  });
  STAMP(12);
  f32x2 csq[2], csk[2];
#pragma unroll
  for (int r = 0; r < 4; ++r) {
    csq[r >> 1][r & 1] = __uint_as_float(__builtin_amdgcn_update_dpp(0, __float_as_uint(aq[0][r]), 0x150, 0xf, 0xf, false));
    csk[r >> 1][r & 1] = __uint_as_float(__builtin_amdgcn_update_dpp(0, __float_as_uint(ak[0][r]), 0x150, 0xf, 0xf, false));
  }
  float csv;
  {
    auto a = __builtin_amdgcn_permlane16_swap(__float_as_uint(av[0][0]), __float_as_uint(av[0][0]), false, false);
    auto c = __builtin_amdgcn_permlane32_swap(a[0], a[0], false, false);
    csv = __uint_as_float(c[0]);
  }
  dmo(std::integral_constant<int, 1>{});
  v2u qp[4], kp[5], vp[5];
  const float sc = 0.25f * 1.44269504088896f;
  const f32x2 c2 = {csv, csv};
#pragma unroll
  for (int t = 0; t < 5; ++t) {
    const f32x2 m2 = {m1[t], m1[t]}, r2 = {r1[t], r1[t]};
    kp[t].x = cvt2((pk2(ak[t][0], ak[t][1]) - m2 * csk[0]) * r2);
    kp[t].y = cvt2((pk2(ak[t][2], ak[t][3]) - m2 * csk[1]) * r2);
    if (t > 0) {
      qp[t - 1].x = cvt2(pk2(aq[t][0], aq[t][1]) - m2 * csq[0]);
      qp[t - 1].y = cvt2(pk2(aq[t][2], aq[t][3]) - m2 * csq[1]);
    }
  }
  SCHED();
  STAMP(29);
  const f32x4 z = {0, 0, 0, 0};
  f32x4 sp[4], scu[4];
#pragma unroll
  for (int t = 0; t < 4; ++t) { sp[t] = mfma16k16(kp[t], qp[t], z); scu[t] = mfma16k16(kp[t + 1], qp[t], z); }
  SCHED();
  dmo(std::integral_constant<int, 2>{});
#pragma unroll
  for (int t = 0; t < 5; ++t) {
    vp[t].x = cvt2((pk2(av[t][0], av[t][1]) - pk2(mv[t][0], mv[t][1]) * c2) * pk2(rv[t][0], rv[t][1]));
    vp[t].y = cvt2((pk2(av[t][2], av[t][3]) - pk2(mv[t][2], mv[t][3]) * c2) * pk2(rv[t][2], rv[t][3]));
  }
  SCHED();
  f32x2 biasp[2], biasc[2];
#pragma unroll
  for (int r = 0; r < 4; ++r) {
    const int dc = tk - 4 * g - r, dp = dc + 16;
    biasc[r >> 1][r & 1] = (dc >= 0 && dc <= 14 && !(dc & 1)) ? 0.f : -1e30f;
    biasp[r >> 1][r & 1] = (dp <= 14 && !(dp & 1)) ? 0.f : -1e30f;
  }
  f32x2 a[4][4];
  float mx[4], sm[4];
#pragma unroll
  for (int t = 0; t < 4; ++t) {
    const float sq = sc * r1[t + 1];
    const f32x2 s2v = {sq, sq};
    f32x2 bp0 = biasp[0], bp1 = biasp[1];
    if (t == 0 && p64 == 0) { bp0 = f32x2{-1e30f, -1e30f}; bp1 = bp0; }
    a[t][0] = pk2(sp[t][0], sp[t][1]) * s2v + bp0; a[t][1] = pk2(sp[t][2], sp[t][3]) * s2v + bp1;
    a[t][2] = pk2(scu[t][0], scu[t][1]) * s2v + biasc[0]; a[t][3] = pk2(scu[t][2], scu[t][3]) * s2v + biasc[1];
  }
#pragma unroll
  for (int t = 0; t < 4; ++t)
    mx[t] = fmaxf(fmaxf(__builtin_fmaxf(a[t][0].x, a[t][0].y), fmaxf(a[t][1].x, a[t][1].y)), fmaxf(fmaxf(a[t][2].x, a[t][2].y), fmaxf(a[t][3].x, a[t][3].y)));
  SCHED();
  auto xred2 = [&](float& A, float& B, auto op) {
    auto s1 = __builtin_amdgcn_permlane16_swap(__float_as_uint(A), __float_as_uint(B), false, false);
    const float u = op(__uint_as_float(s1[0]), __uint_as_float(s1[1]));
    auto s2_ = __builtin_amdgcn_permlane32_swap(__float_as_uint(u), __float_as_uint(u), false, false);
    const float v = op(__uint_as_float(s2_[0]), __uint_as_float(s2_[1]));
    auto s3 = __builtin_amdgcn_permlane16_swap(__float_as_uint(v), __float_as_uint(v), false, false);
    A = __uint_as_float(s3[0]); B = __uint_as_float(s3[1]);
  };
  auto fmx = [](float x, float y) { return fmaxf(x, y); };
  auto fad = [](float x, float y) { return x + y; };
  STAMP(30);
  xred2(mx[0], mx[1], fmx); xred2(mx[2], mx[3], fmx);
  SCHED();
  dmo(std::integral_constant<int, 3>{});
#pragma unroll
  for (int t = 0; t < 4; ++t) {
    const f32x2 m2 = {mx[t], mx[t]};
#pragma unroll
    for (int q = 0; q < 4; ++q) a[t][q] -= m2;
  }
#pragma unroll
  for (int t = 0; t < 4; ++t)
#pragma unroll
    for (int q = 0; q < 4; ++q) { a[t][q].x = __builtin_amdgcn_exp2f(a[t][q].x); a[t][q].y = __builtin_amdgcn_exp2f(a[t][q].y); }
  SCHED();
  v4u pf[4];
#pragma unroll
  for (int t = 0; t < 4; ++t) {
    const f32x2 sv = (a[t][0] + a[t][1]) + (a[t][2] + a[t][3]);
    sm[t] = sv.x + sv.y;
    pf[t] = v4u{cvt2(a[t][0]), cvt2(a[t][1]), cvt2(a[t][2]), cvt2(a[t][3])};
  }
  f32x4 o[4];
#pragma unroll
  for (int t = 0; t < 4; ++t) {
    const v4u vf = {vp[t].x, vp[t].y, vp[t + 1].x, vp[t + 1].y};
    o[t] = mfma16(vf, pf[t], z);
  }
  SCHED();
  STAMP(31);
  xred2(sm[0], sm[1], fad); xred2(sm[2], sm[3], fad);
  v2u ob[4];
#pragma unroll
  for (int t = 0; t < 4; ++t) {
    const float inv = __builtin_amdgcn_rcpf(sm[t]);
    const f32x2 i2 = {inv, inv};
    ob[t] = v2u{cvt2(pk2(o[t][0], o[t][1]) * i2), cvt2(pk2(o[t][2], o[t][3]) * i2)};
  }
  const int head = 4 * hg + w;
#pragma unroll
  for (int u = 0; u < 2; ++u) {
    auto sx = __builtin_amdgcn_permlane16_swap(ob[2 * u].x, ob[2 * u + 1].x, false, false);
    auto sy = __builtin_amdgcn_permlane16_swap(ob[2 * u].y, ob[2 * u + 1].y, false, false);
    const v4u o4 = {sx[0], sy[0], sx[1], sy[1]};
    const int voff = ((g >> 1) * 32 + (g & 1) * 16 + tk) * 16;
    __builtin_amdgcn_raw_buffer_store_b128(o4, rs, voff, (int)WS_O + ((2 * p64 + u) * 32 + head) * 1024, AUX_SC1);
  }
}

DEV void cs_load(const Params& p, int c, int r, int tid, v4u (&v)[4]) {
  const int rg = tid >> 5, c0 = tid & 31, cc = c0 < 28 ? c0 : 27;
  const u16* base = (cc < 14 ? p.wg : p.wu) + (size_t)(32 * r + 4 * rg) * 1792 + 112 * c + (cc < 14 ? cc : cc - 14) * 8;
#pragma unroll
  for (int i = 0; i < 4; ++i) v[i] = *(const GAS v4u*)(base + i * 1792);
}
DEV void cs_finish(LAS unsigned char* L, __amdgpu_buffer_rsrc_t rs, int c, int r, int w, int lane, const v4u (&v)[4]) {
  LAS float* buf = (LAS float*)(L + CS_BUF);
  const int c0 = lane & 31;
  f32x4 a, b;
#pragma unroll
  for (int e = 0; e < 2; ++e) {
    a[2 * e] = xsum32((bf_lo(v[0][e]) + bf_lo(v[1][e])) + (bf_lo(v[2][e]) + bf_lo(v[3][e])));
    a[2 * e + 1] = xsum32((bf_hi(v[0][e]) + bf_hi(v[1][e])) + (bf_hi(v[2][e]) + bf_hi(v[3][e])));
    b[2 * e] = xsum32((bf_lo(v[0][2 + e]) + bf_lo(v[1][2 + e])) + (bf_lo(v[2][2 + e]) + bf_lo(v[3][2 + e])));
    b[2 * e + 1] = xsum32((bf_hi(v[0][2 + e]) + bf_hi(v[1][2 + e])) + (bf_hi(v[2][2 + e]) + bf_hi(v[3][2 + e])));
  }
  if (lane < 28) {
    *(LAS f32x4*)(buf + w * 224 + c0 * 8) = a;
    *(LAS f32x4*)(buf + w * 224 + c0 * 8 + 4) = b;
  }
  BAR_LDS();
  const int tid = w * 64 + lane;
  if (tid < 224) {
    const float s = (buf[tid] + buf[224 + tid]) + (buf[448 + tid] + buf[672 + tid]);
    __builtin_amdgcn_raw_buffer_store_b32(__float_as_uint(s), rs, tid * 4, (int)WS_CS + (c * 16 + r) * 896, AUX_SC1);
  }
}

DEV void p3_voffs(int lane, unsigned (&vo)[7]) {
#pragma unroll
  for (int i = 0; i < 7; ++i) {
    const int q = i * 64 + lane, row = q / 14, cc = q - 14 * row;
    vo[i] = (unsigned)row * 3584u + (unsigned)cc * 16u + 4096u - (unsigned)((i - 3) * 1024);
  }
}
DEV void p3_w_setup(int ds, int w) { set_m0((unsigned)(p3_slot_addr(ds) + (w & 1) * P3_UP + (w >> 1) * 7168 + 3 * 1024)); }
template <int I> DEV void p3_issue_w1(const Params& p, int c, int ds, int w, const unsigned (&vo)[7]) {
  const unsigned char* base = (const unsigned char*)((w & 1) ? p.wu : p.wg) + (64 * ds + 32 * (w >> 1)) * 3584 + 224 * c - 4096;
  dma16i<(I - 3) * 1024>(base, vo[I]);
}
DEV void p3_issue_w(const Params& p, int c, int ds, int w, const unsigned (&vo)[7]) {
  p3_w_setup(ds, w);
  static_for<7>([&](auto I) { p3_issue_w1<I>(p, c, ds, w, vo); });
}

DEV void p2_issue_x(const Params& p, int p64, int nt, int w, int lane) {
  const unsigned vx = (unsigned)(lane >> 3) * 1024u + (unsigned)(((lane & 7) ^ (lane >> 3)) * 16);
  const u16* xb = p.x + (64 * p64 + 16 * w) * 512 + nt * 64;
  set_m0((unsigned)(P2_X + 16 * w * 128));
  dma16i<0>(xb, vx);
  dma16i<1024>(xb, vx + 7168u);
}

template <int WSTRIDE>
DEV f32x16 reduce4(f32x16 (&acc)[2][2], LAS unsigned char* red, int w, int lane) {
  WAIT_LGKM();
#pragma unroll
  for (int a = 0; a < 2; ++a)
#pragma unroll
    for (int b = 0; b < 2; ++b) {
      const int dest = a + 2 * b;
      if (dest != w) {
        const int slot = (dest - w - 1) & 3;
#pragma unroll
        for (int qd = 0; qd < 4; ++qd)
          *(LAS f32x4*)(red + w * WSTRIDE + (slot * 4 + qd) * 1024 + lane * 16) =
              f32x4{acc[a][b][4 * qd], acc[a][b][4 * qd + 1], acc[a][b][4 * qd + 2], acc[a][b][4 * qd + 3]};
      }
    }
  BAR_LDS();
  f32x16 r;
  if (w == 0) r = acc[0][0]; else if (w == 1) r = acc[1][0]; else if (w == 2) r = acc[0][1]; else r = acc[1][1];
#pragma unroll
  for (int s = 1; s < 4; ++s) {
    const int src = (w + s) & 3;
#pragma unroll
    for (int qd = 0; qd < 4; ++qd) {
      const f32x4 v = *(LAS f32x4*)(red + src * WSTRIDE + ((3 - s) * 4 + qd) * 1024 + lane * 16);
#pragma unroll
      for (int e = 0; e < 4; ++e) r[4 * qd + e] += v[e];
    }
  }
  return r;
}

DEV void phase2(const Params& p, LAS unsigned char* L, __amdgpu_buffer_rsrc_t rs, v4u rsv, int p64, int nt, int w, int lane, v4u (&hkeep)[2],
                int c3, const unsigned (&vo)[7], int dom, v4u (&csv4)[4] TSARG) {
  v4u of[2][8];
  const unsigned ob0 = WS_O + ((2 * p64) * 32 + 8 * w) * 1024, ob1 = ob0 + 32768;
  static_for<8>([&](auto I) {
    constexpr int i = I;
    bload_v<(i & 3) * 1024>(of[0][i], rsv, lane * 16, ob0 + (i >> 2) * 4096);
    bload_v<(i & 3) * 1024>(of[1][i], rsv, lane * 16, ob1 + (i >> 2) * 4096);
  });
  p3_issue_w(p, c3, 2, w, vo);
  SCHED();
  const int hh = lane >> 5, blk = (lane >> 4) & 1, q4 = (lane & 15) >> 2, pp = lane & 3;
  const int sw = ((q4 >> 1) & 1) << 2;
  LAS unsigned char* wb0 = L + P2_W + (128 * w + 8 * hh + q4) * 128 + (((2 * blk + (pp >> 1)) ^ sw) * 16) + 8 * (pp & 1);
  LAS unsigned char* wb1 = L + P2_W + (128 * w + 8 * hh + q4) * 128 + (((4 + 2 * blk + (pp >> 1)) ^ sw) * 16) + 8 * (pp & 1);
  f32x16 acc[2][2];
#pragma unroll
  for (int a = 0; a < 2; ++a)
#pragma unroll
    for (int b = 0; b < 2; ++b)
#pragma unroll
      for (int e = 0; e < 16; ++e) acc[a][b][e] = 0.f;
  v4u wa[8][2];
#pragma unroll
  for (int i = 0; i < 8; ++i) {
    wa[i][0] = tr_read2(wb0 + i * 2048, wb0 + i * 2048 + 512);
    wa[i][1] = tr_read2(wb1 + i * 2048, wb1 + i * 2048 + 512);
  }
  SCHED();
  static_for<8>([&](auto I) {
    constexpr int i = I;
    wait_vm_v2<7 + 16 - 2 * i - 2>(of[0][i], of[1][i]);
    acc[0][0] = mfma32(wa[i][0], of[0][i], acc[0][0]);
    acc[1][0] = mfma32(wa[i][1], of[0][i], acc[1][0]);
    acc[0][1] = mfma32(wa[i][0], of[1][i], acc[0][1]);
    acc[1][1] = mfma32(wa[i][1], of[1][i], acc[1][1]);
  });
  SCHED();
  STAMP(13);
  cs_load(p, c3, dom, w * 64 + lane, csv4);
  SCHED();
  f32x16 r = reduce4<16384>(acc, L + P2_W, w, lane);
  const int row = 32 * (w >> 1) + (lane & 31);
  float hv[16];
  float s1 = 0.f, s2 = 0.f;
#pragma unroll
  for (int qd = 0; qd < 4; ++qd) {
    const v2u xr = *(LAS v2u*)(L + P2_X + row * 128 + (((4 * (w & 1) + qd) ^ (row & 7)) * 16) + 8 * hh);
    const unsigned y01 = pk_bf16(r[4 * qd + 0], r[4 * qd + 1]), y23 = pk_bf16(r[4 * qd + 2], r[4 * qd + 3]);
    const unsigned h01 = pk_bf16(bf_lo(xr.x) + bf_lo(y01), bf_hi(xr.x) + bf_hi(y01));
    const unsigned h23 = pk_bf16(bf_lo(xr.y) + bf_lo(y23), bf_hi(xr.y) + bf_hi(y23));
    hkeep[qd >> 1][2 * (qd & 1)] = h01; hkeep[qd >> 1][2 * (qd & 1) + 1] = h23;
    hv[4 * qd + 0] = bf_lo(h01); hv[4 * qd + 1] = bf_hi(h01); hv[4 * qd + 2] = bf_lo(h23); hv[4 * qd + 3] = bf_hi(h23);
  }
  const int tile32 = 2 * p64 + (w >> 1);
  const int nt32 = 2 * nt + (w & 1);
#pragma unroll
  for (int ks = 0; ks < 2; ++ks)
    __builtin_amdgcn_raw_buffer_store_b128(hkeep[ks], rs, lane * 16, (int)WS_H + (tile32 * 32 + 2 * nt32 + ks) * 1024, AUX_SC1);
#pragma unroll
  for (int i = 0; i < 16; ++i) { s1 += hv[i]; s2 += hv[i] * hv[i]; }
  s1 = xsum32(s1); s2 = xsum32(s2);
  if (hh == 0) {
    v2u st = {__float_as_uint(s1), __float_as_uint(s2)};
    __builtin_amdgcn_raw_buffer_store_b64(st, rs, (lane & 31) * 16, (int)WS_HS + (tile32 * 8 + (nt32 >> 1)) * 512 + (nt32 & 1) * 8, AUX_SC1);
  }
}

constexpr int P3_KM = 20;
constexpr int p3_nl(int ks) { return ks < 12 ? 2 : 0; }
constexpr int p3_nd(int ks) { return (ks >= 4 && ks < 16) ? ((ks & 3) == 3 ? 1 : 2) : 0; }
constexpr int p3_nx(int ks) { return (ks == 7 || (ks >= 12 && ks < 16)) ? 1 : 0; }
constexpr int p3_cum(int ks) { int c = 12; for (int k = 0; k < ks; ++k) c += p3_nl(k) + p3_nd(k) + p3_nx(k); return c; }
constexpr int p3_pos(int i) { return i < 8 ? 4 + i : p3_cum((i - 8) / 2) + ((i - 8) & 1); }

DEV void phase3(const Params& p, LAS unsigned char* L, __amdgpu_buffer_rsrc_t rs, v4u rsv, GAS unsigned* ctl, unsigned tag, int p128, int c, int w, int lane,
                int nt4, const unsigned (&vo)[7], v4u (&wr)[8][2] TSARG) {
  const int tile32 = 4 * p128 + w;
  const int hh = lane >> 5, blk = (lane >> 4) & 1, q4 = (lane & 15) >> 2, pp = lane & 3;
  v4u hb[32];
  v4u hs[4];
  const unsigned hbase = WS_H + tile32 * 32768;
  static_for<4>([&](auto I) { bload_v<I * 512>(hs[I], rsv, (4 * hh) * 512 + (lane & 31) * 16, WS_HS + tile32 * 4096); });
  static_for<8>([&](auto I) { bload_a<(I & 3) * 1024>(hb[I], rsv, lane * 16, hbase + (I >> 2) * 4096); });
  SCHED();
  const int abase = blk * P3_UP + (4 * hh + q4) * 224 + pp * 8;
  int ab0 = abase, ab1 = abase + 65536, ab2 = abase + 123136;
  PIN_V(ab0); PIN_V(ab1); PIN_V(ab2);
  auto slot_ptr = [&](int ds) -> LAS unsigned char* {
    const int a = p3_slot_addr(ds);
    return a == 123136 ? L + ab2 : a >= 65536 ? L + ab1 + (a - 65536) : L + ab0 + a;
  };
  f32x16 acc[7];
#pragma unroll
  for (int t = 0; t < 7; ++t)
#pragma unroll
    for (int e = 0; e < 16; ++e) acc[t][e] = 0.f;
  WAIT_VM_BAR(12);
  if (w == 0 && lane == 0) __hip_atomic_store(flag_ptr(ctl, 3, p128, c), tag, __ATOMIC_RELAXED, __HIP_MEMORY_SCOPE_AGENT);
  STAMP(14);
  v4u an[7];
  {
    LAS unsigned char* sl = slot_ptr(0);
#pragma unroll
    for (int t = 0; t < 7; ++t) an[t] = tr_read2(sl + t * 32, sl + t * 32 + 8 * 224);
  }
  SCHED();
  unsigned csflag = 0;
  v4u csl[4];
  v4u fn[12];
  LAS float* cspart = (LAS float*)(L + P3_CSP);
  int cspo = P3_CSP + 896 * 4 + 16 * hh; PIN_V(cspo);
  LAS float* csp = cspart + 896;
  LAS float* csq_ = (LAS float*)(L + cspo);
  float mean = 0.f, rstd = 0.f;
  static_for<P3_KM>([&](auto KS) {
    constexpr int ks = KS;
    v4u ac[7];
#pragma unroll
    for (int t = 0; t < 7; ++t) ac[t] = an[t];
    if constexpr (ks == 19) { WAIT_VM_BAR(0); }
    else wait_vm_a<p3_cum(ks) - 1 - p3_pos(ks)>(hb[ks]);
    if constexpr (ks == 19) {
      PIN_A(hb[19]);
#pragma unroll
      for (int k = P3_KM; k < 32; ++k) PIN_A(hb[k]);
    }
    constexpr int ds = (ks + 1) >> 2, ksl = (ks + 1) & 3;
    LAS unsigned char* sl = slot_ptr(ds) + ksl * 16 * 224;
    auto vm = [&](auto SL) {
      constexpr int slot = SL, nl = p3_nl(ks), nd = p3_nd(ks), nx = p3_nx(ks);
      if constexpr (slot < nl) {
        constexpr int i = 8 + 2 * ks + slot;
        bload_a<(i & 3) * 1024>(hb[i], rsv, lane * 16, hbase + (i >> 2) * 4096);
      } else if constexpr (slot < nl + nd) {
        constexpr int i = 2 * (ks & 3) + (slot - nl);
        if constexpr (i == 0) p3_w_setup(5 + ((ks - 4) >> 2), w);
        p3_issue_w1<i>(p, c, 5 + ((ks - 4) >> 2), w, vo);
      } else if constexpr (slot < nl + nd + nx) {
        if constexpr (ks == 7) bload1_v(csflag, rsv, (unsigned)(FL_F + ((48 + (lane & 15)) * 16 + c) * 32) * 4u, 0u);
        else {
          const unsigned vof = (unsigned)(lane < 56 ? lane : 55) * 16u;
          bload_v<(ks - 12) * 896>(csl[ks - 12], rsv, vof, WS_CS + (unsigned)(c * 16 + 4 * w) * 896u);
        }
      }
    };
    if constexpr (ks == 11) {
      wait_vm_v1<p3_cum(11) - 1 - (p3_cum(7) + p3_nl(7) + p3_nd(7))>(csflag);
      if (!__all(csflag == tag)) wait_flags(flag_ptr(ctl, 3, lane & 15, c), tag);
    }
    if constexpr (ks == 13) {
      float s1 = 0.f, s2 = 0.f;
#pragma unroll
      for (int i = 0; i < 4; ++i) {
        PIN_V(hs[i]);
        s1 += __uint_as_float(hs[i].x) + __uint_as_float(hs[i].z);
        s2 += __uint_as_float(hs[i].y) + __uint_as_float(hs[i].w);
      }
      s1 = xsum32(s1); s2 = xsum32(s2);
      mean = s1 * (1.f / 512.f);
      rstd = __builtin_amdgcn_rsqf(fmaxf(s2 * (1.f / 512.f) - mean * mean, 0.f) + 1e-5f);
    }
    if constexpr (ks == 18) {
      asm volatile("s_waitcnt vmcnt(0)" : "+v"(csl[0]), "+v"(csl[1]), "+v"(csl[2]), "+v"(csl[3]) :: "memory");
      f32x4 a;
#pragma unroll
      for (int e = 0; e < 4; ++e)
        a[e] = (__uint_as_float(csl[0][e]) + __uint_as_float(csl[1][e])) + (__uint_as_float(csl[2][e]) + __uint_as_float(csl[3][e]));
      if (lane < 56) *(LAS f32x4*)(cspart + w * 224 + lane * 4) = a;
    }
    static_for<7>([&](auto T) {
      constexpr int t = T;
      if constexpr (t == 0 || t == 2) asm volatile("" : "+v"(ac[t]), "+v"(ac[t + 1]));
      if constexpr (t == 4) asm volatile("" : "+v"(ac[4]), "+v"(ac[5]), "+v"(ac[6]));
      acc[t] = mfma32(ac[t], hb[ks], acc[t]);
      SCHED();
      if constexpr (ks + 1 < P3_KM) {
        if constexpr (t == 1 || t == 3 || t == 5) {
          an[t - 1] = tr_read2(sl + (t - 1) * 32, sl + (t - 1) * 32 + 8 * 224);
          an[t] = tr_read2(sl + t * 32, sl + t * 32 + 8 * 224);
        }
        if constexpr (t == 6) an[6] = tr_read2(sl + 6 * 32, sl + 6 * 32 + 8 * 224);
      } else {
        static_for<2>([&](auto Q) {
          constexpr int k = 2 * t + Q;
          if constexpr (k < 12) {
            constexpr int k2 = P3_KM + k;
            LAS unsigned char* s2p = slot_ptr(k2 >> 2) + (k2 & 3) * 16 * 224;
            fn[k] = tr_read2(s2p, s2p + 8 * 224);
          }
        });
      }
      if constexpr (t == 0 || t == 2 || t == 4 || t == 6) vm(std::integral_constant<int, (t >> 1)>{});
      SCHED();
    });
    if constexpr ((ks & 3) == 3 && (ks >> 2) < 3) BAR_LDS();
  });
  STAMP(15);
  const f32x2 nm2 = {-mean, -mean};
  const float r2s = rstd * rstd;
  const f32x2 k1 = {-2.302208198f * rstd, -2.302208198f * rstd}, k2 = {-0.1029432397f * rstd * r2s, -0.1029432397f * rstd * r2s};
  const f32x2 rr2 = {r2s, r2s}, one2 = {1.f, 1.f};
  f32x2 yg[4], yu[4], zz[4], rc[4];
  f32x4 cgu[4];
  unsigned gw[4];
  auto epi_piece = [&](auto T, auto K) {
    constexpr int t = T, k = K;
    auto csg = [&](int i) { return pk2(cgu[i >> 1][2 * (i & 1)], cgu[i >> 1][2 * (i & 1) + 1]); };
    auto csu = [&](int i) { return pk2(cgu[2 + (i >> 1)][2 * (i & 1)], cgu[2 + (i >> 1)][2 * (i & 1) + 1]); };
    auto ex2 = [&](int i) { zz[i].x = __builtin_amdgcn_exp2f(zz[i].x); zz[i].y = __builtin_amdgcn_exp2f(zz[i].y); };
    auto rc2 = [&](int i) { rc[i].x = __builtin_amdgcn_rcpf(zz[i].x); rc[i].y = __builtin_amdgcn_rcpf(zz[i].y); };
    if constexpr (k == 0) {
      cgu[0] = *(LAS f32x4*)(csq_ + t * 16);
      cgu[1] = *(LAS f32x4*)(csq_ + t * 16 + 8);
      cgu[2] = *(LAS f32x4*)(csq_ + 112 + t * 16);
      cgu[3] = *(LAS f32x4*)(csq_ + 112 + t * 16 + 8);
    } else if constexpr (k == 1) {
      PIN_A(acc[t]);
#pragma unroll
      for (int i = 0; i < 4; ++i) { yg[i] = pk2(acc[t][2 * i], acc[t][2 * i + 1]); PIN_V(yg[i]); }
    } else if constexpr (k == 2) {
#pragma unroll
      for (int i = 0; i < 4; ++i) { yu[i] = pk2(acc[t][8 + 2 * i], acc[t][8 + 2 * i + 1]); PIN_V(yu[i]); }
    } else if constexpr (k == 3) {
#pragma unroll
      for (int i = 0; i < 4; ++i) yg[i] = nm2 * csg(i) + yg[i];
      yu[0] = nm2 * csu(0) + yu[0]; yu[1] = nm2 * csu(1) + yu[1];
    } else if constexpr (k == 4) {
#pragma unroll
      for (int i = 0; i < 4; ++i) zz[i] = yg[i] * yg[i];
      yu[2] = nm2 * csu(2) + yu[2]; yu[3] = nm2 * csu(3) + yu[3];
    } else if constexpr (k == 5) {
#pragma unroll
      for (int i = 0; i < 4; ++i) zz[i] = zz[i] * k2 + k1;
      yu[0] = yu[0] * yg[0]; yu[1] = yu[1] * yg[1];
    } else if constexpr (k == 6) {
#pragma unroll
      for (int i = 0; i < 4; ++i) zz[i] = zz[i] * yg[i];
      yu[2] = yu[2] * yg[2]; yu[3] = yu[3] * yg[3];
    } else if constexpr (k == 7) {
      ex2(0); ex2(1);
      yu[0] = yu[0] * rr2; yu[1] = yu[1] * rr2;
    } else if constexpr (k == 8) {
      ex2(2); ex2(3);
      zz[0] += one2; zz[1] += one2;
    } else if constexpr (k == 9) {
      rc2(0); rc2(1);
      zz[2] += one2; zz[3] += one2;
    } else if constexpr (k == 10) {
      rc2(2); rc2(3);
      yu[2] = yu[2] * rr2; yu[3] = yu[3] * rr2;
      yu[0] = yu[0] * rc[0]; yu[1] = yu[1] * rc[1];
    } else {
      yu[2] = yu[2] * rc[2]; yu[3] = yu[3] * rc[3];
#pragma unroll
      for (int i = 0; i < 4; ++i) gw[i] = cvt2(yu[i]);
      const v4u f = {gw[0], gw[1], gw[2], gw[3]};
      __builtin_amdgcn_raw_buffer_store_b128(f, rs, lane * 16, (int)WS_G + (tile32 * 112 + 7 * c + t) * 1024, AUX_SC1);
    }
  };
  unsigned wv4[4];
  wrow_voffs(lane, wv4);
  const u16* wdb = p.wd + (448 * w) * 512 + nt4 * 64;
  const int sw4 = ((q4 >> 1) & 1) << 2;
  LAS unsigned char* wb0 = L + w * P4_EARLY + (4 * hh + q4) * 128 + (((2 * blk + (pp >> 1)) ^ sw4) * 16) + 8 * (pp & 1);
  LAS unsigned char* wb1 = L + w * P4_EARLY + (4 * hh + q4) * 128 + (((4 + 2 * blk + (pp >> 1)) ^ sw4) * 16) + 8 * (pp & 1);
  SCHED();
  STAMP(18);
  static_for<7>([&](auto T) {
    constexpr int t = T;
    v4u fc[12];
#pragma unroll
    for (int k = 0; k < 12; ++k) fc[k] = fn[k];
    if constexpr (t == 4) WAIT_VM(3);
    static_for<12>([&](auto K) {
      constexpr int k = K;
      acc[t] = mfma32(fc[k], hb[P3_KM + k], acc[t]);
      SCHED();
      if constexpr (t < 6) {
        constexpr int ks = P3_KM + k, ds = ks >> 2, ksl = ks & 3;
        LAS unsigned char* sl = slot_ptr(ds) + ksl * 16 * 224 + (t + 1) * 32;
        fn[k] = tr_read2(sl, sl + 8 * 224);
      }
      if constexpr (t > 0) epi_piece(std::integral_constant<int, t - 1>{}, K);
      if constexpr (t == 0 && k == 0 ) {
        if (lane < 56) {
          const f32x4 a = (*(LAS f32x4*)(cspart + lane * 4) + *(LAS f32x4*)(cspart + 224 + lane * 4)) +
                          (*(LAS f32x4*)(cspart + 448 + lane * 4) + *(LAS f32x4*)(cspart + 672 + lane * 4));
          *(LAS f32x4*)(csp + lane * 4) = a;
        }
      }
      if constexpr (t < 2) {
        if constexpr (k >= 2 && k < 10) {
          constexpr int n = 8 * t + (k - 2), gq = n >> 2;
          if constexpr ((n & 3) == 0) set_m0((unsigned)(w * P4_EARLY + gq * 4096));
          wrow_issue<(n & 3)>(wdb + gq * 32 * 512, wv4);
        }
      }
      if constexpr (t == 4 && k < 8) {
        wr[k][0] = tr_read2(wb0 + k * 2048, wb0 + k * 2048 + 8 * 128);
        wr[k][1] = tr_read2(wb1 + k * 2048, wb1 + k * 2048 + 8 * 128);
      }
      if constexpr (t == 4 && k == 11) WAIT_LGKM();
      if constexpr (t >= 5) {
        if constexpr (k >= 2 && k < 10) {
          constexpr int n = 8 * (t - 5) + (k - 2), gq = n >> 2;
          if constexpr ((n & 3) == 0) set_m0((unsigned)(w * P4_EARLY + gq * 4096));
          wrow_issue<(n & 3)>(wdb + (4 + gq) * 32 * 512, wv4);
        }
      }
      SCHED();
    });
  });
  STAMP(19);
  asm volatile("s_nop 15\n\ts_nop 7" ::: "memory");
  SCHED();
  static_for<12>([&](auto K) { epi_piece(std::integral_constant<int, 6>{}, K); });
}

constexpr int P4_UP = 10;
DEV void phase4(const Params& p, LAS unsigned char* L, v4u rsv, int p64, int nt, int w, int lane,
                const v4u (&hkeep)[2], const v4u (&wr)[8][2] TSARG) {
  const int hh = lane >> 5, blk = (lane >> 4) & 1, q4 = (lane & 15) >> 2, pp = lane & 3;
  const int sw = ((q4 >> 1) & 1) << 2;
  const int wo0 = (4 * hh + q4) * 128 + (((2 * blk + (pp >> 1)) ^ sw) * 16) + 8 * (pp & 1);
  const int wo1 = (4 * hh + q4) * 128 + (((4 + 2 * blk + (pp >> 1)) ^ sw) * 16) + 8 * (pp & 1);
  int e0 = w * P4_EARLY + wo0, e1 = w * P4_EARLY + wo1, l0 = P4_LATE + w * P4_WAVE + wo0, l1 = P4_LATE + w * P4_WAVE + wo1;
  PIN_V(e0); PIN_V(e1); PIN_V(l0); PIN_V(l1);
  f32x16 acc[2][2];
#pragma unroll
  for (int a = 0; a < 2; ++a)
#pragma unroll
    for (int b = 0; b < 2; ++b)
#pragma unroll
      for (int e = 0; e < 16; ++e) acc[a][b][e] = 0.f;
  v4u gf[2][28];
  const unsigned gb0 = WS_G + ((2 * p64) * 112 + 28 * w) * 1024, gb1 = gb0 + 112 * 1024;
  auto ldg = [&](auto I) {
    constexpr int i = I;
    if constexpr (i < 24) {
      bload_a<(i & 3) * 1024>(gf[0][i], rsv, lane * 16, gb0 + (i >> 2) * 4096);
      bload_a<(i & 3) * 1024>(gf[1][i], rsv, lane * 16, gb1 + (i >> 2) * 4096);
    } else {
      bload_v<(i & 3) * 1024>(gf[0][i], rsv, lane * 16, gb0 + (i >> 2) * 4096);
      bload_v<(i & 3) * 1024>(gf[1][i], rsv, lane * 16, gb1 + (i >> 2) * 4096);
    }
  };
  static_for<P4_UP>(ldg);
  SCHED();
  v4u an0 = wr[0][0], an1 = wr[0][1];
  static_for<28>([&](auto I) {
    constexpr int i = I;
    constexpr int cum = (2 * (P4_UP + i) < 56) ? 2 * (P4_UP + i) : 56;
    constexpr int N = cum - 2 * i - 2;
    if constexpr (i < 24) wait_vm_a2<N>(gf[0][i], gf[1][i]); else wait_vm_v2<N>(gf[0][i], gf[1][i]);
    const v4u a0 = an0, a1 = an1;
    if constexpr (i + 1 < 8) { an0 = wr[i + 1][0]; an1 = wr[i + 1][1]; }
    else if constexpr (i + 1 < 28) {
      constexpr bool early = (i + 1) < 16;
      constexpr int sl = early ? (i + 1 - 8) : (i + 1 - 16);
      LAS unsigned char* q0 = L + (early ? e0 : l0) + sl * 2048;
      LAS unsigned char* q1 = L + (early ? e1 : l1) + sl * 2048;
      an0 = tr_read2(q0, q0 + 8 * 128);
      an1 = tr_read2(q1, q1 + 8 * 128);
    }
    acc[0][0] = mfma32(a0, gf[0][i], acc[0][0]);
    acc[1][0] = mfma32(a1, gf[0][i], acc[1][0]);
    SCHED();
    if constexpr (i + P4_UP < 28) ldg(std::integral_constant<int, i + P4_UP>{});
    SCHED();
    acc[0][1] = mfma32(a0, gf[1][i], acc[0][1]);
    acc[1][1] = mfma32(a1, gf[1][i], acc[1][1]);
    SCHED();
  });
  STAMP(16);
  f32x16 r = reduce4<P4_WAVE>(acc, L + P4_LATE, w, lane);
  const int tok = 64 * p64 + 32 * (w >> 1) + (lane & 31);
  v2u o[4];
#pragma unroll
  for (int qd = 0; qd < 4; ++qd) {
    const unsigned h01 = hkeep[qd >> 1][2 * (qd & 1)], h23 = hkeep[qd >> 1][2 * (qd & 1) + 1];
    const unsigned y01 = pk_bf16(r[4 * qd + 0], r[4 * qd + 1]), y23 = pk_bf16(r[4 * qd + 2], r[4 * qd + 3]);
    o[qd] = v2u{pk_bf16(bf_lo(h01) + bf_lo(y01), bf_hi(h01) + bf_hi(y01)), pk_bf16(bf_lo(h23) + bf_lo(y23), bf_hi(h23) + bf_hi(y23))};
  }
  const __amdgpu_buffer_rsrc_t ro = __builtin_amdgcn_make_buffer_rsrc((void*)p.out, 0, 2048 * 512 * 2, 0x00020000);
#pragma unroll
  for (int u = 0; u < 2; ++u) {
    auto sx = __builtin_amdgcn_permlane32_swap(o[2 * u].x, o[2 * u + 1].x, false, false);
    auto sy = __builtin_amdgcn_permlane32_swap(o[2 * u].y, o[2 * u + 1].y, false, false);
    const v4u o4 = {sx[0], sy[0], sx[1], sy[1]};
    __builtin_amdgcn_raw_buffer_store_b128(o4, ro, tok * 1024 + (64 * nt + 32 * (w & 1) + 16 * u + 8 * hh) * 2, 0, OUT_AUX);
  }
}

__global__ void __launch_bounds__(256, 1) mk_kernel(Params p) {
  extern __shared__ __attribute__((aligned(16))) unsigned char smem[];
  LAS unsigned char* L = (LAS unsigned char*)smem;
  const int tid = threadIdx.x, lane = tid & 63, w = __builtin_amdgcn_readfirstlane(tid >> 6);
  const int b = blockIdx.x;
  const int dom = (b & 7) * 2 + (b >> 7), j = (b >> 3) & 15;
  const int p64 = 2 * dom + (j >> 3), j8 = j & 7;
  const __amdgpu_buffer_rsrc_t rs = __builtin_amdgcn_make_buffer_rsrc((void*)p.ws, 0, (int)WS_BYTES, 0x00020000);
  const v4u rsv = {(unsigned)(size_t)p.ws, (unsigned)((size_t)p.ws >> 32), WS_BYTES, 0x00020000u};
  GAS unsigned* ctl = (GAS unsigned*)(p.ws + WS_FLAGS);
  STAMP(0);
  unsigned wvo[6], vx[5];
  {
    const int tok0 = 64 * p64 - 16, s0 = (p64 & 3) * 2;
    p1_wvoff(w, lane, wvo);
    p1_w_setup(0, w);
    static_for<6>([&](auto I) { p1_issue_w1<I>(p, j8, s0, wvo); });
    SCHED();
    p1_xvoff(tok0, w, lane, vx);
    p1_x_setup(s0, w);
    static_for<5>([&](auto I) { p1_issue_x1<I>(p, s0, w, lane, vx); });
    p1_w_setup(1, w);
    static_for<6>([&](auto I) { p1_issue_w1<I>(p, j8, s0 + 1, wvo); });
    SCHED();
    if (w == 0) *(LAS v4u*)(L + P1_X + (lane >> 3) * 10240 + (lane & 7) * 16) = v4u{0x3f803f80u, 0x3f803f80u, 0x3f803f80u, 0x3f803f80u};
  }
  {
    unsigned long long pc; unsigned csz;
    asm volatile("s_getpc_b64 %0\n.Lmk_pc_here:\n\ts_mov_b32 %1, .Lmk_code_end-.Lmk_pc_here" : "=s"(pc), "=s"(csz));
    const unsigned off = (unsigned)((b >> 3) * 16 + (lane & 15)) * 128u + (unsigned)w * 65536u;
    if (lane < 16 && off < csz) {
      asm volatile("s_mov_b32 m0, %2\n\ts_nop 0\n\tglobal_load_lds_dword %0, %1" :: "v"(off), "s"(pc), "s"(160768u) : "memory");
    }
  }
  unsigned wv4[4], vo[7];
  wrow_voffs(lane, wv4);
  p3_voffs(lane, vo);
#pragma unroll
  for (int i = 0; i < 7; ++i) PIN_V(vo[i]);
  const unsigned tag = __hip_atomic_load(ctl + FL_EPOCH, __ATOMIC_RELAXED, __HIP_MEMORY_SCOPE_AGENT) + 1u;
  STAMP(1);
  phase1(p, L, rs, p64, j8, w, lane, wvo, vx, wv4 TSPASS);
  asm volatile("" ::: "memory"); SCHED();
  STAMP(2);
  WAIT_VM_BAR(0);
  if (tid == 0) __hip_atomic_store(flag_ptr(ctl, 0, dom, j), tag, __ATOMIC_RELAXED, __HIP_MEMORY_SCOPE_AGENT);
  STAMP(3);
  p2_issue_x(p, p64, j8, w, lane);
#pragma unroll
  for (int ds = 0; ds < 2; ++ds) p3_issue_w(p, j, ds, w, vo);
  wait_flags(flag_ptr(ctl, 0, dom, (j & 8) + (lane & 7)), tag);
  STAMP(4);
  v4u hkeep[2];
  v4u csv4[4];
  phase2(p, L, rs, rsv, p64, j8, w, lane, hkeep, j, vo, dom, csv4 TSPASS);
  STAMP(5);
  WAIT_VM_BAR(0);
  if (tid == 0) __hip_atomic_store(flag_ptr(ctl, 1, dom, j), tag, __ATOMIC_RELAXED, __HIP_MEMORY_SCOPE_AGENT);
  STAMP(6);
#pragma unroll
  for (int ds = 3; ds < 5; ++ds) p3_issue_w(p, j, ds, w, vo);
  cs_finish(L, rs, j, dom, w, lane, csv4);
  wait_flags(flag_ptr(ctl, 1, dom, lane & 15), tag);
  STAMP(7);
  v4u wr[8][2];
  phase3(p, L, rs, rsv, ctl, tag, dom, j, w, lane, j8, vo, wr TSPASS);
  STAMP(8);
  WAIT_VM_BAR(0);
  if (tid == 0) __hip_atomic_store(flag_ptr(ctl, 2, dom, j), tag, __ATOMIC_RELAXED, __HIP_MEMORY_SCOPE_AGENT);
  STAMP(9);
  {
    unsigned wv4[4];
    wrow_voffs(lane, wv4);
    const u16* wdb = p.wd + (448 * w + 256) * 512 + j8 * 64;
    static_for<6>([&](auto G) {
      constexpr int gq = G;
      set_m0((unsigned)(P4_LATE + w * P4_WAVE + gq * 4096));
      static_for<4>([&](auto I) { wrow_issue<I>(wdb + gq * 32 * 512, wv4); });
    });
  }
  wait_flags(flag_ptr(ctl, 2, dom, lane & 15), tag);
  STAMP(10);
  if (b == 0 && tid == 0) __hip_atomic_store(ctl + FL_EPOCH, tag, __ATOMIC_RELAXED, __HIP_MEMORY_SCOPE_AGENT);
  phase4(p, L, rsv, p64, j8, w, lane, hkeep, wr TSPASS);
  STAMP(17);
  asm volatile(".Lmk_code_end:" ::: "memory");
}

extern "C" void kernel_launch(void* const* d_in, const int* in_sizes, int n_in, void* d_out, int out_size,
                              void* d_ws, size_t ws_size, hipStream_t stream) {
  Params p;
  p.x = (const u16*)d_in[0];
  p.wqkv = (const u16*)d_in[1];
  p.wo = (const u16*)d_in[2];
  p.wg = (const u16*)d_in[4];
  p.wu = (const u16*)d_in[5];
  p.wd = (const u16*)d_in[6];
  p.out = (u16*)d_out;
  p.ws = (unsigned char*)d_ws;
  (void)hipFuncSetAttribute((const void*)mk_kernel, hipFuncAttributeMaxDynamicSharedMemorySize, LDS_BYTES);
  mk_kernel<<<256, 256, LDS_BYTES, stream>>>(p);
}
```

```cpp
#include <hip/hip_runtime.h>
#include <stdint.h>
#include <utility>

#define OUT_AUX 0
#define MK_STAMP 0

typedef unsigned short u16;
typedef unsigned v4u __attribute__((ext_vector_type(4)));
typedef unsigned v2u __attribute__((ext_vector_type(2)));
typedef short v8s __attribute__((ext_vector_type(8)));
typedef short v4s __attribute__((ext_vector_type(4)));
typedef float f32x2 __attribute__((ext_vector_type(2)));
typedef float f32x4 __attribute__((ext_vector_type(4)));
typedef float f32x16 __attribute__((ext_vector_type(16)));
typedef __bf16 bf16x2 __attribute__((ext_vector_type(2)));
#define GAS __attribute__((address_space(1)))
#define LAS __attribute__((address_space(3)))
#define DEV __device__ __forceinline__

constexpr unsigned WS_FLAGS = 0;
constexpr unsigned WS_O = 262144;
constexpr unsigned WS_H = WS_O + 2097152;
constexpr unsigned WS_HS = WS_H + 2097152;
constexpr unsigned WS_G = WS_HS + 262144;
constexpr unsigned WS_CS = WS_G + 64 * 112 * 1024;
constexpr unsigned WS_STAMP = WS_CS + 16 * 16 * 224 * 4;
constexpr unsigned WS_BYTES = WS_STAMP + 256 * 4 * 48 * 8;
constexpr int FL_EPOCH = 0, FL_F = 1024;

constexpr int LDS_BYTES = 163840;
constexpr int AUX_SC1 = 16;

struct Params {
  const u16* x; const u16* wqkv; const u16* wo; const u16* wg; const u16* wu; const u16* wd;
  u16* out; unsigned char* ws;
};

template <class F, int... I> DEV void static_for_impl(F&& f, std::integer_sequence<int, I...>) { (f(std::integral_constant<int, I>{}), ...); }
template <int N, class F> DEV void static_for(F&& f) { static_for_impl(f, std::make_integer_sequence<int, N>{}); }

DEV float bf_lo(unsigned u) { return __uint_as_float(u << 16); }
DEV float bf_hi(unsigned u) { return __uint_as_float(u & 0xffff0000u); }
DEV unsigned pk_bf16(float a, float b) {
  bf16x2 p; p[0] = (__bf16)a; p[1] = (__bf16)b;
  return __builtin_bit_cast(unsigned, p);
}
DEV f32x2 pk2(float a, float b) { return f32x2{a, b}; }
DEV unsigned cvt2(f32x2 v) { return pk_bf16(v.x, v.y); }
DEV float dot2(unsigned a, unsigned b, float c) {
  return __builtin_amdgcn_fdot2_f32_bf16(__builtin_bit_cast(bf16x2, a), __builtin_bit_cast(bf16x2, b), c, false);
}
DEV f32x4 mfma16(v4u a, v4u b, f32x4 c) {
  return __builtin_amdgcn_mfma_f32_16x16x32_bf16(__builtin_bit_cast(v8s, a), __builtin_bit_cast(v8s, b), c, 0, 0, 0);
}
DEV f32x4 mfma16k16(v2u a, v2u b, f32x4 c) {
  return __builtin_amdgcn_mfma_f32_16x16x16bf16_1k(__builtin_bit_cast(v4s, a), __builtin_bit_cast(v4s, b), c, 0, 0, 0);
}
DEV f32x16 mfma32(v4u a, v4u b, f32x16 c) {
  return __builtin_amdgcn_mfma_f32_32x32x16_bf16(__builtin_bit_cast(v8s, a), __builtin_bit_cast(v8s, b), c, 0, 0, 0);
}
DEV v2u tr_read(LAS unsigned char* p) {
  return __builtin_bit_cast(v2u, __builtin_amdgcn_ds_read_tr16_b64_v4i16((LAS v4s*)p));
}
DEV v4u tr_read2(LAS unsigned char* p0, LAS unsigned char* p1) {
  v2u a = tr_read(p0), b = tr_read(p1);
  return v4u{a.x, a.y, b.x, b.y};
}
DEV void dma16(const void* sbase, unsigned voff, unsigned lds_dst) {
  lds_dst = __builtin_amdgcn_readfirstlane(lds_dst);
  asm volatile("s_mov_b32 m0, %2\n\ts_nop 0\n\tglobal_load_lds_dwordx4 %0, %1"
               :: "v"(voff), "s"(sbase), "s"(lds_dst) : "memory");
}
DEV void set_m0(unsigned v) {
  v = __builtin_amdgcn_readfirstlane(v);
  asm volatile("s_mov_b32 m0, %0\n\ts_nop 0" :: "s"(v) : "memory");
}
DEV const void* uni64(const void* p) {
  const unsigned long long a = (unsigned long long)p;
  const unsigned lo = __builtin_amdgcn_readfirstlane((unsigned)a), hi = __builtin_amdgcn_readfirstlane((unsigned)(a >> 32));
  unsigned long long u = ((unsigned long long)hi << 32) | lo;
  asm volatile("s_nop 4" : "+s"(u));
  return (const void*)u;
}
template <int IMM> DEV void dma16i(const void* sbase, unsigned voff) {
  asm volatile("global_load_lds_dwordx4 %0, %1 offset:%c2" :: "v"(voff), "s"(sbase), "i"(IMM) : "memory");
}
template <int IMM> DEV void bload_a(v4u& dst, v4u rsrc, unsigned voff, unsigned soff) {
  asm volatile("buffer_load_dwordx4 %0, %1, %2, %3 offen offset:%c4 sc1" : "=a"(dst) : "v"(voff), "s"(rsrc), "s"(soff), "i"(IMM) : "memory");
}
template <int IMM> DEV void bload_v(v4u& dst, v4u rsrc, unsigned voff, unsigned soff) {
  asm volatile("buffer_load_dwordx4 %0, %1, %2, %3 offen offset:%c4 sc1" : "=v"(dst) : "v"(voff), "s"(rsrc), "s"(soff), "i"(IMM) : "memory");
}
DEV void bload1_v(unsigned& dst, v4u rsrc, unsigned voff, unsigned soff) {
  asm volatile("buffer_load_dword %0, %1, %2, %3 offen sc1" : "=v"(dst) : "v"(voff), "s"(rsrc), "s"(soff) : "memory");
}
template <int N> DEV void wait_vm_a(v4u& x) { asm volatile("s_waitcnt vmcnt(%c1)" : "+a"(x) : "i"(N) : "memory"); }
template <int N> DEV void wait_vm_a2(v4u& x, v4u& y) { asm volatile("s_waitcnt vmcnt(%c2)" : "+a"(x), "+a"(y) : "i"(N) : "memory"); }
template <int N> DEV void wait_vm_v2(v4u& x, v4u& y) { asm volatile("s_waitcnt vmcnt(%c2)" : "+v"(x), "+v"(y) : "i"(N) : "memory"); }
template <int N> DEV void wait_vm_v1(unsigned& x) { asm volatile("s_waitcnt vmcnt(%c1)" : "+v"(x) : "i"(N) : "memory"); }
#define PIN_V(x) asm volatile("" : "+v"(x))
#define PIN_A(x) asm volatile("" : "+a"(x))
#define WAIT_VM(n) asm volatile("s_waitcnt vmcnt(" #n ")" ::: "memory")
#define WAIT_VM_BAR(n) asm volatile("s_waitcnt vmcnt(" #n ") lgkmcnt(0)\n\ts_barrier" ::: "memory")
#define BAR_LDS() asm volatile("s_waitcnt lgkmcnt(0)\n\ts_barrier" ::: "memory")
#define WAIT_LGKM() asm volatile("s_waitcnt lgkmcnt(0)" ::: "memory")
#define SCHED() __builtin_amdgcn_sched_barrier(0)

DEV float xsum16_32(float v) {
  auto r = __builtin_amdgcn_permlane16_swap(__float_as_uint(v), __float_as_uint(v), false, false);
  v = __uint_as_float(r[0]) + __uint_as_float(r[1]);
  auto s = __builtin_amdgcn_permlane32_swap(__float_as_uint(v), __float_as_uint(v), false, false);
  return __uint_as_float(s[0]) + __uint_as_float(s[1]);
}
DEV float xmax16_32(float v) {
  auto r = __builtin_amdgcn_permlane16_swap(__float_as_uint(v), __float_as_uint(v), false, false);
  v = fmaxf(__uint_as_float(r[0]), __uint_as_float(r[1]));
  auto s = __builtin_amdgcn_permlane32_swap(__float_as_uint(v), __float_as_uint(v), false, false);
  return fmaxf(__uint_as_float(s[0]), __uint_as_float(s[1]));
}
DEV float xsum32(float v) {
  auto s = __builtin_amdgcn_permlane32_swap(__float_as_uint(v), __float_as_uint(v), false, false);
  return __uint_as_float(s[0]) + __uint_as_float(s[1]);
}

#define STAMP(i) do {} while (0)
#define TSARG
#define TSPASS

DEV GAS unsigned* flag_ptr(GAS unsigned* ctl, int ph, int dom, int j) { return ctl + FL_F + ((ph * 16 + dom) * 16 + j) * 32; }
DEV void wait_flags(GAS unsigned* word, unsigned tag) {
  for (unsigned spins = 0; spins < (1u << 20); ++spins) {
    const unsigned v = __hip_atomic_load(word, __ATOMIC_RELAXED, __HIP_MEMORY_SCOPE_AGENT);
    if (__all(v == tag)) break;
    __builtin_amdgcn_s_sleep(1);
  }
  __builtin_amdgcn_fence(__ATOMIC_ACQUIRE, "wavefront");
  asm volatile("" ::: "memory");
}

constexpr int P1_X = 0;
constexpr int P1_W = 81920;
constexpr int P1_WSLOT = 24576;
constexpr int P1_MISC = 155648;
constexpr int P2_W = 0;
constexpr int P2_X = 151936;
constexpr int CS_BUF = 160128;
constexpr int P3_UP = 14464;
constexpr int P3_CSP = 151936;
DEV constexpr int p3_slot_addr(int ds) { return (ds % 5) == 0 ? 65536 : (ds % 5) == 1 ? 94336 : (ds % 5) == 2 ? 123136 : (ds % 5) == 3 ? 0 : 28800; }
constexpr int P4_EARLY = 16384;
constexpr int P4_LATE = 65536, P4_WAVE = 24576;

DEV unsigned wrow_voff(int lane) { return (unsigned)(lane >> 3) * 1024u + (unsigned)((lane & 7) ^ (((lane >> 4) & 1) << 2)) * 16u; }

DEV void p1_xvoff(int tok0, int w, int lane, unsigned (&vx)[5]) {
#pragma unroll
  for (int i = 0; i < 5; ++i) {
    const int rg = 5 * (w & 1) + i;
    const int row = 8 * rg + (lane >> 3);
    int tok = tok0 + row; tok = tok < 0 ? 0 : tok;
    const int ch = (lane & 7) ^ ((row >> 1) & 7);
    vx[i] = (unsigned)tok * 1024u + (unsigned)ch * 16u + 4096u - (unsigned)((i - 2) * 1024);
  }
}
DEV void p1_x_setup(int sp0, int w) { set_m0((unsigned)(P1_X + (sp0 + (w >> 1)) * 10240 + (5 * (w & 1) + 2) * 1024)); }
template <int I> DEV void p1_issue_x1(const Params& p, int sp0, int w, int lane, const unsigned (&vx)[5]) {
  const unsigned char* base = (const unsigned char*)p.x + (sp0 + (w >> 1)) * 128 - 4096;
  if constexpr (I == 0) { if ((w & 1) || lane >= 8) dma16i<(I - 2) * 1024>(base, vx[I]); }
  else dma16i<(I - 2) * 1024>(base, vx[I]);
}
DEV void p1_wvoff(int w, int lane, unsigned (&vo)[6]) {
#pragma unroll
  for (int i = 0; i < 6; ++i) {
    const int idx = 6 * w + i;
    const int part = idx >> 3, rg = idx & 7;
    const int row = 8 * rg + (lane >> 3);
    const int f = ((row >> 1) & 1) | (((row >> 3) & 1) << 1);
    const int ch = (lane & 7) ^ (f << 1);
    vo[i] = (unsigned)row * 3072u + (unsigned)part * 1024u + (unsigned)ch * 16u + 4096u - (unsigned)((i - 3) * 1024);
  }
}
DEV void p1_w_setup(int slot, int w) { set_m0((unsigned)(P1_W + slot * P1_WSLOT + (6 * w + 3) * 1024)); }
template <int I> DEV void p1_issue_w1(const Params& p, int hg, int st, const unsigned (&vo)[6]) {
  const unsigned char* base = (const unsigned char*)p.wqkv + st * 64 * 3072 + hg * 128 - 4096;
  dma16i<(I - 3) * 1024>(base, vo[I]);
}
DEV void wrow_voffs(int lane, unsigned (&v)[4]) {
#pragma unroll
  for (int i = 0; i < 4; ++i) v[i] = wrow_voff(lane) + 7168u * i;
}
template <int I> DEV void wrow_issue(const u16* rowbase  , const unsigned (&v)[4]) { dma16i<I * 1024>(rowbase, v[I]); }

struct P1Frag { v4u xf[5]; v4u wq, wk, wv; };

DEV void phase1(const Params& p, LAS unsigned char* L, __amdgpu_buffer_rsrc_t rs, int p64, int hg, int w, int lane,
                const unsigned (&wvo)[6], const unsigned (&vx)[5], const unsigned (&wv4)[4], const void* pfb TSARG) {
  const int s0 = (p64 & 3) * 2;
  const int g = lane >> 4, tk = lane & 15, q4 = (lane & 15) >> 2, pp = lane & 3;
  const int fsw = ((q4 >> 1) & 1) | ((g & 1) << 1);
  const int wbase = P1_W + (8 * g + q4) * 128 + (((2 * w + (pp >> 1)) ^ (fsw << 1)) * 16) + 8 * (pp & 1);
  const int xsw = (tk >> 1) & 7;
  const int xb0 = P1_X + tk * 128 + ((g ^ xsw) * 16), xb1 = P1_X + tk * 128 + (((4 + g) ^ xsw) * 16);
  const int xbw = (w & 1) ? xb1 : xb0;

  f32x4 aq[5], ak[5], av[5], ssum[5], sgr[5];
#pragma unroll
  for (int t = 0; t < 5; ++t) {
    aq[t] = f32x4{0, 0, 0, 0}; ak[t] = f32x4{0, 0, 0, 0}; av[t] = f32x4{0, 0, 0, 0}; ssum[t] = f32x4{0, 0, 0, 0}; sgr[t] = f32x4{0, 0, 0, 0};
  }
  const v4u ones = {0x3f803f80u, 0x3f803f80u, 0x3f803f80u, 0x3f803f80u};
  LAS f32x2* part = (LAS f32x2*)(L + P1_MISC);
  LAS float* fmean = (LAS float*)(L + P1_MISC + 2560 + w * 640);
  LAS float* frstd = fmean + 80;
  const u16* wob = p.wo + (128 * w) * 512 + hg * 64;
  auto dmo = [&](auto G) {
    constexpr int gq = G;
    set_m0((unsigned)(P2_W + (128 * w + 32 * gq) * 128));
    static_for<4>([&](auto I) { wrow_issue<I>(wob + gq * 32 * 512, wv4); });
  };
  float m1[5], r1[5];
  f32x4 mv[5], rv[5];

  WAIT_VM_BAR(6);
  STAMP(11);
  P1Frag cur;
  {
    LAS unsigned char* xs = L + s0 * 10240 + xb0;
    LAS unsigned char* wsl = L + wbase;
    cur.wk = tr_read2(wsl + 8192, wsl + 8192 + 512);
    cur.wv = tr_read2(wsl + 16384, wsl + 16384 + 512);
    cur.wq = tr_read2(wsl, wsl + 512);
#pragma unroll
    for (int t = 0; t < 5; ++t) cur.xf[t] = *(LAS v4u*)(xs + t * 2048);
  }
  SCHED();
  static_for<16>([&](auto KS) {
    constexpr int ks = KS;
    if constexpr (ks == 1) { WAIT_VM_BAR(5); }
    else if constexpr (ks == 3 || ks == 7) { WAIT_VM_BAR(6); }
    else if constexpr (ks == 11) { WAIT_VM_BAR(7); }
    else if constexpr (ks == 5 || ks == 9) { WAIT_VM_BAR(11); }
    else if constexpr (ks == 13) { WAIT_VM_BAR(1); }
    else if constexpr (ks == 15) { BAR_LDS(); }
    if constexpr (ks == 10) {
      const int nl = w == 0 ? 16 : 56;
      const unsigned off = (unsigned)((int)blockIdx.x * nl + (lane < nl ? lane : nl - 1)) * 128u;
      asm volatile("s_mov_b32 m0, %2\n\ts_nop 0\n\tglobal_load_lds_dword %0, %1" :: "v"(off), "s"(pfb), "s"(160768u) : "memory");
    }
    constexpr bool isx = (ks == 0 || ks == 4 || ks == 8);
    constexpr int wst = ks == 1 ? 2 : ks == 2 ? 3 : ks == 3 ? 4 : ks == 5 ? 5 : ks == 7 ? 6 : ks == 9 ? 7 : -1;
    if constexpr (isx) p1_x_setup((s0 + 2 + ks / 2) & 7, w);
    if constexpr (wst >= 0) p1_w_setup(wst % 3, w);
    auto dma = [&](auto D) {
      constexpr int d = D;
      if constexpr (isx && d < 5) p1_issue_x1<d>(p, (s0 + 2 + ks / 2) & 7, w, lane, vx);
      if constexpr (wst >= 0) p1_issue_w1<d>(p, hg, (s0 + wst) & 7, wvo);
      if constexpr (ks == 15 && d == 2) dmo(std::integral_constant<int, 0>{});
    };
    P1Frag nxt;
    constexpr int kn = ks + 1, itn = kn >> 1, k2n = kn & 1;
    LAS unsigned char* xs = L + ((s0 + itn) & 7) * 10240 + (k2n ? xb1 : xb0);
    LAS unsigned char* wsl = L + wbase + (itn % 3) * P1_WSLOT + k2n * 4096;
    constexpr int G = ks < 8 ? (ks >> 2) : ks < 11 ? 2 : 3;
    constexpr int st0 = ks < 8 ? (ks & 3) : ks == 8 ? 0 : ks == 9 ? 2 : ks == 10 ? 4 : ks == 11 ? 0 : 2;
    constexpr int nst = ks < 8 ? ((ks & 3) == 3 ? 2 : 1) : ks == 8 ? 2 : ks == 9 ? 2 : ks == 10 ? 1 : ks == 11 ? 2 : ks == 12 ? 3 : 0;
    LAS unsigned char* sx = L + ((s0 + 2 * G + (w >> 1)) & 7) * 10240 + xbw;
    v4u sv[3];
    dma(std::integral_constant<int, 0>{});
    static_for<5>([&](auto T) {
      constexpr int t = T;
      if constexpr (ks < 15) {
        if constexpr (t == 0) nxt.wk = tr_read2(wsl + 8192, wsl + 8192 + 512);
        if constexpr (t == 1) nxt.wv = tr_read2(wsl + 16384, wsl + 16384 + 512);
        if constexpr (t == 2) nxt.wq = tr_read2(wsl, wsl + 512);
        nxt.xf[t] = *(LAS v4u*)(xs + t * 2048);
      }
      if constexpr (t < nst) sv[t] = *(LAS v4u*)(sx + (st0 + t) * 2048);
      if constexpr (ks == 15) {
        m1[t] = fmean[t * 16 + tk]; r1[t] = frstd[t * 16 + tk];
        mv[t] = *(LAS f32x4*)(fmean + t * 16 + 4 * g); rv[t] = *(LAS f32x4*)(frstd + t * 16 + 4 * g);
      }
      ak[t] = mfma16(cur.wk, cur.xf[t], ak[t]);
      av[t] = mfma16(cur.xf[t], cur.wv, av[t]);
      aq[t] = mfma16(cur.wq, cur.xf[t], aq[t]);
      if constexpr (t == 4) {
        static_for<3>([&](auto I) {
          constexpr int i = I;
          if constexpr (i < nst) { ssum[st0 + i] = mfma16(ones, sv[i], ssum[st0 + i]); sgr[st0 + i] = mfma16(sv[i], sv[i], sgr[st0 + i]); }
        });
      }
      if constexpr (ks == 13 && t < 2) {
        const int tok = t * 64 + lane;
        if (tok < 80) {
          const f32x2 a = (part[tok] + part[80 + tok]) + (part[160 + tok] + part[240 + tok]);
          const float mean = a.x * (1.f / 512.f);
          const float var = fmaxf(a.y * (1.f / 512.f) - mean * mean, 0.f);
          fmean[tok] = mean; frstd[tok] = __builtin_amdgcn_rsqf(var + 1e-5f);
        }
      }
      SCHED();
      dma(std::integral_constant<int, t + 1>{});
      SCHED();
    });
    if constexpr (ks == 12) {
      const int r = tk & 3;
#pragma unroll
      for (int t = 0; t < 5; ++t) {
        const float d = r == 0 ? sgr[t][0] : r == 1 ? sgr[t][1] : r == 2 ? sgr[t][2] : sgr[t][3];
        if ((tk >> 2) == g) part[w * 80 + t * 16 + tk] = f32x2{ssum[t][0], d};
      }
      SCHED();
    }
    if constexpr (ks < 15) cur = nxt;
  });
  STAMP(12);
  f32x2 csq[2], csk[2];
#pragma unroll
  for (int r = 0; r < 4; ++r) {
    csq[r >> 1][r & 1] = __uint_as_float(__builtin_amdgcn_update_dpp(0, __float_as_uint(aq[0][r]), 0x150, 0xf, 0xf, false));
    csk[r >> 1][r & 1] = __uint_as_float(__builtin_amdgcn_update_dpp(0, __float_as_uint(ak[0][r]), 0x150, 0xf, 0xf, false));
  }
  float csv;
  {
    auto a = __builtin_amdgcn_permlane16_swap(__float_as_uint(av[0][0]), __float_as_uint(av[0][0]), false, false);
    auto c = __builtin_amdgcn_permlane32_swap(a[0], a[0], false, false);
    csv = __uint_as_float(c[0]);
  }
  dmo(std::integral_constant<int, 1>{});
  v2u qp[4], kp[5], vp[5];
  const float sc = 0.25f * 1.44269504088896f;
  const f32x2 c2 = {csv, csv};
#pragma unroll
  for (int t = 0; t < 5; ++t) {
    const f32x2 m2 = {m1[t], m1[t]}, r2 = {r1[t], r1[t]};
    kp[t].x = cvt2((pk2(ak[t][0], ak[t][1]) - m2 * csk[0]) * r2);
    kp[t].y = cvt2((pk2(ak[t][2], ak[t][3]) - m2 * csk[1]) * r2);
    if (t > 0) {
      qp[t - 1].x = cvt2(pk2(aq[t][0], aq[t][1]) - m2 * csq[0]);
      qp[t - 1].y = cvt2(pk2(aq[t][2], aq[t][3]) - m2 * csq[1]);
    }
  }
  SCHED();
  STAMP(29);
  const f32x4 z = {0, 0, 0, 0};
  f32x4 sp[4], scu[4];
#pragma unroll
  for (int t = 0; t < 4; ++t) { sp[t] = mfma16k16(kp[t], qp[t], z); scu[t] = mfma16k16(kp[t + 1], qp[t], z); }
  SCHED();
  dmo(std::integral_constant<int, 2>{});
#pragma unroll
  for (int t = 0; t < 5; ++t) {
    vp[t].x = cvt2((pk2(av[t][0], av[t][1]) - pk2(mv[t][0], mv[t][1]) * c2) * pk2(rv[t][0], rv[t][1]));
    vp[t].y = cvt2((pk2(av[t][2], av[t][3]) - pk2(mv[t][2], mv[t][3]) * c2) * pk2(rv[t][2], rv[t][3]));
  }
  SCHED();
  f32x2 biasp[2], biasc[2];
#pragma unroll
  for (int r = 0; r < 4; ++r) {
    const int dc = tk - 4 * g - r, dp = dc + 16;
    biasc[r >> 1][r & 1] = (dc >= 0 && dc <= 14 && !(dc & 1)) ? 0.f : -1e30f;
    biasp[r >> 1][r & 1] = (dp <= 14 && !(dp & 1)) ? 0.f : -1e30f;
  }
  f32x2 a[4][4];
  float mx[4], sm[4];
#pragma unroll
  for (int t = 0; t < 4; ++t) {
    const float sq = sc * r1[t + 1];
    const f32x2 s2v = {sq, sq};
    f32x2 bp0 = biasp[0], bp1 = biasp[1];
    if (t == 0 && p64 == 0) { bp0 = f32x2{-1e30f, -1e30f}; bp1 = bp0; }
    a[t][0] = pk2(sp[t][0], sp[t][1]) * s2v + bp0; a[t][1] = pk2(sp[t][2], sp[t][3]) * s2v + bp1;
    a[t][2] = pk2(scu[t][0], scu[t][1]) * s2v + biasc[0]; a[t][3] = pk2(scu[t][2], scu[t][3]) * s2v + biasc[1];
  }
#pragma unroll
  for (int t = 0; t < 4; ++t)
    mx[t] = fmaxf(fmaxf(__builtin_fmaxf(a[t][0].x, a[t][0].y), fmaxf(a[t][1].x, a[t][1].y)), fmaxf(fmaxf(a[t][2].x, a[t][2].y), fmaxf(a[t][3].x, a[t][3].y)));
  SCHED();
  auto xred2 = [&](float& A, float& B, auto op) {
    auto s1 = __builtin_amdgcn_permlane16_swap(__float_as_uint(A), __float_as_uint(B), false, false);
    const float u = op(__uint_as_float(s1[0]), __uint_as_float(s1[1]));
    auto s2_ = __builtin_amdgcn_permlane32_swap(__float_as_uint(u), __float_as_uint(u), false, false);
    const float v = op(__uint_as_float(s2_[0]), __uint_as_float(s2_[1]));
    auto s3 = __builtin_amdgcn_permlane16_swap(__float_as_uint(v), __float_as_uint(v), false, false);
    A = __uint_as_float(s3[0]); B = __uint_as_float(s3[1]);
  };
  auto fmx = [](float x, float y) { return fmaxf(x, y); };
  auto fad = [](float x, float y) { return x + y; };
  STAMP(30);
  xred2(mx[0], mx[1], fmx); xred2(mx[2], mx[3], fmx);
  SCHED();
  dmo(std::integral_constant<int, 3>{});
#pragma unroll
  for (int t = 0; t < 4; ++t) {
    const f32x2 m2 = {mx[t], mx[t]};
#pragma unroll
    for (int q = 0; q < 4; ++q) a[t][q] -= m2;
  }
#pragma unroll
  for (int t = 0; t < 4; ++t)
#pragma unroll
    for (int q = 0; q < 4; ++q) { a[t][q].x = __builtin_amdgcn_exp2f(a[t][q].x); a[t][q].y = __builtin_amdgcn_exp2f(a[t][q].y); }
  SCHED();
  v4u pf[4];
#pragma unroll
  for (int t = 0; t < 4; ++t) {
    const f32x2 sv = (a[t][0] + a[t][1]) + (a[t][2] + a[t][3]);
    sm[t] = sv.x + sv.y;
    pf[t] = v4u{cvt2(a[t][0]), cvt2(a[t][1]), cvt2(a[t][2]), cvt2(a[t][3])};
  }
  f32x4 o[4];
#pragma unroll
  for (int t = 0; t < 4; ++t) {
    const v4u vf = {vp[t].x, vp[t].y, vp[t + 1].x, vp[t + 1].y};
    o[t] = mfma16(vf, pf[t], z);
  }
  SCHED();
  STAMP(31);
  xred2(sm[0], sm[1], fad); xred2(sm[2], sm[3], fad);
  v2u ob[4];
#pragma unroll
  for (int t = 0; t < 4; ++t) {
    const float inv = __builtin_amdgcn_rcpf(sm[t]);
    const f32x2 i2 = {inv, inv};
    ob[t] = v2u{cvt2(pk2(o[t][0], o[t][1]) * i2), cvt2(pk2(o[t][2], o[t][3]) * i2)};
  }
  const int head = 4 * hg + w;
#pragma unroll
  for (int u = 0; u < 2; ++u) {
    auto sx = __builtin_amdgcn_permlane16_swap(ob[2 * u].x, ob[2 * u + 1].x, false, false);
    auto sy = __builtin_amdgcn_permlane16_swap(ob[2 * u].y, ob[2 * u + 1].y, false, false);
    const v4u o4 = {sx[0], sy[0], sx[1], sy[1]};
    const int voff = ((g >> 1) * 32 + (g & 1) * 16 + tk) * 16;
    __builtin_amdgcn_raw_buffer_store_b128(o4, rs, voff, (int)WS_O + ((2 * p64 + u) * 32 + head) * 1024, AUX_SC1);
  }
}

DEV void cs_load(const Params& p, int c, int r, int tid, v4u (&v)[4]) {
  const int rg = tid >> 5, c0 = tid & 31, cc = c0 < 28 ? c0 : 27;
  const u16* base = (cc < 14 ? p.wg : p.wu) + (size_t)(32 * r + 4 * rg) * 1792 + 112 * c + (cc < 14 ? cc : cc - 14) * 8;
#pragma unroll
  for (int i = 0; i < 4; ++i) v[i] = *(const GAS v4u*)(base + i * 1792);
}
DEV void cs_finish(LAS unsigned char* L, __amdgpu_buffer_rsrc_t rs, int c, int r, int w, int lane, const v4u (&v)[4]) {
  LAS float* buf = (LAS float*)(L + CS_BUF);
  const int c0 = lane & 31;
  f32x4 a, b;
#pragma unroll
  for (int e = 0; e < 2; ++e) {
    a[2 * e] = xsum32((bf_lo(v[0][e]) + bf_lo(v[1][e])) + (bf_lo(v[2][e]) + bf_lo(v[3][e])));
    a[2 * e + 1] = xsum32((bf_hi(v[0][e]) + bf_hi(v[1][e])) + (bf_hi(v[2][e]) + bf_hi(v[3][e])));
    b[2 * e] = xsum32((bf_lo(v[0][2 + e]) + bf_lo(v[1][2 + e])) + (bf_lo(v[2][2 + e]) + bf_lo(v[3][2 + e])));
    b[2 * e + 1] = xsum32((bf_hi(v[0][2 + e]) + bf_hi(v[1][2 + e])) + (bf_hi(v[2][2 + e]) + bf_hi(v[3][2 + e])));
  }
  if (lane < 28) {
    *(LAS f32x4*)(buf + w * 224 + c0 * 8) = a;
    *(LAS f32x4*)(buf + w * 224 + c0 * 8 + 4) = b;
  }
  BAR_LDS();
  const int tid = w * 64 + lane;
  if (tid < 224) {
    const float s = (buf[tid] + buf[224 + tid]) + (buf[448 + tid] + buf[672 + tid]);
    __builtin_amdgcn_raw_buffer_store_b32(__float_as_uint(s), rs, tid * 4, (int)WS_CS + (c * 16 + r) * 896, AUX_SC1);
  }
}

DEV void p3_voffs(int lane, unsigned (&vo)[7]) {
#pragma unroll
  for (int i = 0; i < 7; ++i) {
    const int q = i * 64 + lane, row = q / 14, cc = q - 14 * row;
    vo[i] = (unsigned)row * 3584u + (unsigned)cc * 16u + 4096u - (unsigned)((i - 3) * 1024);
  }
}
DEV void p3_w_setup(int ds, int w) { set_m0((unsigned)(p3_slot_addr(ds) + (w & 1) * P3_UP + (w >> 1) * 7168 + 3 * 1024)); }
template <int I> DEV void p3_issue_w1(const void* wgu, int c, int ds, int w, const unsigned (&vo)[7]) {
  const unsigned char* base = (const unsigned char*)wgu + (64 * ds + 32 * (w >> 1)) * 3584 + 224 * c - 4096;
  dma16i<(I - 3) * 1024>(base, vo[I]);
}
DEV void p3_issue_w(const void* wgu, int c, int ds, int w, const unsigned (&vo)[7]) {
  p3_w_setup(ds, w);
  static_for<7>([&](auto I) { p3_issue_w1<I>(wgu, c, ds, w, vo); });
}

DEV void p2_issue_x(const Params& p, int p64, int nt, int w, int lane) {
  const unsigned vx = (unsigned)(lane >> 3) * 1024u + (unsigned)(((lane & 7) ^ (lane >> 3)) * 16);
  const u16* xb = p.x + (64 * p64 + 16 * w) * 512 + nt * 64;
  set_m0((unsigned)(P2_X + 16 * w * 128));
  dma16i<0>(xb, vx);
  dma16i<1024>(xb, vx + 7168u);
}

template <int WSTRIDE>
DEV f32x16 reduce4(f32x16 (&acc)[2][2], LAS unsigned char* red, int w, int lane) {
  WAIT_LGKM();
#pragma unroll
  for (int a = 0; a < 2; ++a)
#pragma unroll
    for (int b = 0; b < 2; ++b) {
      const int dest = a + 2 * b;
      if (dest != w) {
        const int slot = (dest - w - 1) & 3;
#pragma unroll
        for (int qd = 0; qd < 4; ++qd)
          *(LAS f32x4*)(red + w * WSTRIDE + (slot * 4 + qd) * 1024 + lane * 16) =
              f32x4{acc[a][b][4 * qd], acc[a][b][4 * qd + 1], acc[a][b][4 * qd + 2], acc[a][b][4 * qd + 3]};
      }
    }
  BAR_LDS();
  f32x16 r;
  if (w == 0) r = acc[0][0]; else if (w == 1) r = acc[1][0]; else if (w == 2) r = acc[0][1]; else r = acc[1][1];
#pragma unroll
  for (int s = 1; s < 4; ++s) {
    const int src = (w + s) & 3;
#pragma unroll
    for (int qd = 0; qd < 4; ++qd) {
      const f32x4 v = *(LAS f32x4*)(red + src * WSTRIDE + ((3 - s) * 4 + qd) * 1024 + lane * 16);
#pragma unroll
      for (int e = 0; e < 4; ++e) r[4 * qd + e] += v[e];
    }
  }
  return r;
}

DEV void phase2(const Params& p, LAS unsigned char* L, __amdgpu_buffer_rsrc_t rs, v4u rsv, int p64, int nt, int w, int lane, v4u (&hkeep)[2],
                int c3, const unsigned (&vo)[7], int dom, v4u (&csv4)[4], const void* wgu TSARG) {
  v4u of[2][8];
  const unsigned ob0 = WS_O + ((2 * p64) * 32 + 8 * w) * 1024, ob1 = ob0 + 32768;
  static_for<8>([&](auto I) {
    constexpr int i = I;
    bload_v<(i & 3) * 1024>(of[0][i], rsv, lane * 16, ob0 + (i >> 2) * 4096);
    bload_v<(i & 3) * 1024>(of[1][i], rsv, lane * 16, ob1 + (i >> 2) * 4096);
  });
  p3_issue_w(wgu, c3, 2, w, vo);
  SCHED();
  const int hh = lane >> 5, blk = (lane >> 4) & 1, q4 = (lane & 15) >> 2, pp = lane & 3;
  const int sw = ((q4 >> 1) & 1) << 2;
  LAS unsigned char* wb0 = L + P2_W + (128 * w + 8 * hh + q4) * 128 + (((2 * blk + (pp >> 1)) ^ sw) * 16) + 8 * (pp & 1);
  LAS unsigned char* wb1 = L + P2_W + (128 * w + 8 * hh + q4) * 128 + (((4 + 2 * blk + (pp >> 1)) ^ sw) * 16) + 8 * (pp & 1);
  f32x16 acc[2][2];
#pragma unroll
  for (int a = 0; a < 2; ++a)
#pragma unroll
    for (int b = 0; b < 2; ++b)
#pragma unroll
      for (int e = 0; e < 16; ++e) acc[a][b][e] = 0.f;
  v4u wa[8][2];
#pragma unroll
  for (int i = 0; i < 8; ++i) {
    wa[i][0] = tr_read2(wb0 + i * 2048, wb0 + i * 2048 + 512);
    wa[i][1] = tr_read2(wb1 + i * 2048, wb1 + i * 2048 + 512);
  }
  SCHED();
  static_for<8>([&](auto I) {
    constexpr int i = I;
    wait_vm_v2<7 + 16 - 2 * i - 2>(of[0][i], of[1][i]);
    acc[0][0] = mfma32(wa[i][0], of[0][i], acc[0][0]);
    acc[1][0] = mfma32(wa[i][1], of[0][i], acc[1][0]);
    acc[0][1] = mfma32(wa[i][0], of[1][i], acc[0][1]);
    acc[1][1] = mfma32(wa[i][1], of[1][i], acc[1][1]);
  });
  SCHED();
  STAMP(13);
  cs_load(p, c3, dom, w * 64 + lane, csv4);
  SCHED();
  f32x16 r = reduce4<16384>(acc, L + P2_W, w, lane);
  const int row = 32 * (w >> 1) + (lane & 31);
  float hv[16];
  float s1 = 0.f, s2 = 0.f;
#pragma unroll
  for (int qd = 0; qd < 4; ++qd) {
    const v2u xr = *(LAS v2u*)(L + P2_X + row * 128 + (((4 * (w & 1) + qd) ^ (row & 7)) * 16) + 8 * hh);
    const unsigned y01 = pk_bf16(r[4 * qd + 0], r[4 * qd + 1]), y23 = pk_bf16(r[4 * qd + 2], r[4 * qd + 3]);
    const unsigned h01 = pk_bf16(bf_lo(xr.x) + bf_lo(y01), bf_hi(xr.x) + bf_hi(y01));
    const unsigned h23 = pk_bf16(bf_lo(xr.y) + bf_lo(y23), bf_hi(xr.y) + bf_hi(y23));
    hkeep[qd >> 1][2 * (qd & 1)] = h01; hkeep[qd >> 1][2 * (qd & 1) + 1] = h23;
    hv[4 * qd + 0] = bf_lo(h01); hv[4 * qd + 1] = bf_hi(h01); hv[4 * qd + 2] = bf_lo(h23); hv[4 * qd + 3] = bf_hi(h23);
  }
  const int tile32 = 2 * p64 + (w >> 1);
  const int nt32 = 2 * nt + (w & 1);
#pragma unroll
  for (int ks = 0; ks < 2; ++ks)
    __builtin_amdgcn_raw_buffer_store_b128(hkeep[ks], rs, lane * 16, (int)WS_H + (tile32 * 32 + 2 * nt32 + ks) * 1024, AUX_SC1);
#pragma unroll
  for (int i = 0; i < 16; ++i) { s1 += hv[i]; s2 += hv[i] * hv[i]; }
  s1 = xsum32(s1); s2 = xsum32(s2);
  if (hh == 0) {
    v2u st = {__float_as_uint(s1), __float_as_uint(s2)};
    __builtin_amdgcn_raw_buffer_store_b64(st, rs, (lane & 31) * 16, (int)WS_HS + (tile32 * 8 + (nt32 >> 1)) * 512 + (nt32 & 1) * 8, AUX_SC1);
  }
}

constexpr int P3_KM = 20;
constexpr int p3_nl(int ks) { return ks < 12 ? 2 : 0; }
constexpr int p3_nd(int ks) { return (ks >= 4 && ks < 16) ? ((ks & 3) == 3 ? 1 : 2) : 0; }
constexpr int p3_nx(int ks) { return (ks == 7 || (ks >= 12 && ks < 16)) ? 1 : 0; }
constexpr int p3_cum(int ks) { int c = 12; for (int k = 0; k < ks; ++k) c += p3_nl(k) + p3_nd(k) + p3_nx(k); return c; }
constexpr int p3_pos(int i) { return i < 8 ? 4 + i : p3_cum((i - 8) / 2) + ((i - 8) & 1); }

DEV void phase3(const Params& p, LAS unsigned char* L, __amdgpu_buffer_rsrc_t rs, v4u rsv, GAS unsigned* ctl, unsigned tag, int p128, int c, int w, int lane,
                int nt4, const unsigned (&vo)[7], v4u (&wr)[8][2], const void* wgu TSARG) {
  const int tile32 = 4 * p128 + w;
  const int hh = lane >> 5, blk = (lane >> 4) & 1, q4 = (lane & 15) >> 2, pp = lane & 3;
  v4u hb[32];
  v4u hs[4];
  const unsigned hbase = WS_H + tile32 * 32768;
  static_for<4>([&](auto I) { bload_v<I * 512>(hs[I], rsv, (4 * hh) * 512 + (lane & 31) * 16, WS_HS + tile32 * 4096); });
  static_for<8>([&](auto I) { bload_a<(I & 3) * 1024>(hb[I], rsv, lane * 16, hbase + (I >> 2) * 4096); });
  SCHED();
  const int abase = blk * P3_UP + (4 * hh + q4) * 224 + pp * 8;
  int ab0 = abase, ab1 = abase + 65536, ab2 = abase + 123136;
  PIN_V(ab0); PIN_V(ab1); PIN_V(ab2);
  auto slot_ptr = [&](int ds) -> LAS unsigned char* {
    const int a = p3_slot_addr(ds);
    return a == 123136 ? L + ab2 : a >= 65536 ? L + ab1 + (a - 65536) : L + ab0 + a;
  };
  f32x16 acc[7];
#pragma unroll
  for (int t = 0; t < 7; ++t)
#pragma unroll
    for (int e = 0; e < 16; ++e) acc[t][e] = 0.f;
  WAIT_VM_BAR(12);
  if (w == 0 && lane == 0) __hip_atomic_store(flag_ptr(ctl, 3, p128, c), tag, __ATOMIC_RELAXED, __HIP_MEMORY_SCOPE_AGENT);
  STAMP(14);
  v4u an[7];
  {
    LAS unsigned char* sl = slot_ptr(0);
#pragma unroll
    for (int t = 0; t < 7; ++t) an[t] = tr_read2(sl + t * 32, sl + t * 32 + 8 * 224);
  }
  SCHED();
  unsigned csflag = 0;
  v4u csl[4];
  v4u fn[12];
  LAS float* cspart = (LAS float*)(L + P3_CSP);
  int cspo = P3_CSP + 896 * 4 + 16 * hh; PIN_V(cspo);
  LAS float* csp = cspart + 896;
  LAS float* csq_ = (LAS float*)(L + cspo);
  float mean = 0.f, rstd = 0.f;
  static_for<P3_KM>([&](auto KS) {
    constexpr int ks = KS;
    v4u ac[7];
#pragma unroll
    for (int t = 0; t < 7; ++t) ac[t] = an[t];
    if constexpr (ks == 19) { WAIT_VM_BAR(0); }
    else wait_vm_a<p3_cum(ks) - 1 - p3_pos(ks)>(hb[ks]);
    if constexpr (ks == 19) {
      PIN_A(hb[19]);
#pragma unroll
      for (int k = P3_KM; k < 32; ++k) PIN_A(hb[k]);
    }
    constexpr int ds = (ks + 1) >> 2, ksl = (ks + 1) & 3;
    LAS unsigned char* sl = slot_ptr(ds) + ksl * 16 * 224;
    auto vm = [&](auto SL) {
      constexpr int slot = SL, nl = p3_nl(ks), nd = p3_nd(ks), nx = p3_nx(ks);
      if constexpr (slot < nl) {
        constexpr int i = 8 + 2 * ks + slot;
        bload_a<(i & 3) * 1024>(hb[i], rsv, lane * 16, hbase + (i >> 2) * 4096);
      } else if constexpr (slot < nl + nd) {
        constexpr int i = 2 * (ks & 3) + (slot - nl);
        if constexpr (i == 0) p3_w_setup(5 + ((ks - 4) >> 2), w);
        p3_issue_w1<i>(wgu, c, 5 + ((ks - 4) >> 2), w, vo);
      } else if constexpr (slot < nl + nd + nx) {
        if constexpr (ks == 7) bload1_v(csflag, rsv, (unsigned)(FL_F + ((48 + (lane & 15)) * 16 + c) * 32) * 4u, 0u);
        else {
          const unsigned vof = (unsigned)(lane < 56 ? lane : 55) * 16u;
          bload_v<(ks - 12) * 896>(csl[ks - 12], rsv, vof, WS_CS + (unsigned)(c * 16 + 4 * w) * 896u);
        }
      }
    };
    if constexpr (ks == 11) {
      wait_vm_v1<p3_cum(11) - 1 - (p3_cum(7) + p3_nl(7) + p3_nd(7))>(csflag);
      if (!__all(csflag == tag)) wait_flags(flag_ptr(ctl, 3, lane & 15, c), tag);
    }
    if constexpr (ks == 13) {
      float s1 = 0.f, s2 = 0.f;
#pragma unroll
      for (int i = 0; i < 4; ++i) {
        PIN_V(hs[i]);
        s1 += __uint_as_float(hs[i].x) + __uint_as_float(hs[i].z);
        s2 += __uint_as_float(hs[i].y) + __uint_as_float(hs[i].w);
      }
      s1 = xsum32(s1); s2 = xsum32(s2);
      mean = s1 * (1.f / 512.f);
      rstd = __builtin_amdgcn_rsqf(fmaxf(s2 * (1.f / 512.f) - mean * mean, 0.f) + 1e-5f);
    }
    if constexpr (ks == 18) {
      asm volatile("s_waitcnt vmcnt(0)" : "+v"(csl[0]), "+v"(csl[1]), "+v"(csl[2]), "+v"(csl[3]) :: "memory");
      f32x4 a;
#pragma unroll
      for (int e = 0; e < 4; ++e)
        a[e] = (__uint_as_float(csl[0][e]) + __uint_as_float(csl[1][e])) + (__uint_as_float(csl[2][e]) + __uint_as_float(csl[3][e]));
      if (lane < 56) *(LAS f32x4*)(cspart + w * 224 + lane * 4) = a;
    }
    static_for<7>([&](auto T) {
      constexpr int t = T;
      if constexpr (t == 0 || t == 2) asm volatile("" : "+v"(ac[t]), "+v"(ac[t + 1]));
      if constexpr (t == 4) asm volatile("" : "+v"(ac[4]), "+v"(ac[5]), "+v"(ac[6]));
      acc[t] = mfma32(ac[t], hb[ks], acc[t]);
      SCHED();
      if constexpr (ks + 1 < P3_KM) {
        if constexpr (t == 1 || t == 3 || t == 5) {
          an[t - 1] = tr_read2(sl + (t - 1) * 32, sl + (t - 1) * 32 + 8 * 224);
          an[t] = tr_read2(sl + t * 32, sl + t * 32 + 8 * 224);
        }
        if constexpr (t == 6) an[6] = tr_read2(sl + 6 * 32, sl + 6 * 32 + 8 * 224);
      } else {
        static_for<2>([&](auto Q) {
          constexpr int k = 2 * t + Q;
          if constexpr (k < 12) {
            constexpr int k2 = P3_KM + k;
            LAS unsigned char* s2p = slot_ptr(k2 >> 2) + (k2 & 3) * 16 * 224;
            fn[k] = tr_read2(s2p, s2p + 8 * 224);
          }
        });
      }
      if constexpr (t == 0 || t == 2 || t == 4 || t == 6) vm(std::integral_constant<int, (t >> 1)>{});
      SCHED();
    });
    if constexpr ((ks & 3) == 3 && (ks >> 2) < 3) BAR_LDS();
  });
  STAMP(15);
  const f32x2 nm2 = {-mean, -mean};
  const float r2s = rstd * rstd;
  const f32x2 k1 = {-2.302208198f * rstd, -2.302208198f * rstd}, k2 = {-0.1029432397f * rstd * r2s, -0.1029432397f * rstd * r2s};
  const f32x2 rr2 = {r2s, r2s}, one2 = {1.f, 1.f};
  f32x2 yg[4], yu[4], zz[4], rc[4];
  f32x4 cgu[4];
  unsigned gw[4];
  auto epi_piece = [&](auto T, auto K) {
    constexpr int t = T, k = K;
    auto csg = [&](int i) { return pk2(cgu[i >> 1][2 * (i & 1)], cgu[i >> 1][2 * (i & 1) + 1]); };
    auto csu = [&](int i) { return pk2(cgu[2 + (i >> 1)][2 * (i & 1)], cgu[2 + (i >> 1)][2 * (i & 1) + 1]); };
    auto ex2 = [&](int i) { zz[i].x = __builtin_amdgcn_exp2f(zz[i].x); zz[i].y = __builtin_amdgcn_exp2f(zz[i].y); };
    auto rc2 = [&](int i) { rc[i].x = __builtin_amdgcn_rcpf(zz[i].x); rc[i].y = __builtin_amdgcn_rcpf(zz[i].y); };
    if constexpr (k == 0) {
      cgu[0] = *(LAS f32x4*)(csq_ + t * 16);
      cgu[1] = *(LAS f32x4*)(csq_ + t * 16 + 8);
      cgu[2] = *(LAS f32x4*)(csq_ + 112 + t * 16);
      cgu[3] = *(LAS f32x4*)(csq_ + 112 + t * 16 + 8);
    } else if constexpr (k == 1) {
      PIN_A(acc[t]);
#pragma unroll
      for (int i = 0; i < 4; ++i) { yg[i] = pk2(acc[t][2 * i], acc[t][2 * i + 1]); PIN_V(yg[i]); }
    } else if constexpr (k == 2) {
#pragma unroll
      for (int i = 0; i < 4; ++i) { yu[i] = pk2(acc[t][8 + 2 * i], acc[t][8 + 2 * i + 1]); PIN_V(yu[i]); }
    } else if constexpr (k == 3) {
#pragma unroll
      for (int i = 0; i < 4; ++i) yg[i] = nm2 * csg(i) + yg[i];
      yu[0] = nm2 * csu(0) + yu[0]; yu[1] = nm2 * csu(1) + yu[1];
    } else if constexpr (k == 4) {
#pragma unroll
      for (int i = 0; i < 4; ++i) zz[i] = yg[i] * yg[i];
      yu[2] = nm2 * csu(2) + yu[2]; yu[3] = nm2 * csu(3) + yu[3];
    } else if constexpr (k == 5) {
#pragma unroll
      for (int i = 0; i < 4; ++i) zz[i] = zz[i] * k2 + k1;
      yu[0] = yu[0] * yg[0]; yu[1] = yu[1] * yg[1];
    } else if constexpr (k == 6) {
#pragma unroll
      for (int i = 0; i < 4; ++i) zz[i] = zz[i] * yg[i];
      yu[2] = yu[2] * yg[2]; yu[3] = yu[3] * yg[3];
    } else if constexpr (k == 7) {
      ex2(0); ex2(1);
      yu[0] = yu[0] * rr2; yu[1] = yu[1] * rr2;
    } else if constexpr (k == 8) {
      ex2(2); ex2(3);
      zz[0] += one2; zz[1] += one2;
    } else if constexpr (k == 9) {
      rc2(0); rc2(1);
      zz[2] += one2; zz[3] += one2;
    } else if constexpr (k == 10) {
      rc2(2); rc2(3);
      yu[2] = yu[2] * rr2; yu[3] = yu[3] * rr2;
      yu[0] = yu[0] * rc[0]; yu[1] = yu[1] * rc[1];
    } else {
      yu[2] = yu[2] * rc[2]; yu[3] = yu[3] * rc[3];
#pragma unroll
      for (int i = 0; i < 4; ++i) gw[i] = cvt2(yu[i]);
      const v4u f = {gw[0], gw[1], gw[2], gw[3]};
      __builtin_amdgcn_raw_buffer_store_b128(f, rs, lane * 16, (int)WS_G + (tile32 * 112 + 7 * c + t) * 1024, AUX_SC1);
    }
  };
  unsigned wv4[4];
  wrow_voffs(lane, wv4);
  const u16* wdb = p.wd + (448 * w) * 512 + nt4 * 64;
  const int sw4 = ((q4 >> 1) & 1) << 2;
  LAS unsigned char* wb0 = L + w * P4_EARLY + (4 * hh + q4) * 128 + (((2 * blk + (pp >> 1)) ^ sw4) * 16) + 8 * (pp & 1);
  LAS unsigned char* wb1 = L + w * P4_EARLY + (4 * hh + q4) * 128 + (((4 + 2 * blk + (pp >> 1)) ^ sw4) * 16) + 8 * (pp & 1);
  SCHED();
  STAMP(18);
  static_for<7>([&](auto T) {
    constexpr int t = T;
    v4u fc[12];
#pragma unroll
    for (int k = 0; k < 12; ++k) fc[k] = fn[k];
    if constexpr (t == 4) WAIT_VM(3);
    static_for<12>([&](auto K) {
      constexpr int k = K;
      acc[t] = mfma32(fc[k], hb[P3_KM + k], acc[t]);
      SCHED();
      if constexpr (t < 6) {
        constexpr int ks = P3_KM + k, ds = ks >> 2, ksl = ks & 3;
        LAS unsigned char* sl = slot_ptr(ds) + ksl * 16 * 224 + (t + 1) * 32;
        fn[k] = tr_read2(sl, sl + 8 * 224);
      }
      if constexpr (t > 0) epi_piece(std::integral_constant<int, t - 1>{}, K);
      if constexpr (t == 0 && k == 0 ) {
        if (lane < 56) {
          const f32x4 a = (*(LAS f32x4*)(cspart + lane * 4) + *(LAS f32x4*)(cspart + 224 + lane * 4)) +
                          (*(LAS f32x4*)(cspart + 448 + lane * 4) + *(LAS f32x4*)(cspart + 672 + lane * 4));
          *(LAS f32x4*)(csp + lane * 4) = a;
        }
      }
      if constexpr (t < 2) {
        if constexpr (k >= 2 && k < 10) {
          constexpr int n = 8 * t + (k - 2), gq = n >> 2;
          if constexpr ((n & 3) == 0) set_m0((unsigned)(w * P4_EARLY + gq * 4096));
          wrow_issue<(n & 3)>(wdb + gq * 32 * 512, wv4);
        }
      }
      if constexpr (t == 4 && k < 8) {
        wr[k][0] = tr_read2(wb0 + k * 2048, wb0 + k * 2048 + 8 * 128);
        wr[k][1] = tr_read2(wb1 + k * 2048, wb1 + k * 2048 + 8 * 128);
      }
      if constexpr (t == 4 && k == 11) WAIT_LGKM();
      if constexpr (t >= 5) {
        if constexpr (k >= 2 && k < 10) {
          constexpr int n = 8 * (t - 5) + (k - 2), gq = n >> 2;
          if constexpr ((n & 3) == 0) set_m0((unsigned)(w * P4_EARLY + gq * 4096));
          wrow_issue<(n & 3)>(wdb + (4 + gq) * 32 * 512, wv4);
        }
      }
      SCHED();
    });
  });
  STAMP(19);
  asm volatile("s_nop 15\n\ts_nop 7" ::: "memory");
  SCHED();
  static_for<12>([&](auto K) { epi_piece(std::integral_constant<int, 6>{}, K); });
}

constexpr int P4_UP = 10;
DEV void phase4(const Params& p, LAS unsigned char* L, v4u rsv, int p64, int nt, int w, int lane,
                const v4u (&hkeep)[2], const v4u (&wr)[8][2] TSARG) {
  const int hh = lane >> 5, blk = (lane >> 4) & 1, q4 = (lane & 15) >> 2, pp = lane & 3;
  const int sw = ((q4 >> 1) & 1) << 2;
  const int wo0 = (4 * hh + q4) * 128 + (((2 * blk + (pp >> 1)) ^ sw) * 16) + 8 * (pp & 1);
  const int wo1 = (4 * hh + q4) * 128 + (((4 + 2 * blk + (pp >> 1)) ^ sw) * 16) + 8 * (pp & 1);
  int e0 = w * P4_EARLY + wo0, e1 = w * P4_EARLY + wo1, l0 = P4_LATE + w * P4_WAVE + wo0, l1 = P4_LATE + w * P4_WAVE + wo1;
  PIN_V(e0); PIN_V(e1); PIN_V(l0); PIN_V(l1);
  f32x16 acc[2][2];
#pragma unroll
  for (int a = 0; a < 2; ++a)
#pragma unroll
    for (int b = 0; b < 2; ++b)
#pragma unroll
      for (int e = 0; e < 16; ++e) acc[a][b][e] = 0.f;
  v4u gf[2][28];
  const unsigned gb0 = WS_G + ((2 * p64) * 112 + 28 * w) * 1024, gb1 = gb0 + 112 * 1024;
  auto ldg = [&](auto I) {
    constexpr int i = I;
    if constexpr (i < 24) {
      bload_a<(i & 3) * 1024>(gf[0][i], rsv, lane * 16, gb0 + (i >> 2) * 4096);
      bload_a<(i & 3) * 1024>(gf[1][i], rsv, lane * 16, gb1 + (i >> 2) * 4096);
    } else {
      bload_v<(i & 3) * 1024>(gf[0][i], rsv, lane * 16, gb0 + (i >> 2) * 4096);
      bload_v<(i & 3) * 1024>(gf[1][i], rsv, lane * 16, gb1 + (i >> 2) * 4096);
    }
  };
  static_for<P4_UP>(ldg);
  SCHED();
  v4u an0 = wr[0][0], an1 = wr[0][1];
  static_for<28>([&](auto I) {
    constexpr int i = I;
    constexpr int cum = (2 * (P4_UP + i) < 56) ? 2 * (P4_UP + i) : 56;
    constexpr int N = cum - 2 * i - 2;
    if constexpr (i < 24) wait_vm_a2<N>(gf[0][i], gf[1][i]); else wait_vm_v2<N>(gf[0][i], gf[1][i]);
    const v4u a0 = an0, a1 = an1;
    if constexpr (i + 1 < 8) { an0 = wr[i + 1][0]; an1 = wr[i + 1][1]; }
    else if constexpr (i + 1 < 28) {
      constexpr bool early = (i + 1) < 16;
      constexpr int sl = early ? (i + 1 - 8) : (i + 1 - 16);
      LAS unsigned char* q0 = L + (early ? e0 : l0) + sl * 2048;
      LAS unsigned char* q1 = L + (early ? e1 : l1) + sl * 2048;
      an0 = tr_read2(q0, q0 + 8 * 128);
      an1 = tr_read2(q1, q1 + 8 * 128);
    }
    acc[0][0] = mfma32(a0, gf[0][i], acc[0][0]);
    acc[1][0] = mfma32(a1, gf[0][i], acc[1][0]);
    SCHED();
    if constexpr (i + P4_UP < 28) ldg(std::integral_constant<int, i + P4_UP>{});
    SCHED();
    acc[0][1] = mfma32(a0, gf[1][i], acc[0][1]);
    acc[1][1] = mfma32(a1, gf[1][i], acc[1][1]);
    SCHED();
  });
  STAMP(16);
  f32x16 r = reduce4<P4_WAVE>(acc, L + P4_LATE, w, lane);
  const int tok = 64 * p64 + 32 * (w >> 1) + (lane & 31);
  v2u o[4];
#pragma unroll
  for (int qd = 0; qd < 4; ++qd) {
    const unsigned h01 = hkeep[qd >> 1][2 * (qd & 1)], h23 = hkeep[qd >> 1][2 * (qd & 1) + 1];
    const unsigned y01 = pk_bf16(r[4 * qd + 0], r[4 * qd + 1]), y23 = pk_bf16(r[4 * qd + 2], r[4 * qd + 3]);
    o[qd] = v2u{pk_bf16(bf_lo(h01) + bf_lo(y01), bf_hi(h01) + bf_hi(y01)), pk_bf16(bf_lo(h23) + bf_lo(y23), bf_hi(h23) + bf_hi(y23))};
  }
  const __amdgpu_buffer_rsrc_t ro = __builtin_amdgcn_make_buffer_rsrc((void*)p.out, 0, 2048 * 512 * 2, 0x00020000);
#pragma unroll
  for (int u = 0; u < 2; ++u) {
    auto sx = __builtin_amdgcn_permlane32_swap(o[2 * u].x, o[2 * u + 1].x, false, false);
    auto sy = __builtin_amdgcn_permlane32_swap(o[2 * u].y, o[2 * u + 1].y, false, false);
    const v4u o4 = {sx[0], sy[0], sx[1], sy[1]};
    __builtin_amdgcn_raw_buffer_store_b128(o4, ro, tok * 1024 + (64 * nt + 32 * (w & 1) + 16 * u + 8 * hh) * 2, 0, OUT_AUX);
  }
}

__global__ void __launch_bounds__(256, 1) mk_kernel(Params p) {
  extern __shared__ __attribute__((aligned(16))) unsigned char smem[];
  LAS unsigned char* L = (LAS unsigned char*)smem;
  const int tid = threadIdx.x, lane = tid & 63, w = __builtin_amdgcn_readfirstlane(tid >> 6);
  const int b = blockIdx.x;
  const int dom = (b & 7) * 2 + (b >> 7), j = (b >> 3) & 15;
  const int p64 = 2 * dom + (j >> 3), j8 = j & 7;
  const __amdgpu_buffer_rsrc_t rs = __builtin_amdgcn_make_buffer_rsrc((void*)p.ws, 0, (int)WS_BYTES, 0x00020000);
  const v4u rsv = {(unsigned)(size_t)p.ws, (unsigned)((size_t)p.ws >> 32), WS_BYTES, 0x00020000u};
  GAS unsigned* ctl = (GAS unsigned*)(p.ws + WS_FLAGS);
  const void* wgu = uni64((w & 1) ? p.wu : p.wg);
  const void* pfb = uni64(w == 0 ? p.wo : w == 1 ? p.wg : w == 2 ? p.wu : p.wd);
  STAMP(0);
  unsigned wvo[6], vx[5];
  {
    const int tok0 = 64 * p64 - 16, s0 = (p64 & 3) * 2;
    p1_wvoff(w, lane, wvo);
    p1_w_setup(0, w);
    static_for<6>([&](auto I) { p1_issue_w1<I>(p, j8, s0, wvo); });
    SCHED();
    p1_xvoff(tok0, w, lane, vx);
    p1_x_setup(s0, w);
    static_for<5>([&](auto I) { p1_issue_x1<I>(p, s0, w, lane, vx); });
    p1_w_setup(1, w);
    static_for<6>([&](auto I) { p1_issue_w1<I>(p, j8, s0 + 1, wvo); });
    SCHED();
    if (w == 0) *(LAS v4u*)(L + P1_X + (lane >> 3) * 10240 + (lane & 7) * 16) = v4u{0x3f803f80u, 0x3f803f80u, 0x3f803f80u, 0x3f803f80u};
  }
  {
    unsigned long long pc; unsigned csz;
    asm volatile("s_getpc_b64 %0\n.Lmk_pc_here:\n\ts_mov_b32 %1, .Lmk_code_end-.Lmk_pc_here" : "=s"(pc), "=s"(csz));
    const unsigned off = (unsigned)((b >> 3) * 16 + (lane & 15)) * 128u + (unsigned)w * 65536u;
    if (lane < 16 && off < csz) {
      asm volatile("s_mov_b32 m0, %2\n\ts_nop 0\n\tglobal_load_lds_dword %0, %1" :: "v"(off), "s"(pc), "s"(160768u) : "memory");
    }
  }
  unsigned wv4[4], vo[7];
  wrow_voffs(lane, wv4);
  p3_voffs(lane, vo);
#pragma unroll
  for (int i = 0; i < 7; ++i) PIN_V(vo[i]);
  const unsigned tag = __hip_atomic_load(ctl + FL_EPOCH, __ATOMIC_RELAXED, __HIP_MEMORY_SCOPE_AGENT) + 1u;
  STAMP(1);
  phase1(p, L, rs, p64, j8, w, lane, wvo, vx, wv4, pfb TSPASS);
  asm volatile("" ::: "memory"); SCHED();
  STAMP(2);
  WAIT_VM_BAR(0);
  if (tid == 0) __hip_atomic_store(flag_ptr(ctl, 0, dom, j), tag, __ATOMIC_RELAXED, __HIP_MEMORY_SCOPE_AGENT);
  STAMP(3);
  p2_issue_x(p, p64, j8, w, lane);
#pragma unroll
  for (int ds = 0; ds < 2; ++ds) p3_issue_w(wgu, j, ds, w, vo);
  wait_flags(flag_ptr(ctl, 0, dom, (j & 8) + (lane & 7)), tag);
  STAMP(4);
  v4u hkeep[2];
  v4u csv4[4];
  phase2(p, L, rs, rsv, p64, j8, w, lane, hkeep, j, vo, dom, csv4, wgu TSPASS);
  STAMP(5);
  WAIT_VM_BAR(0);
  if (tid == 0) __hip_atomic_store(flag_ptr(ctl, 1, dom, j), tag, __ATOMIC_RELAXED, __HIP_MEMORY_SCOPE_AGENT);
  STAMP(6);
#pragma unroll
  for (int ds = 3; ds < 5; ++ds) p3_issue_w(wgu, j, ds, w, vo);
  cs_finish(L, rs, j, dom, w, lane, csv4);
  wait_flags(flag_ptr(ctl, 1, dom, lane & 15), tag);
  STAMP(7);
  v4u wr[8][2];
  phase3(p, L, rs, rsv, ctl, tag, dom, j, w, lane, j8, vo, wr, wgu TSPASS);
  STAMP(8);
  WAIT_VM_BAR(0);
  if (tid == 0) __hip_atomic_store(flag_ptr(ctl, 2, dom, j), tag, __ATOMIC_RELAXED, __HIP_MEMORY_SCOPE_AGENT);
  STAMP(9);
  {
    unsigned wv4[4];
    wrow_voffs(lane, wv4);
    const u16* wdb = p.wd + (448 * w + 256) * 512 + j8 * 64;
    static_for<6>([&](auto G) {
      constexpr int gq = G;
      set_m0((unsigned)(P4_LATE + w * P4_WAVE + gq * 4096));
      static_for<4>([&](auto I) { wrow_issue<I>(wdb + gq * 32 * 512, wv4); });
    });
  }
  wait_flags(flag_ptr(ctl, 2, dom, lane & 15), tag);
  STAMP(10);
  if (b == 0 && tid == 0) __hip_atomic_store(ctl + FL_EPOCH, tag, __ATOMIC_RELAXED, __HIP_MEMORY_SCOPE_AGENT);
  phase4(p, L, rsv, p64, j8, w, lane, hkeep, wr TSPASS);
  STAMP(17);
  asm volatile(".Lmk_code_end:" ::: "memory");
}

extern "C" void kernel_launch(void* const* d_in, const int* in_sizes, int n_in, void* d_out, int out_size,
                              void* d_ws, size_t ws_size, hipStream_t stream) {
  Params p;
  p.x = (const u16*)d_in[0];
  p.wqkv = (const u16*)d_in[1];
  p.wo = (const u16*)d_in[2];
  p.wg = (const u16*)d_in[4];
  p.wu = (const u16*)d_in[5];
  p.wd = (const u16*)d_in[6];
  p.out = (u16*)d_out;
  p.ws = (unsigned char*)d_ws;
  (void)hipFuncSetAttribute((const void*)mk_kernel, hipFuncAttributeMaxDynamicSharedMemorySize, LDS_BYTES);
  mk_kernel<<<256, 256, LDS_BYTES, stream>>>(p);
}
```

```cpp
#include <hip/hip_runtime.h>
#include <stdint.h>
#include <utility>

#define OUT_AUX 0
#define MK_STAMP 0

typedef unsigned short u16;
typedef unsigned v4u __attribute__((ext_vector_type(4)));
typedef unsigned v2u __attribute__((ext_vector_type(2)));
typedef short v8s __attribute__((ext_vector_type(8)));
typedef short v4s __attribute__((ext_vector_type(4)));
typedef float f32x2 __attribute__((ext_vector_type(2)));
typedef float f32x4 __attribute__((ext_vector_type(4)));
typedef float f32x16 __attribute__((ext_vector_type(16)));
typedef __bf16 bf16x2 __attribute__((ext_vector_type(2)));
#define GAS __attribute__((address_space(1)))
#define LAS __attribute__((address_space(3)))
#define DEV __device__ __forceinline__

constexpr unsigned WS_FLAGS = 0;
constexpr unsigned WS_O = 262144;
constexpr unsigned WS_H = WS_O + 2097152;
constexpr unsigned WS_HS = WS_H + 2097152;
constexpr unsigned WS_G = WS_HS + 262144;
constexpr unsigned WS_CS = WS_G + 64 * 112 * 1024;
constexpr unsigned WS_STAMP = WS_CS + 16 * 16 * 224 * 4;
constexpr unsigned WS_BYTES = WS_STAMP + 256 * 4 * 48 * 8;
constexpr int FL_EPOCH = 0, FL_F = 1024;

constexpr int LDS_BYTES = 163840;
constexpr int AUX_SC1 = 16;

struct Params {
  const u16* x; const u16* wqkv; const u16* wo; const u16* wg; const u16* wu; const u16* wd;
  u16* out; unsigned char* ws;
};

template <class F, int... I> DEV void static_for_impl(F&& f, std::integer_sequence<int, I...>) { (f(std::integral_constant<int, I>{}), ...); }
template <int N, class F> DEV void static_for(F&& f) { static_for_impl(f, std::make_integer_sequence<int, N>{}); }

DEV float bf_lo(unsigned u) { return __uint_as_float(u << 16); }
DEV float bf_hi(unsigned u) { return __uint_as_float(u & 0xffff0000u); }
DEV unsigned pk_bf16(float a, float b) {
  bf16x2 p; p[0] = (__bf16)a; p[1] = (__bf16)b;
  return __builtin_bit_cast(unsigned, p);
}
DEV f32x2 pk2(float a, float b) { return f32x2{a, b}; }
DEV unsigned cvt2(f32x2 v) { return pk_bf16(v.x, v.y); }
DEV float dot2(unsigned a, unsigned b, float c) {
  return __builtin_amdgcn_fdot2_f32_bf16(__builtin_bit_cast(bf16x2, a), __builtin_bit_cast(bf16x2, b), c, false);
}
DEV f32x4 mfma16(v4u a, v4u b, f32x4 c) {
  return __builtin_amdgcn_mfma_f32_16x16x32_bf16(__builtin_bit_cast(v8s, a), __builtin_bit_cast(v8s, b), c, 0, 0, 0);
}
DEV f32x4 mfma16k16(v2u a, v2u b, f32x4 c) {
  return __builtin_amdgcn_mfma_f32_16x16x16bf16_1k(__builtin_bit_cast(v4s, a), __builtin_bit_cast(v4s, b), c, 0, 0, 0);
}
DEV f32x16 mfma32(v4u a, v4u b, f32x16 c) {
  return __builtin_amdgcn_mfma_f32_32x32x16_bf16(__builtin_bit_cast(v8s, a), __builtin_bit_cast(v8s, b), c, 0, 0, 0);
}
DEV v2u tr_read(LAS unsigned char* p) {
  return __builtin_bit_cast(v2u, __builtin_amdgcn_ds_read_tr16_b64_v4i16((LAS v4s*)p));
}
DEV v4u tr_read2(LAS unsigned char* p0, LAS unsigned char* p1) {
  v2u a = tr_read(p0), b = tr_read(p1);
  return v4u{a.x, a.y, b.x, b.y};
}
DEV void dma16(const void* sbase, unsigned voff, unsigned lds_dst) {
  lds_dst = __builtin_amdgcn_readfirstlane(lds_dst);
  asm volatile("s_mov_b32 m0, %2\n\ts_nop 0\n\tglobal_load_lds_dwordx4 %0, %1"
               :: "v"(voff), "s"(sbase), "s"(lds_dst) : "memory");
}
DEV void set_m0(unsigned v) {
  v = __builtin_amdgcn_readfirstlane(v);
  asm volatile("s_mov_b32 m0, %0\n\ts_nop 0" :: "s"(v) : "memory");
}
DEV const void* uni64(const void* p) {
  const unsigned long long a = (unsigned long long)p;
  const unsigned lo = __builtin_amdgcn_readfirstlane((unsigned)a), hi = __builtin_amdgcn_readfirstlane((unsigned)(a >> 32));
  unsigned long long u = ((unsigned long long)hi << 32) | lo;
  asm volatile("s_nop 4" : "+s"(u));
  return (const void*)u;
}
template <int IMM> DEV void dma16i(const void* sbase, unsigned voff) {
  asm volatile("global_load_lds_dwordx4 %0, %1 offset:%c2" :: "v"(voff), "s"(sbase), "i"(IMM) : "memory");
}
template <int IMM> DEV void bload_a(v4u& dst, v4u rsrc, unsigned voff, unsigned soff) {
  asm volatile("buffer_load_dwordx4 %0, %1, %2, %3 offen offset:%c4 sc1" : "=a"(dst) : "v"(voff), "s"(rsrc), "s"(soff), "i"(IMM) : "memory");
}
template <int IMM> DEV void bload_v(v4u& dst, v4u rsrc, unsigned voff, unsigned soff) {
  asm volatile("buffer_load_dwordx4 %0, %1, %2, %3 offen offset:%c4 sc1" : "=v"(dst) : "v"(voff), "s"(rsrc), "s"(soff), "i"(IMM) : "memory");
}
DEV void bload1_v(unsigned& dst, v4u rsrc, unsigned voff, unsigned soff) {
  asm volatile("buffer_load_dword %0, %1, %2, %3 offen sc1" : "=v"(dst) : "v"(voff), "s"(rsrc), "s"(soff) : "memory");
}
template <int N> DEV void wait_vm_a(v4u& x) { asm volatile("s_waitcnt vmcnt(%c1)" : "+a"(x) : "i"(N) : "memory"); }
template <int N> DEV void wait_vm_a2(v4u& x, v4u& y) { asm volatile("s_waitcnt vmcnt(%c2)" : "+a"(x), "+a"(y) : "i"(N) : "memory"); }
template <int N> DEV void wait_vm_v2(v4u& x, v4u& y) { asm volatile("s_waitcnt vmcnt(%c2)" : "+v"(x), "+v"(y) : "i"(N) : "memory"); }
template <int N> DEV void wait_vm_v1(unsigned& x) { asm volatile("s_waitcnt vmcnt(%c1)" : "+v"(x) : "i"(N) : "memory"); }
#define PIN_V(x) asm volatile("" : "+v"(x))
#define PIN_A(x) asm volatile("" : "+a"(x))
#define WAIT_VM(n) asm volatile("s_waitcnt vmcnt(" #n ")" ::: "memory")
#define WAIT_VM_BAR(n) asm volatile("s_waitcnt vmcnt(" #n ") lgkmcnt(0)\n\ts_barrier" ::: "memory")
#define BAR_LDS() asm volatile("s_waitcnt lgkmcnt(0)\n\ts_barrier" ::: "memory")
#define WAIT_LGKM() asm volatile("s_waitcnt lgkmcnt(0)" ::: "memory")
#define SCHED() __builtin_amdgcn_sched_barrier(0)

DEV float xsum16_32(float v) {
  auto r = __builtin_amdgcn_permlane16_swap(__float_as_uint(v), __float_as_uint(v), false, false);
  v = __uint_as_float(r[0]) + __uint_as_float(r[1]);
  auto s = __builtin_amdgcn_permlane32_swap(__float_as_uint(v), __float_as_uint(v), false, false);
  return __uint_as_float(s[0]) + __uint_as_float(s[1]);
}
DEV float xmax16_32(float v) {
  auto r = __builtin_amdgcn_permlane16_swap(__float_as_uint(v), __float_as_uint(v), false, false);
  v = fmaxf(__uint_as_float(r[0]), __uint_as_float(r[1]));
  auto s = __builtin_amdgcn_permlane32_swap(__float_as_uint(v), __float_as_uint(v), false, false);
  return fmaxf(__uint_as_float(s[0]), __uint_as_float(s[1]));
}
DEV float xsum32(float v) {
  auto s = __builtin_amdgcn_permlane32_swap(__float_as_uint(v), __float_as_uint(v), false, false);
  return __uint_as_float(s[0]) + __uint_as_float(s[1]);
}

#define STAMP(i) do {} while (0)
#define TSARG
#define TSPASS

DEV GAS unsigned* flag_ptr(GAS unsigned* ctl, int ph, int dom, int j) { return ctl + FL_F + ((ph * 16 + dom) * 16 + j) * 32; }
DEV void wait_flags(GAS unsigned* word, unsigned tag) {
  for (unsigned spins = 0; spins < (1u << 20); ++spins) {
    const unsigned v = __hip_atomic_load(word, __ATOMIC_RELAXED, __HIP_MEMORY_SCOPE_AGENT);
    if (__all(v == tag)) break;
    __builtin_amdgcn_s_sleep(1);
  }
  __builtin_amdgcn_fence(__ATOMIC_ACQUIRE, "wavefront");
  asm volatile("" ::: "memory");
}

constexpr int P1_X = 0;
constexpr int P1_W = 81920;
constexpr int P1_WSLOT = 24576;
constexpr int P1_MISC = 155648;
constexpr int P2_W = 0;
constexpr int P2_X = 151936;
constexpr int CS_BUF = 160128;
constexpr int P3_UP = 14464;
constexpr int P3_CSP = 151936;
DEV constexpr int p3_slot_addr(int ds) { return (ds % 5) == 0 ? 65536 : (ds % 5) == 1 ? 94336 : (ds % 5) == 2 ? 123136 : (ds % 5) == 3 ? 0 : 28800; }
constexpr int P4_EARLY = 16384;
constexpr int P4_LATE = 65536, P4_WAVE = 24576;

DEV unsigned wrow_voff(int lane) { return (unsigned)(lane >> 3) * 1024u + (unsigned)((lane & 7) ^ (((lane >> 4) & 1) << 2)) * 16u; }

DEV void p1_xvoff(int tok0, int w, int lane, unsigned (&vx)[5]) {
#pragma unroll
  for (int i = 0; i < 5; ++i) {
    const int rg = 5 * (w & 1) + i;
    const int row = 8 * rg + (lane >> 3);
    int tok = tok0 + row; tok = tok < 0 ? 0 : tok;
    const int ch = (lane & 7) ^ ((row >> 1) & 7);
    vx[i] = (unsigned)tok * 1024u + (unsigned)ch * 16u + 4096u - (unsigned)((i - 2) * 1024);
  }
}
DEV void p1_x_setup(int sp0, int w) { set_m0((unsigned)(P1_X + (sp0 + (w >> 1)) * 10240 + (5 * (w & 1) + 2) * 1024)); }
template <int I> DEV void p1_issue_x1(const Params& p, int sp0, int w, int lane, const unsigned (&vx)[5]) {
  const unsigned char* base = (const unsigned char*)p.x + (sp0 + (w >> 1)) * 128 - 4096;
  if constexpr (I == 0) { if ((w & 1) || lane >= 8) dma16i<(I - 2) * 1024>(base, vx[I]); }
  else dma16i<(I - 2) * 1024>(base, vx[I]);
}
DEV void p1_wvoff(int w, int lane, unsigned (&vo)[6]) {
#pragma unroll
  for (int i = 0; i < 6; ++i) {
    const int idx = 6 * w + i;
    const int part = idx >> 3, rg = idx & 7;
    const int row = 8 * rg + (lane >> 3);
    const int f = ((row >> 1) & 1) | (((row >> 3) & 1) << 1);
    const int ch = (lane & 7) ^ (f << 1);
    vo[i] = (unsigned)row * 3072u + (unsigned)part * 1024u + (unsigned)ch * 16u + 4096u - (unsigned)((i - 3) * 1024);
  }
}
DEV void p1_w_setup(int slot, int w) { set_m0((unsigned)(P1_W + slot * P1_WSLOT + (6 * w + 3) * 1024)); }
template <int I> DEV void p1_issue_w1(const Params& p, int hg, int st, const unsigned (&vo)[6]) {
  const unsigned char* base = (const unsigned char*)p.wqkv + st * 64 * 3072 + hg * 128 - 4096;
  dma16i<(I - 3) * 1024>(base, vo[I]);
}
DEV void wrow_voffs(int lane, unsigned (&v)[4]) {
#pragma unroll
  for (int i = 0; i < 4; ++i) v[i] = wrow_voff(lane) + 7168u * i;
}
template <int I> DEV void wrow_issue(const u16* rowbase  , const unsigned (&v)[4]) { dma16i<I * 1024>(rowbase, v[I]); }

struct P1Frag { v4u xf[5]; v4u wq, wk, wv; };

DEV void phase1(const Params& p, LAS unsigned char* L, __amdgpu_buffer_rsrc_t rs, int p64, int hg, int w, int lane,
                const unsigned (&wvo)[6], const unsigned (&vx)[5], const unsigned (&wv4)[4], const void* pfb TSARG) {
  const int s0 = (p64 & 3) * 2;
  const int g = lane >> 4, tk = lane & 15, q4 = (lane & 15) >> 2, pp = lane & 3;
  const int fsw = ((q4 >> 1) & 1) | ((g & 1) << 1);
  const int wbase = P1_W + (8 * g + q4) * 128 + (((2 * w + (pp >> 1)) ^ (fsw << 1)) * 16) + 8 * (pp & 1);
  const int xsw = (tk >> 1) & 7;
  const int xb0 = P1_X + tk * 128 + ((g ^ xsw) * 16), xb1 = P1_X + tk * 128 + (((4 + g) ^ xsw) * 16);
  const int xbw = (w & 1) ? xb1 : xb0;

  f32x4 aq[5], ak[5], av[5], ssum[5], sgr[5];
#pragma unroll
  for (int t = 0; t < 5; ++t) {
    aq[t] = f32x4{0, 0, 0, 0}; ak[t] = f32x4{0, 0, 0, 0}; av[t] = f32x4{0, 0, 0, 0}; ssum[t] = f32x4{0, 0, 0, 0}; sgr[t] = f32x4{0, 0, 0, 0};
  }
  const v4u ones = {0x3f803f80u, 0x3f803f80u, 0x3f803f80u, 0x3f803f80u};
  LAS f32x2* part = (LAS f32x2*)(L + P1_MISC);
  LAS float* fmean = (LAS float*)(L + P1_MISC + 2560 + w * 640);
  LAS float* frstd = fmean + 80;
  const u16* wob = p.wo + (128 * w) * 512 + hg * 64;
  auto dmo = [&](auto G) {
    constexpr int gq = G;
    set_m0((unsigned)(P2_W + (128 * w + 32 * gq) * 128));
    static_for<4>([&](auto I) { wrow_issue<I>(wob + gq * 32 * 512, wv4); });
  };
  float m1[5], r1[5];
  f32x4 mv[5], rv[5];

  WAIT_VM_BAR(6);
  STAMP(11);
  P1Frag cur;
  {
    LAS unsigned char* xs = L + s0 * 10240 + xb0;
    LAS unsigned char* wsl = L + wbase;
    cur.wk = tr_read2(wsl + 8192, wsl + 8192 + 512);
    cur.wv = tr_read2(wsl + 16384, wsl + 16384 + 512);
    cur.wq = tr_read2(wsl, wsl + 512);
#pragma unroll
    for (int t = 0; t < 5; ++t) cur.xf[t] = *(LAS v4u*)(xs + t * 2048);
  }
  SCHED();
  static_for<16>([&](auto KS) {
    constexpr int ks = KS;
    if constexpr (ks == 1) { WAIT_VM_BAR(5); }
    else if constexpr (ks == 3 || ks == 7) { WAIT_VM_BAR(6); }
    else if constexpr (ks == 11) { WAIT_VM_BAR(7); }
    else if constexpr (ks == 5 || ks == 9) { WAIT_VM_BAR(11); }
    else if constexpr (ks == 13) { WAIT_VM_BAR(1); }
    else if constexpr (ks == 15) { BAR_LDS(); }
    if constexpr (ks == 10) {
      const int nl = w == 0 ? 16 : 56;
      const unsigned off = (unsigned)((int)blockIdx.x * nl + (lane < nl ? lane : nl - 1)) * 128u;
      asm volatile("s_mov_b32 m0, %2\n\ts_nop 0\n\tglobal_load_lds_dword %0, %1" :: "v"(off), "s"(pfb), "s"(160768u) : "memory");
    }
    constexpr bool isx = (ks == 0 || ks == 4 || ks == 8);
    constexpr int wst = ks == 1 ? 2 : ks == 2 ? 3 : ks == 3 ? 4 : ks == 5 ? 5 : ks == 7 ? 6 : ks == 9 ? 7 : -1;
    if constexpr (isx) p1_x_setup((s0 + 2 + ks / 2) & 7, w);
    if constexpr (wst >= 0) p1_w_setup(wst % 3, w);
    auto dma = [&](auto D) {
      constexpr int d = D;
      if constexpr (isx && d < 5) p1_issue_x1<d>(p, (s0 + 2 + ks / 2) & 7, w, lane, vx);
      if constexpr (wst >= 0) p1_issue_w1<d>(p, hg, (s0 + wst) & 7, wvo);
      if constexpr (ks == 15 && d == 2) dmo(std::integral_constant<int, 0>{});
    };
    P1Frag nxt;
    constexpr int kn = ks + 1, itn = kn >> 1, k2n = kn & 1;
    LAS unsigned char* xs = L + ((s0 + itn) & 7) * 10240 + (k2n ? xb1 : xb0);
    LAS unsigned char* wsl = L + wbase + (itn % 3) * P1_WSLOT + k2n * 4096;
    constexpr int G = ks < 8 ? (ks >> 2) : ks < 11 ? 2 : 3;
    constexpr int st0 = ks < 8 ? (ks & 3) : ks == 8 ? 0 : ks == 9 ? 2 : ks == 10 ? 4 : ks == 11 ? 0 : 2;
    constexpr int nst = ks < 8 ? ((ks & 3) == 3 ? 2 : 1) : ks == 8 ? 2 : ks == 9 ? 2 : ks == 10 ? 1 : ks == 11 ? 2 : ks == 12 ? 3 : 0;
    LAS unsigned char* sx = L + ((s0 + 2 * G + (w >> 1)) & 7) * 10240 + xbw;
    v4u sv[3];
    dma(std::integral_constant<int, 0>{});
    static_for<5>([&](auto T) {
      constexpr int t = T;
      if constexpr (ks < 15) {
        if constexpr (t == 0) nxt.wk = tr_read2(wsl + 8192, wsl + 8192 + 512);
        if constexpr (t == 1) nxt.wv = tr_read2(wsl + 16384, wsl + 16384 + 512);
        if constexpr (t == 2) nxt.wq = tr_read2(wsl, wsl + 512);
        nxt.xf[t] = *(LAS v4u*)(xs + t * 2048);
      }
      if constexpr (t < nst) sv[t] = *(LAS v4u*)(sx + (st0 + t) * 2048);
      if constexpr (ks == 15) {
        m1[t] = fmean[t * 16 + tk]; r1[t] = frstd[t * 16 + tk];
        mv[t] = *(LAS f32x4*)(fmean + t * 16 + 4 * g); rv[t] = *(LAS f32x4*)(frstd + t * 16 + 4 * g);
      }
      ak[t] = mfma16(cur.wk, cur.xf[t], ak[t]);
      av[t] = mfma16(cur.xf[t], cur.wv, av[t]);
      aq[t] = mfma16(cur.wq, cur.xf[t], aq[t]);
      if constexpr (t == 4) {
        static_for<3>([&](auto I) {
          constexpr int i = I;
          if constexpr (i < nst) { ssum[st0 + i] = mfma16(ones, sv[i], ssum[st0 + i]); sgr[st0 + i] = mfma16(sv[i], sv[i], sgr[st0 + i]); }
        });
      }
      if constexpr (ks == 13 && t < 2) {
        const int tok = t * 64 + lane;
        if (tok < 80) {
          const f32x2 a = (part[tok] + part[80 + tok]) + (part[160 + tok] + part[240 + tok]);
          const float mean = a.x * (1.f / 512.f);
          const float var = fmaxf(a.y * (1.f / 512.f) - mean * mean, 0.f);
          fmean[tok] = mean; frstd[tok] = __builtin_amdgcn_rsqf(var + 1e-5f);
        }
      }
      SCHED();
      dma(std::integral_constant<int, t + 1>{});
      SCHED();
    });
    if constexpr (ks == 12) {
      const int r = tk & 3;
#pragma unroll
      for (int t = 0; t < 5; ++t) {
        const float d = r == 0 ? sgr[t][0] : r == 1 ? sgr[t][1] : r == 2 ? sgr[t][2] : sgr[t][3];
        if ((tk >> 2) == g) part[w * 80 + t * 16 + tk] = f32x2{ssum[t][0], d};
      }
      SCHED();
    }
    if constexpr (ks < 15) cur = nxt;
  });
  STAMP(12);
  f32x2 csq[2], csk[2];
#pragma unroll
  for (int r = 0; r < 4; ++r) {
    csq[r >> 1][r & 1] = __uint_as_float(__builtin_amdgcn_update_dpp(0, __float_as_uint(aq[0][r]), 0x150, 0xf, 0xf, false));
    csk[r >> 1][r & 1] = __uint_as_float(__builtin_amdgcn_update_dpp(0, __float_as_uint(ak[0][r]), 0x150, 0xf, 0xf, false));
  }
  float csv;
  {
    auto a = __builtin_amdgcn_permlane16_swap(__float_as_uint(av[0][0]), __float_as_uint(av[0][0]), false, false);
    auto c = __builtin_amdgcn_permlane32_swap(a[0], a[0], false, false);
    csv = __uint_as_float(c[0]);
  }
  dmo(std::integral_constant<int, 1>{});
  v2u qp[4], kp[5], vp[5];
  const float sc = 0.25f * 1.44269504088896f;
  const f32x2 c2 = {csv, csv};
#pragma unroll
  for (int t = 0; t < 5; ++t) {
    const f32x2 m2 = {m1[t], m1[t]}, r2 = {r1[t], r1[t]};
    kp[t].x = cvt2((pk2(ak[t][0], ak[t][1]) - m2 * csk[0]) * r2);
    kp[t].y = cvt2((pk2(ak[t][2], ak[t][3]) - m2 * csk[1]) * r2);
    if (t > 0) {
      qp[t - 1].x = cvt2(pk2(aq[t][0], aq[t][1]) - m2 * csq[0]);
      qp[t - 1].y = cvt2(pk2(aq[t][2], aq[t][3]) - m2 * csq[1]);
    }
  }
  SCHED();
  STAMP(29);
  const f32x4 z = {0, 0, 0, 0};
  f32x4 sp[4], scu[4];
#pragma unroll
  for (int t = 0; t < 4; ++t) { sp[t] = mfma16k16(kp[t], qp[t], z); scu[t] = mfma16k16(kp[t + 1], qp[t], z); }
  SCHED();
  dmo(std::integral_constant<int, 2>{});
#pragma unroll
  for (int t = 0; t < 5; ++t) {
    vp[t].x = cvt2((pk2(av[t][0], av[t][1]) - pk2(mv[t][0], mv[t][1]) * c2) * pk2(rv[t][0], rv[t][1]));
    vp[t].y = cvt2((pk2(av[t][2], av[t][3]) - pk2(mv[t][2], mv[t][3]) * c2) * pk2(rv[t][2], rv[t][3]));
  }
  SCHED();
  f32x2 biasp[2], biasc[2];
#pragma unroll
  for (int r = 0; r < 4; ++r) {
    const int dc = tk - 4 * g - r, dp = dc + 16;
    biasc[r >> 1][r & 1] = (dc >= 0 && dc <= 14 && !(dc & 1)) ? 0.f : -1e30f;
    biasp[r >> 1][r & 1] = (dp <= 14 && !(dp & 1)) ? 0.f : -1e30f;
  }
  f32x2 a[4][4];
  float mx[4], sm[4];
#pragma unroll
  for (int t = 0; t < 4; ++t) {
    const float sq = sc * r1[t + 1];
    const f32x2 s2v = {sq, sq};
    f32x2 bp0 = biasp[0], bp1 = biasp[1];
    if (t == 0 && p64 == 0) { bp0 = f32x2{-1e30f, -1e30f}; bp1 = bp0; }
    a[t][0] = pk2(sp[t][0], sp[t][1]) * s2v + bp0; a[t][1] = pk2(sp[t][2], sp[t][3]) * s2v + bp1;
    a[t][2] = pk2(scu[t][0], scu[t][1]) * s2v + biasc[0]; a[t][3] = pk2(scu[t][2], scu[t][3]) * s2v + biasc[1];
  }
#pragma unroll
  for (int t = 0; t < 4; ++t)
    mx[t] = fmaxf(fmaxf(__builtin_fmaxf(a[t][0].x, a[t][0].y), fmaxf(a[t][1].x, a[t][1].y)), fmaxf(fmaxf(a[t][2].x, a[t][2].y), fmaxf(a[t][3].x, a[t][3].y)));
  SCHED();
  auto xred2 = [&](float& A, float& B, auto op) {
    auto s1 = __builtin_amdgcn_permlane16_swap(__float_as_uint(A), __float_as_uint(B), false, false);
    const float u = op(__uint_as_float(s1[0]), __uint_as_float(s1[1]));
    auto s2_ = __builtin_amdgcn_permlane32_swap(__float_as_uint(u), __float_as_uint(u), false, false);
    const float v = op(__uint_as_float(s2_[0]), __uint_as_float(s2_[1]));
    auto s3 = __builtin_amdgcn_permlane16_swap(__float_as_uint(v), __float_as_uint(v), false, false);
    A = __uint_as_float(s3[0]); B = __uint_as_float(s3[1]);
  };
  auto fmx = [](float x, float y) { return fmaxf(x, y); };
  auto fad = [](float x, float y) { return x + y; };
  STAMP(30);
  xred2(mx[0], mx[1], fmx); xred2(mx[2], mx[3], fmx);
  SCHED();
  dmo(std::integral_constant<int, 3>{});
#pragma unroll
  for (int t = 0; t < 4; ++t) {
    const f32x2 m2 = {mx[t], mx[t]};
#pragma unroll
    for (int q = 0; q < 4; ++q) a[t][q] -= m2;
  }
#pragma unroll
  for (int t = 0; t < 4; ++t)
#pragma unroll
    for (int q = 0; q < 4; ++q) { a[t][q].x = __builtin_amdgcn_exp2f(a[t][q].x); a[t][q].y = __builtin_amdgcn_exp2f(a[t][q].y); }
  SCHED();
  v4u pf[4];
#pragma unroll
  for (int t = 0; t < 4; ++t) {
    const f32x2 sv = (a[t][0] + a[t][1]) + (a[t][2] + a[t][3]);
    sm[t] = sv.x + sv.y;
    pf[t] = v4u{cvt2(a[t][0]), cvt2(a[t][1]), cvt2(a[t][2]), cvt2(a[t][3])};
  }
  f32x4 o[4];
#pragma unroll
  for (int t = 0; t < 4; ++t) {
    const v4u vf = {vp[t].x, vp[t].y, vp[t + 1].x, vp[t + 1].y};
    o[t] = mfma16(vf, pf[t], z);
  }
  SCHED();
  STAMP(31);
  xred2(sm[0], sm[1], fad); xred2(sm[2], sm[3], fad);
  v2u ob[4];
#pragma unroll
  for (int t = 0; t < 4; ++t) {
    const float inv = __builtin_amdgcn_rcpf(sm[t]);
    const f32x2 i2 = {inv, inv};
    ob[t] = v2u{cvt2(pk2(o[t][0], o[t][1]) * i2), cvt2(pk2(o[t][2], o[t][3]) * i2)};
  }
  const int head = 4 * hg + w;
#pragma unroll
  for (int u = 0; u < 2; ++u) {
    auto sx = __builtin_amdgcn_permlane16_swap(ob[2 * u].x, ob[2 * u + 1].x, false, false);
    auto sy = __builtin_amdgcn_permlane16_swap(ob[2 * u].y, ob[2 * u + 1].y, false, false);
    const v4u o4 = {sx[0], sy[0], sx[1], sy[1]};
    const int voff = ((g >> 1) * 32 + (g & 1) * 16 + tk) * 16;
    __builtin_amdgcn_raw_buffer_store_b128(o4, rs, voff, (int)WS_O + ((2 * p64 + u) * 32 + head) * 1024, AUX_SC1);
  }
}

DEV void cs_load(const Params& p, int c, int r, int tid, v4u (&v)[4]) {
  const int rg = tid >> 5, c0 = tid & 31, cc = c0 < 28 ? c0 : 27;
  const u16* base = (cc < 14 ? p.wg : p.wu) + (size_t)(32 * r + 4 * rg) * 1792 + 112 * c + (cc < 14 ? cc : cc - 14) * 8;
#pragma unroll
  for (int i = 0; i < 4; ++i) v[i] = *(const GAS v4u*)(base + i * 1792);
}
DEV void cs_finish(LAS unsigned char* L, __amdgpu_buffer_rsrc_t rs, int c, int r, int w, int lane, const v4u (&v)[4]) {
  LAS float* buf = (LAS float*)(L + CS_BUF);
  const int c0 = lane & 31;
  f32x4 a, b;
#pragma unroll
  for (int e = 0; e < 2; ++e) {
    a[2 * e] = xsum32((bf_lo(v[0][e]) + bf_lo(v[1][e])) + (bf_lo(v[2][e]) + bf_lo(v[3][e])));
    a[2 * e + 1] = xsum32((bf_hi(v[0][e]) + bf_hi(v[1][e])) + (bf_hi(v[2][e]) + bf_hi(v[3][e])));
    b[2 * e] = xsum32((bf_lo(v[0][2 + e]) + bf_lo(v[1][2 + e])) + (bf_lo(v[2][2 + e]) + bf_lo(v[3][2 + e])));
    b[2 * e + 1] = xsum32((bf_hi(v[0][2 + e]) + bf_hi(v[1][2 + e])) + (bf_hi(v[2][2 + e]) + bf_hi(v[3][2 + e])));
  }
  if (lane < 28) {
    *(LAS f32x4*)(buf + w * 224 + c0 * 8) = a;
    *(LAS f32x4*)(buf + w * 224 + c0 * 8 + 4) = b;
  }
  BAR_LDS();
  const int tid = w * 64 + lane;
  if (tid < 224) {
    const float s = (buf[tid] + buf[224 + tid]) + (buf[448 + tid] + buf[672 + tid]);
    __builtin_amdgcn_raw_buffer_store_b32(__float_as_uint(s), rs, tid * 4, (int)WS_CS + (c * 16 + r) * 896, AUX_SC1);
  }
}

DEV void p3_voffs(int lane, unsigned (&vo)[7]) {
#pragma unroll
  for (int i = 0; i < 7; ++i) {
    const int q = i * 64 + lane, row = q / 14, cc = q - 14 * row;
    vo[i] = (unsigned)row * 3584u + (unsigned)cc * 16u + 4096u - (unsigned)((i - 3) * 1024);
  }
}
DEV void p3_w_setup(int ds, int w) { set_m0((unsigned)(p3_slot_addr(ds) + (w & 1) * P3_UP + (w >> 1) * 7168 + 3 * 1024)); }
template <int I> DEV void p3_issue_w1(const void* wgu, int c, int ds, int w, const unsigned (&vo)[7]) {
  const unsigned char* base = (const unsigned char*)wgu + (64 * ds + 32 * (w >> 1)) * 3584 + 224 * c - 4096;
  dma16i<(I - 3) * 1024>(base, vo[I]);
}
DEV void p3_issue_w(const void* wgu, int c, int ds, int w, const unsigned (&vo)[7]) {
  p3_w_setup(ds, w);
  static_for<7>([&](auto I) { p3_issue_w1<I>(wgu, c, ds, w, vo); });
}

DEV void p2_issue_x(const Params& p, int p64, int nt, int w, int lane) {
  const unsigned vx = (unsigned)(lane >> 3) * 1024u + (unsigned)(((lane & 7) ^ (lane >> 3)) * 16);
  const u16* xb = p.x + (64 * p64 + 16 * w) * 512 + nt * 64;
  set_m0((unsigned)(P2_X + 16 * w * 128));
  dma16i<0>(xb, vx);
  dma16i<1024>(xb, vx + 7168u);
}

template <int WSTRIDE>
DEV f32x16 reduce4(f32x16 (&acc)[2][2], LAS unsigned char* red, int w, int lane) {
  WAIT_LGKM();
#pragma unroll
  for (int a = 0; a < 2; ++a)
#pragma unroll
    for (int b = 0; b < 2; ++b) {
      const int dest = a + 2 * b;
      if (dest != w) {
        const int slot = (dest - w - 1) & 3;
#pragma unroll
        for (int qd = 0; qd < 4; ++qd)
          *(LAS f32x4*)(red + w * WSTRIDE + (slot * 4 + qd) * 1024 + lane * 16) =
              f32x4{acc[a][b][4 * qd], acc[a][b][4 * qd + 1], acc[a][b][4 * qd + 2], acc[a][b][4 * qd + 3]};
      }
    }
  BAR_LDS();
  f32x16 r;
  if (w == 0) r = acc[0][0]; else if (w == 1) r = acc[1][0]; else if (w == 2) r = acc[0][1]; else r = acc[1][1];
#pragma unroll
  for (int s = 1; s < 4; ++s) {
    const int src = (w + s) & 3;
#pragma unroll
    for (int qd = 0; qd < 4; ++qd) {
      const f32x4 v = *(LAS f32x4*)(red + src * WSTRIDE + ((3 - s) * 4 + qd) * 1024 + lane * 16);
#pragma unroll
      for (int e = 0; e < 4; ++e) r[4 * qd + e] += v[e];
    }
  }
  return r;
}

DEV void phase2(const Params& p, LAS unsigned char* L, __amdgpu_buffer_rsrc_t rs, v4u rsv, int p64, int nt, int w, int lane, v4u (&hkeep)[2],
                int c3, const unsigned (&vo)[7], int dom, v4u (&csv4)[4], const void* wgu TSARG) {
  v4u of[2][8];
  const unsigned ob0 = WS_O + ((2 * p64) * 32 + 8 * w) * 1024, ob1 = ob0 + 32768;
  static_for<8>([&](auto I) {
    constexpr int i = I;
    bload_v<(i & 3) * 1024>(of[0][i], rsv, lane * 16, ob0 + (i >> 2) * 4096);
    bload_v<(i & 3) * 1024>(of[1][i], rsv, lane * 16, ob1 + (i >> 2) * 4096);
  });
  p3_issue_w(wgu, c3, 2, w, vo);
  SCHED();
  const int hh = lane >> 5, blk = (lane >> 4) & 1, q4 = (lane & 15) >> 2, pp = lane & 3;
  const int sw = ((q4 >> 1) & 1) << 2;
  LAS unsigned char* wb0 = L + P2_W + (128 * w + 8 * hh + q4) * 128 + (((2 * blk + (pp >> 1)) ^ sw) * 16) + 8 * (pp & 1);
  LAS unsigned char* wb1 = L + P2_W + (128 * w + 8 * hh + q4) * 128 + (((4 + 2 * blk + (pp >> 1)) ^ sw) * 16) + 8 * (pp & 1);
  f32x16 acc[2][2];
#pragma unroll
  for (int a = 0; a < 2; ++a)
#pragma unroll
    for (int b = 0; b < 2; ++b)
#pragma unroll
      for (int e = 0; e < 16; ++e) acc[a][b][e] = 0.f;
  v4u wa[8][2];
#pragma unroll
  for (int i = 0; i < 8; ++i) {
    wa[i][0] = tr_read2(wb0 + i * 2048, wb0 + i * 2048 + 512);
    wa[i][1] = tr_read2(wb1 + i * 2048, wb1 + i * 2048 + 512);
  }
  SCHED();
  static_for<8>([&](auto I) {
    constexpr int i = I;
    wait_vm_v2<7 + 16 - 2 * i - 2>(of[0][i], of[1][i]);
    acc[0][0] = mfma32(wa[i][0], of[0][i], acc[0][0]);
    acc[1][0] = mfma32(wa[i][1], of[0][i], acc[1][0]);
    acc[0][1] = mfma32(wa[i][0], of[1][i], acc[0][1]);
    acc[1][1] = mfma32(wa[i][1], of[1][i], acc[1][1]);
  });
  SCHED();
  STAMP(13);
  cs_load(p, c3, dom, w * 64 + lane, csv4);
  SCHED();
  f32x16 r = reduce4<16384>(acc, L + P2_W, w, lane);
  const int row = 32 * (w >> 1) + (lane & 31);
  float hv[16];
  float s1 = 0.f, s2 = 0.f;
#pragma unroll
  for (int qd = 0; qd < 4; ++qd) {
    const v2u xr = *(LAS v2u*)(L + P2_X + row * 128 + (((4 * (w & 1) + qd) ^ (row & 7)) * 16) + 8 * hh);
    const unsigned y01 = pk_bf16(r[4 * qd + 0], r[4 * qd + 1]), y23 = pk_bf16(r[4 * qd + 2], r[4 * qd + 3]);
    const unsigned h01 = pk_bf16(bf_lo(xr.x) + bf_lo(y01), bf_hi(xr.x) + bf_hi(y01));
    const unsigned h23 = pk_bf16(bf_lo(xr.y) + bf_lo(y23), bf_hi(xr.y) + bf_hi(y23));
    hkeep[qd >> 1][2 * (qd & 1)] = h01; hkeep[qd >> 1][2 * (qd & 1) + 1] = h23;
    hv[4 * qd + 0] = bf_lo(h01); hv[4 * qd + 1] = bf_hi(h01); hv[4 * qd + 2] = bf_lo(h23); hv[4 * qd + 3] = bf_hi(h23);
  }
  const int tile32 = 2 * p64 + (w >> 1);
  const int nt32 = 2 * nt + (w & 1);
#pragma unroll
  for (int ks = 0; ks < 2; ++ks)
    __builtin_amdgcn_raw_buffer_store_b128(hkeep[ks], rs, lane * 16, (int)WS_H + (tile32 * 32 + 2 * nt32 + ks) * 1024, AUX_SC1);
#pragma unroll
  for (int i = 0; i < 16; ++i) { s1 += hv[i]; s2 += hv[i] * hv[i]; }
  s1 = xsum32(s1); s2 = xsum32(s2);
  if (hh == 0) {
    v2u st = {__float_as_uint(s1), __float_as_uint(s2)};
    __builtin_amdgcn_raw_buffer_store_b64(st, rs, (lane & 31) * 16, (int)WS_HS + (tile32 * 8 + (nt32 >> 1)) * 512 + (nt32 & 1) * 8, AUX_SC1);
  }
}

constexpr int P3_KM = 20;
constexpr int p3_nl(int ks) { return ks < 12 ? 2 : 0; }
constexpr int p3_nd(int ks) { return (ks >= 4 && ks < 16) ? ((ks & 3) == 3 ? 1 : 2) : 0; }
constexpr int p3_nx(int ks) { return (ks == 7 || (ks >= 12 && ks < 16)) ? 1 : 0; }
constexpr int p3_cum(int ks) { int c = 12; for (int k = 0; k < ks; ++k) c += p3_nl(k) + p3_nd(k) + p3_nx(k); return c; }
constexpr int p3_pos(int i) { return i < 8 ? i : p3_cum((i - 8) / 2) + ((i - 8) & 1); }

DEV void phase3(const Params& p, LAS unsigned char* L, __amdgpu_buffer_rsrc_t rs, v4u rsv, GAS unsigned* ctl, unsigned tag, int p128, int c, int w, int lane,
                int nt4, const unsigned (&vo)[7], v4u (&wr)[8][2], const void* wgu TSARG) {
  const int tile32 = 4 * p128 + w;
  const int hh = lane >> 5, blk = (lane >> 4) & 1, q4 = (lane & 15) >> 2, pp = lane & 3;
  v4u hb[32];
  v4u hs[4];
  const unsigned hbase = WS_H + tile32 * 32768;
  static_for<8>([&](auto I) { bload_a<(I & 3) * 1024>(hb[I], rsv, lane * 16, hbase + (I >> 2) * 4096); });
  static_for<4>([&](auto I) { bload_v<I * 512>(hs[I], rsv, (4 * hh) * 512 + (lane & 31) * 16, WS_HS + tile32 * 4096); });
  SCHED();
  const int abase = blk * P3_UP + (4 * hh + q4) * 224 + pp * 8;
  int ab0 = abase, ab1 = abase + 65536, ab2 = abase + 123136;
  PIN_V(ab0); PIN_V(ab1); PIN_V(ab2);
  auto slot_ptr = [&](int ds) -> LAS unsigned char* {
    const int a = p3_slot_addr(ds);
    return a == 123136 ? L + ab2 : a >= 65536 ? L + ab1 + (a - 65536) : L + ab0 + a;
  };
  f32x16 acc[7];
#pragma unroll
  for (int t = 0; t < 7; ++t)
#pragma unroll
    for (int e = 0; e < 16; ++e) acc[t][e] = 0.f;
  WAIT_VM_BAR(12);
  if (w == 0 && lane == 0) __hip_atomic_store(flag_ptr(ctl, 3, p128, c), tag, __ATOMIC_RELAXED, __HIP_MEMORY_SCOPE_AGENT);
  STAMP(14);
  v4u an[7];
  {
    LAS unsigned char* sl = slot_ptr(0);
#pragma unroll
    for (int t = 0; t < 7; ++t) an[t] = tr_read2(sl + t * 32, sl + t * 32 + 8 * 224);
  }
  SCHED();
  unsigned csflag = 0;
  v4u csl[4];
  v4u fn[12];
  LAS float* cspart = (LAS float*)(L + P3_CSP);
  int cspo = P3_CSP + 896 * 4 + 16 * hh; PIN_V(cspo);
  LAS float* csp = cspart + 896;
  LAS float* csq_ = (LAS float*)(L + cspo);
  float mean = 0.f, rstd = 0.f;
  static_for<P3_KM>([&](auto KS) {
    constexpr int ks = KS;
    v4u ac[7];
#pragma unroll
    for (int t = 0; t < 7; ++t) ac[t] = an[t];
    if constexpr (ks == 19) { WAIT_VM_BAR(0); }
    else wait_vm_a<p3_cum(ks) - 1 - p3_pos(ks)>(hb[ks]);
    if constexpr (ks == 19) {
      PIN_A(hb[19]);
#pragma unroll
      for (int k = P3_KM; k < 32; ++k) PIN_A(hb[k]);
    }
    constexpr int ds = (ks + 1) >> 2, ksl = (ks + 1) & 3;
    LAS unsigned char* sl = slot_ptr(ds) + ksl * 16 * 224;
    auto vm = [&](auto SL) {
      constexpr int slot = SL, nl = p3_nl(ks), nd = p3_nd(ks), nx = p3_nx(ks);
      if constexpr (slot < nl) {
        constexpr int i = 8 + 2 * ks + slot;
        bload_a<(i & 3) * 1024>(hb[i], rsv, lane * 16, hbase + (i >> 2) * 4096);
      } else if constexpr (slot < nl + nd) {
        constexpr int i = 2 * (ks & 3) + (slot - nl);
        if constexpr (i == 0) p3_w_setup(5 + ((ks - 4) >> 2), w);
        p3_issue_w1<i>(wgu, c, 5 + ((ks - 4) >> 2), w, vo);
      } else if constexpr (slot < nl + nd + nx) {
        if constexpr (ks == 7) bload1_v(csflag, rsv, (unsigned)(FL_F + ((48 + (lane & 15)) * 16 + c) * 32) * 4u, 0u);
        else {
          const unsigned vof = (unsigned)(lane < 56 ? lane : 55) * 16u;
          bload_v<(ks - 12) * 896>(csl[ks - 12], rsv, vof, WS_CS + (unsigned)(c * 16 + 4 * w) * 896u);
        }
      }
    };
    if constexpr (ks == 11) {
      wait_vm_v1<p3_cum(11) - 1 - (p3_cum(7) + p3_nl(7) + p3_nd(7))>(csflag);
      if (!__all(csflag == tag)) wait_flags(flag_ptr(ctl, 3, lane & 15, c), tag);
    }
    if constexpr (ks == 13) {
      float s1 = 0.f, s2 = 0.f;
#pragma unroll
      for (int i = 0; i < 4; ++i) {
        PIN_V(hs[i]);
        s1 += __uint_as_float(hs[i].x) + __uint_as_float(hs[i].z);
        s2 += __uint_as_float(hs[i].y) + __uint_as_float(hs[i].w);
      }
      s1 = xsum32(s1); s2 = xsum32(s2);
      mean = s1 * (1.f / 512.f);
      rstd = __builtin_amdgcn_rsqf(fmaxf(s2 * (1.f / 512.f) - mean * mean, 0.f) + 1e-5f);
    }
    if constexpr (ks == 18) {
      asm volatile("s_waitcnt vmcnt(0)" : "+v"(csl[0]), "+v"(csl[1]), "+v"(csl[2]), "+v"(csl[3]) :: "memory");
      f32x4 a;
#pragma unroll
      for (int e = 0; e < 4; ++e)
        a[e] = (__uint_as_float(csl[0][e]) + __uint_as_float(csl[1][e])) + (__uint_as_float(csl[2][e]) + __uint_as_float(csl[3][e]));
      if (lane < 56) *(LAS f32x4*)(cspart + w * 224 + lane * 4) = a;
    }
    static_for<7>([&](auto T) {
      constexpr int t = T;
      if constexpr (t == 0 || t == 2) asm volatile("" : "+v"(ac[t]), "+v"(ac[t + 1]));
      if constexpr (t == 4) asm volatile("" : "+v"(ac[4]), "+v"(ac[5]), "+v"(ac[6]));
      acc[t] = mfma32(ac[t], hb[ks], acc[t]);
      SCHED();
      if constexpr (ks + 1 < P3_KM) {
        if constexpr (t == 1 || t == 3 || t == 5) {
          an[t - 1] = tr_read2(sl + (t - 1) * 32, sl + (t - 1) * 32 + 8 * 224);
          an[t] = tr_read2(sl + t * 32, sl + t * 32 + 8 * 224);
        }
        if constexpr (t == 6) an[6] = tr_read2(sl + 6 * 32, sl + 6 * 32 + 8 * 224);
      } else {
        static_for<2>([&](auto Q) {
          constexpr int k = 2 * t + Q;
          if constexpr (k < 12) {
            constexpr int k2 = P3_KM + k;
            LAS unsigned char* s2p = slot_ptr(k2 >> 2) + (k2 & 3) * 16 * 224;
            fn[k] = tr_read2(s2p, s2p + 8 * 224);
          }
        });
      }
      if constexpr (t == 0 || t == 2 || t == 4 || t == 6) vm(std::integral_constant<int, (t >> 1)>{});
      SCHED();
    });
    if constexpr ((ks & 3) == 3 && (ks >> 2) < 3) BAR_LDS();
  });
  STAMP(15);
  const f32x2 nm2 = {-mean, -mean};
  const float r2s = rstd * rstd;
  const f32x2 k1 = {-2.302208198f * rstd, -2.302208198f * rstd}, k2 = {-0.1029432397f * rstd * r2s, -0.1029432397f * rstd * r2s};
  const f32x2 rr2 = {r2s, r2s}, one2 = {1.f, 1.f};
  f32x2 yg[4], yu[4], zz[4], rc[4];
  f32x4 cgu[4];
  unsigned gw[4];
  auto epi_piece = [&](auto T, auto K) {
    constexpr int t = T, k = K;
    auto csg = [&](int i) { return pk2(cgu[i >> 1][2 * (i & 1)], cgu[i >> 1][2 * (i & 1) + 1]); };
    auto csu = [&](int i) { return pk2(cgu[2 + (i >> 1)][2 * (i & 1)], cgu[2 + (i >> 1)][2 * (i & 1) + 1]); };
    auto ex2 = [&](int i) { zz[i].x = __builtin_amdgcn_exp2f(zz[i].x); zz[i].y = __builtin_amdgcn_exp2f(zz[i].y); };
    auto rc2 = [&](int i) { rc[i].x = __builtin_amdgcn_rcpf(zz[i].x); rc[i].y = __builtin_amdgcn_rcpf(zz[i].y); };
    if constexpr (k == 0) {
      cgu[0] = *(LAS f32x4*)(csq_ + t * 16);
      cgu[1] = *(LAS f32x4*)(csq_ + t * 16 + 8);
      cgu[2] = *(LAS f32x4*)(csq_ + 112 + t * 16);
      cgu[3] = *(LAS f32x4*)(csq_ + 112 + t * 16 + 8);
    } else if constexpr (k == 1) {
      PIN_A(acc[t]);
#pragma unroll
      for (int i = 0; i < 4; ++i) { yg[i] = pk2(acc[t][2 * i], acc[t][2 * i + 1]); PIN_V(yg[i]); }
    } else if constexpr (k == 2) {
#pragma unroll
      for (int i = 0; i < 4; ++i) { yu[i] = pk2(acc[t][8 + 2 * i], acc[t][8 + 2 * i + 1]); PIN_V(yu[i]); }
    } else if constexpr (k == 3) {
#pragma unroll
      for (int i = 0; i < 4; ++i) yg[i] = nm2 * csg(i) + yg[i];
      yu[0] = nm2 * csu(0) + yu[0]; yu[1] = nm2 * csu(1) + yu[1];
    } else if constexpr (k == 4) {
#pragma unroll
      for (int i = 0; i < 4; ++i) zz[i] = yg[i] * yg[i];
      yu[2] = nm2 * csu(2) + yu[2]; yu[3] = nm2 * csu(3) + yu[3];
    } else if constexpr (k == 5) {
#pragma unroll
      for (int i = 0; i < 4; ++i) zz[i] = zz[i] * k2 + k1;
      yu[0] = yu[0] * yg[0]; yu[1] = yu[1] * yg[1];
    } else if constexpr (k == 6) {
#pragma unroll
      for (int i = 0; i < 4; ++i) zz[i] = zz[i] * yg[i];
      yu[2] = yu[2] * yg[2]; yu[3] = yu[3] * yg[3];
    } else if constexpr (k == 7) {
      ex2(0); ex2(1);
      yu[0] = yu[0] * rr2; yu[1] = yu[1] * rr2;
    } else if constexpr (k == 8) {
      ex2(2); ex2(3);
      zz[0] += one2; zz[1] += one2;
    } else if constexpr (k == 9) {
      rc2(0); rc2(1);
      zz[2] += one2; zz[3] += one2;
    } else if constexpr (k == 10) {
      rc2(2); rc2(3);
      yu[2] = yu[2] * rr2; yu[3] = yu[3] * rr2;
      yu[0] = yu[0] * rc[0]; yu[1] = yu[1] * rc[1];
    } else {
      yu[2] = yu[2] * rc[2]; yu[3] = yu[3] * rc[3];
#pragma unroll
      for (int i = 0; i < 4; ++i) gw[i] = cvt2(yu[i]);
      const v4u f = {gw[0], gw[1], gw[2], gw[3]};
      __builtin_amdgcn_raw_buffer_store_b128(f, rs, lane * 16, (int)WS_G + (tile32 * 112 + 7 * c + t) * 1024, AUX_SC1);
    }
  };
  unsigned wv4[4];
  wrow_voffs(lane, wv4);
  const u16* wdb = p.wd + (448 * w) * 512 + nt4 * 64;
  const int sw4 = ((q4 >> 1) & 1) << 2;
  LAS unsigned char* wb0 = L + w * P4_EARLY + (4 * hh + q4) * 128 + (((2 * blk + (pp >> 1)) ^ sw4) * 16) + 8 * (pp & 1);
  LAS unsigned char* wb1 = L + w * P4_EARLY + (4 * hh + q4) * 128 + (((4 + 2 * blk + (pp >> 1)) ^ sw4) * 16) + 8 * (pp & 1);
  SCHED();
  STAMP(18);
  static_for<7>([&](auto T) {
    constexpr int t = T;
    v4u fc[12];
#pragma unroll
    for (int k = 0; k < 12; ++k) fc[k] = fn[k];
    if constexpr (t == 4) WAIT_VM(3);
    static_for<12>([&](auto K) {
      constexpr int k = K;
      acc[t] = mfma32(fc[k], hb[P3_KM + k], acc[t]);
      SCHED();
      if constexpr (t < 6) {
        constexpr int ks = P3_KM + k, ds = ks >> 2, ksl = ks & 3;
        LAS unsigned char* sl = slot_ptr(ds) + ksl * 16 * 224 + (t + 1) * 32;
        fn[k] = tr_read2(sl, sl + 8 * 224);
      }
      if constexpr (t > 0) epi_piece(std::integral_constant<int, t - 1>{}, K);
      if constexpr (t == 0 && k == 0 ) {
        if (lane < 56) {
          const f32x4 a = (*(LAS f32x4*)(cspart + lane * 4) + *(LAS f32x4*)(cspart + 224 + lane * 4)) +
                          (*(LAS f32x4*)(cspart + 448 + lane * 4) + *(LAS f32x4*)(cspart + 672 + lane * 4));
          *(LAS f32x4*)(csp + lane * 4) = a;
        }
      }
      if constexpr (t < 2) {
        if constexpr (k >= 2 && k < 10) {
          constexpr int n = 8 * t + (k - 2), gq = n >> 2;
          if constexpr ((n & 3) == 0) set_m0((unsigned)(w * P4_EARLY + gq * 4096));
          wrow_issue<(n & 3)>(wdb + gq * 32 * 512, wv4);
        }
      }
      if constexpr (t == 4 && k < 8) {
        wr[k][0] = tr_read2(wb0 + k * 2048, wb0 + k * 2048 + 8 * 128);
        wr[k][1] = tr_read2(wb1 + k * 2048, wb1 + k * 2048 + 8 * 128);
      }
      if constexpr (t == 4 && k == 11) WAIT_LGKM();
      if constexpr (t >= 5) {
        if constexpr (k >= 2 && k < 10) {
          constexpr int n = 8 * (t - 5) + (k - 2), gq = n >> 2;
          if constexpr ((n & 3) == 0) set_m0((unsigned)(w * P4_EARLY + gq * 4096));
          wrow_issue<(n & 3)>(wdb + (4 + gq) * 32 * 512, wv4);
        }
      }
      SCHED();
    });
  });
  STAMP(19);
  asm volatile("s_nop 15\n\ts_nop 7" ::: "memory");
  SCHED();
  static_for<12>([&](auto K) { epi_piece(std::integral_constant<int, 6>{}, K); });
}

constexpr int P4_UP = 10;
DEV void phase4(const Params& p, LAS unsigned char* L, v4u rsv, int p64, int nt, int w, int lane,
                const v4u (&hkeep)[2], const v4u (&wr)[8][2] TSARG) {
  const int hh = lane >> 5, blk = (lane >> 4) & 1, q4 = (lane & 15) >> 2, pp = lane & 3;
  const int sw = ((q4 >> 1) & 1) << 2;
  const int wo0 = (4 * hh + q4) * 128 + (((2 * blk + (pp >> 1)) ^ sw) * 16) + 8 * (pp & 1);
  const int wo1 = (4 * hh + q4) * 128 + (((4 + 2 * blk + (pp >> 1)) ^ sw) * 16) + 8 * (pp & 1);
  int e0 = w * P4_EARLY + wo0, e1 = w * P4_EARLY + wo1, l0 = P4_LATE + w * P4_WAVE + wo0, l1 = P4_LATE + w * P4_WAVE + wo1;
  PIN_V(e0); PIN_V(e1); PIN_V(l0); PIN_V(l1);
  f32x16 acc[2][2];
#pragma unroll
  for (int a = 0; a < 2; ++a)
#pragma unroll
    for (int b = 0; b < 2; ++b)
#pragma unroll
      for (int e = 0; e < 16; ++e) acc[a][b][e] = 0.f;
  v4u gf[2][28];
  const unsigned gb0 = WS_G + ((2 * p64) * 112 + 28 * w) * 1024, gb1 = gb0 + 112 * 1024;
  auto ldg = [&](auto I) {
    constexpr int i = I;
    if constexpr (i < 24) {
      bload_a<(i & 3) * 1024>(gf[0][i], rsv, lane * 16, gb0 + (i >> 2) * 4096);
      bload_a<(i & 3) * 1024>(gf[1][i], rsv, lane * 16, gb1 + (i >> 2) * 4096);
    } else {
      bload_v<(i & 3) * 1024>(gf[0][i], rsv, lane * 16, gb0 + (i >> 2) * 4096);
      bload_v<(i & 3) * 1024>(gf[1][i], rsv, lane * 16, gb1 + (i >> 2) * 4096);
    }
  };
  static_for<P4_UP>(ldg);
  SCHED();
  v4u an0 = wr[0][0], an1 = wr[0][1];
  static_for<28>([&](auto I) {
    constexpr int i = I;
    constexpr int cum = (2 * (P4_UP + i) < 56) ? 2 * (P4_UP + i) : 56;
    constexpr int N = cum - 2 * i - 2;
    if constexpr (i < 24) wait_vm_a2<N>(gf[0][i], gf[1][i]); else wait_vm_v2<N>(gf[0][i], gf[1][i]);
    const v4u a0 = an0, a1 = an1;
    if constexpr (i + 1 < 8) { an0 = wr[i + 1][0]; an1 = wr[i + 1][1]; }
    else if constexpr (i + 1 < 28) {
      constexpr bool early = (i + 1) < 16;
      constexpr int sl = early ? (i + 1 - 8) : (i + 1 - 16);
      LAS unsigned char* q0 = L + (early ? e0 : l0) + sl * 2048;
      LAS unsigned char* q1 = L + (early ? e1 : l1) + sl * 2048;
      an0 = tr_read2(q0, q0 + 8 * 128);
      an1 = tr_read2(q1, q1 + 8 * 128);
    }
    acc[0][0] = mfma32(a0, gf[0][i], acc[0][0]);
    acc[1][0] = mfma32(a1, gf[0][i], acc[1][0]);
    SCHED();
    if constexpr (i + P4_UP < 28) ldg(std::integral_constant<int, i + P4_UP>{});
    SCHED();
    acc[0][1] = mfma32(a0, gf[1][i], acc[0][1]);
    acc[1][1] = mfma32(a1, gf[1][i], acc[1][1]);
    SCHED();
  });
  STAMP(16);
  f32x16 r = reduce4<P4_WAVE>(acc, L + P4_LATE, w, lane);
  const int tok = 64 * p64 + 32 * (w >> 1) + (lane & 31);
  v2u o[4];
#pragma unroll
  for (int qd = 0; qd < 4; ++qd) {
    const unsigned h01 = hkeep[qd >> 1][2 * (qd & 1)], h23 = hkeep[qd >> 1][2 * (qd & 1) + 1];
    const unsigned y01 = pk_bf16(r[4 * qd + 0], r[4 * qd + 1]), y23 = pk_bf16(r[4 * qd + 2], r[4 * qd + 3]);
    o[qd] = v2u{pk_bf16(bf_lo(h01) + bf_lo(y01), bf_hi(h01) + bf_hi(y01)), pk_bf16(bf_lo(h23) + bf_lo(y23), bf_hi(h23) + bf_hi(y23))};
  }
  const __amdgpu_buffer_rsrc_t ro = __builtin_amdgcn_make_buffer_rsrc((void*)p.out, 0, 2048 * 512 * 2, 0x00020000);
#pragma unroll
  for (int u = 0; u < 2; ++u) {
    auto sx = __builtin_amdgcn_permlane32_swap(o[2 * u].x, o[2 * u + 1].x, false, false);
    auto sy = __builtin_amdgcn_permlane32_swap(o[2 * u].y, o[2 * u + 1].y, false, false);
    const v4u o4 = {sx[0], sy[0], sx[1], sy[1]};
    __builtin_amdgcn_raw_buffer_store_b128(o4, ro, tok * 1024 + (64 * nt + 32 * (w & 1) + 16 * u + 8 * hh) * 2, 0, OUT_AUX);
  }
}

__global__ void __launch_bounds__(256, 1) mk_kernel(Params p) {
  extern __shared__ __attribute__((aligned(16))) unsigned char smem[];
  LAS unsigned char* L = (LAS unsigned char*)smem;
  const int tid = threadIdx.x, lane = tid & 63, w = __builtin_amdgcn_readfirstlane(tid >> 6);
  const int b = blockIdx.x;
  const int dom = (b & 7) * 2 + (b >> 7), j = (b >> 3) & 15;
  const int p64 = 2 * dom + (j >> 3), j8 = j & 7;
  const __amdgpu_buffer_rsrc_t rs = __builtin_amdgcn_make_buffer_rsrc((void*)p.ws, 0, (int)WS_BYTES, 0x00020000);
  const v4u rsv = {(unsigned)(size_t)p.ws, (unsigned)((size_t)p.ws >> 32), WS_BYTES, 0x00020000u};
  GAS unsigned* ctl = (GAS unsigned*)(p.ws + WS_FLAGS);
  STAMP(0);
  unsigned wvo[6], vx[5];
  {
    const int tok0 = 64 * p64 - 16, s0 = (p64 & 3) * 2;
    p1_wvoff(w, lane, wvo);
    p1_xvoff(tok0, w, lane, vx);
#pragma unroll
    for (int i = 0; i < 6; ++i) PIN_V(wvo[i]);
#pragma unroll
    for (int i = 0; i < 5; ++i) PIN_V(vx[i]);
    SCHED();
    p1_w_setup(0, w);
    static_for<6>([&](auto I) { p1_issue_w1<I>(p, j8, s0, wvo); });
    p1_x_setup(s0, w);
    static_for<5>([&](auto I) { p1_issue_x1<I>(p, s0, w, lane, vx); });
    p1_w_setup(1, w);
    static_for<6>([&](auto I) { p1_issue_w1<I>(p, j8, s0 + 1, wvo); });
    SCHED();
    if (w == 0) *(LAS v4u*)(L + P1_X + (lane >> 3) * 10240 + (lane & 7) * 16) = v4u{0x3f803f80u, 0x3f803f80u, 0x3f803f80u, 0x3f803f80u};
  }
  {
    unsigned long long pc; unsigned csz;
    asm volatile("s_getpc_b64 %0\n.Lmk_pc_here:\n\ts_mov_b32 %1, .Lmk_code_end-.Lmk_pc_here" : "=s"(pc), "=s"(csz));
    const unsigned off = (unsigned)((b >> 3) * 16 + (lane & 15)) * 128u + (unsigned)w * 65536u;
    if (lane < 16 && off < csz) {
      asm volatile("s_mov_b32 m0, %2\n\ts_nop 0\n\tglobal_load_lds_dword %0, %1" :: "v"(off), "s"(pc), "s"(160768u) : "memory");
    }
  }
  {
    const int l = (j8 * 4 + w) * 20 + (lane < 20 ? lane : 19);
    int tok = 64 * p64 - 16 + (l >> 3); tok = tok < 0 ? 0 : tok;
    asm volatile("s_mov_b32 m0, %2\n\ts_nop 0\n\tglobal_load_lds_dword %0, %1" :: "v"((unsigned)tok * 1024u + (unsigned)(l & 7) * 128u), "s"(p.x), "s"(160768u) : "memory");
  }
  const void* wgu = uni64((w & 1) ? p.wu : p.wg);
  const void* pfb = uni64(w == 0 ? p.wo : w == 1 ? p.wg : w == 2 ? p.wu : p.wd);
  unsigned wv4[4], vo[7];
  wrow_voffs(lane, wv4);
  p3_voffs(lane, vo);
#pragma unroll
  for (int i = 0; i < 7; ++i) PIN_V(vo[i]);
  const unsigned tag = __hip_atomic_load(ctl + FL_EPOCH, __ATOMIC_RELAXED, __HIP_MEMORY_SCOPE_AGENT) + 1u;
  STAMP(1);
  phase1(p, L, rs, p64, j8, w, lane, wvo, vx, wv4, pfb TSPASS);
  asm volatile("" ::: "memory"); SCHED();
  STAMP(2);
  WAIT_VM_BAR(0);
  if (tid == 0) __hip_atomic_store(flag_ptr(ctl, 0, dom, j), tag, __ATOMIC_RELAXED, __HIP_MEMORY_SCOPE_AGENT);
  STAMP(3);
  p2_issue_x(p, p64, j8, w, lane);
#pragma unroll
  for (int ds = 0; ds < 2; ++ds) p3_issue_w(wgu, j, ds, w, vo);
  wait_flags(flag_ptr(ctl, 0, dom, (j & 8) + (lane & 7)), tag);
  STAMP(4);
  v4u hkeep[2];
  v4u csv4[4];
  phase2(p, L, rs, rsv, p64, j8, w, lane, hkeep, j, vo, dom, csv4, wgu TSPASS);
  STAMP(5);
  WAIT_VM_BAR(0);
  if (tid == 0) __hip_atomic_store(flag_ptr(ctl, 1, dom, j), tag, __ATOMIC_RELAXED, __HIP_MEMORY_SCOPE_AGENT);
  STAMP(6);
#pragma unroll
  for (int ds = 3; ds < 5; ++ds) p3_issue_w(wgu, j, ds, w, vo);
  cs_finish(L, rs, j, dom, w, lane, csv4);
  wait_flags(flag_ptr(ctl, 1, dom, lane & 15), tag);
  STAMP(7);
  v4u wr[8][2];
  phase3(p, L, rs, rsv, ctl, tag, dom, j, w, lane, j8, vo, wr, wgu TSPASS);
  STAMP(8);
  WAIT_VM_BAR(0);
  if (tid == 0) __hip_atomic_store(flag_ptr(ctl, 2, dom, j), tag, __ATOMIC_RELAXED, __HIP_MEMORY_SCOPE_AGENT);
  STAMP(9);
  {
    unsigned wv4[4];
    wrow_voffs(lane, wv4);
    const u16* wdb = p.wd + (448 * w + 256) * 512 + j8 * 64;
    static_for<6>([&](auto G) {
      constexpr int gq = G;
      set_m0((unsigned)(P4_LATE + w * P4_WAVE + gq * 4096));
      static_for<4>([&](auto I) { wrow_issue<I>(wdb + gq * 32 * 512, wv4); });
    });
  }
  wait_flags(flag_ptr(ctl, 2, dom, lane & 15), tag);
  STAMP(10);
  if (b == 0 && tid == 0) __hip_atomic_store(ctl + FL_EPOCH, tag, __ATOMIC_RELAXED, __HIP_MEMORY_SCOPE_AGENT);
  phase4(p, L, rsv, p64, j8, w, lane, hkeep, wr TSPASS);
  STAMP(17);
  asm volatile(".Lmk_code_end:" ::: "memory");
}

extern "C" void kernel_launch(void* const* d_in, const int* in_sizes, int n_in, void* d_out, int out_size,
                              void* d_ws, size_t ws_size, hipStream_t stream) {
  Params p;
  p.x = (const u16*)d_in[0];
  p.wqkv = (const u16*)d_in[1];
  p.wo = (const u16*)d_in[2];
  p.wg = (const u16*)d_in[4];
  p.wu = (const u16*)d_in[5];
  p.wd = (const u16*)d_in[6];
  p.out = (u16*)d_out;
  p.ws = (unsigned char*)d_ws;
  (void)hipFuncSetAttribute((const void*)mk_kernel, hipFuncAttributeMaxDynamicSharedMemorySize, LDS_BYTES);
  mk_kernel<<<256, 256, LDS_BYTES, stream>>>(p);
}
```
